```python
import torch

def _softmax(x, axis=-1):
    return torch.softmax(x, dim=axis)
def _gelu(x):
    return torch.nn.functional.gelu(x, approximate="tanh")
def _silu(x):
    return torch.nn.functional.silu(x)
def _sigmoid(x):
    return torch.sigmoid(x)
def _layernorm(x, axis=-1, eps=1e-5):
    m = x.mean(dim=axis, keepdim=True)
    v = x.var(dim=axis, keepdim=True, unbiased=False)
    return (x - m) / torch.sqrt(v + eps)
def _scatter_add(tgt, idx, src, axis=0):
    return tgt.clone().index_add_(axis, idx, src)
def _segment_sum(src, seg, n):
    out = torch.zeros((n,) + src.shape[1:], dtype=src.dtype)
    return out.index_add_(0, seg, src)
def _pad(x, pw):
    flat = tuple(v for pair in reversed(pw) for v in pair)
    return torch.nn.functional.pad(x, flat)

kernel_name = 'concat_heads__sliding_local_dilated__ssm_conv_h3__prompt'

def setup_inputs():

    _w = (lambda a: (lambda t: t.to(torch.bfloat16) if t.is_floating_point() else t)(torch.as_tensor(a)))
    def _A_setup():
        B, S, D = 1, 2048, 576
        return {
            "x": _w((lambda *s: torch.randn(*s, dtype=torch.bfloat16) * 0.2)(B, S, D)),
            "wqkv": _w((lambda *s: torch.randn(*s, dtype=torch.bfloat16) * 0.2)(D, 3 * D)),
            "wo": _w((lambda *s: torch.randn(*s, dtype=torch.bfloat16) * 0.05)(D, D)),
        }
    _A_in = _A_setup()

    def _B_setup():
        B, S, D = 1, 2048, 576
        ks = 4
        return {
            "q": _w((lambda *s: torch.randn(*s, dtype=torch.bfloat16) * 0.05)(B, S, D)),
            "k": _w((lambda *s: torch.randn(*s, dtype=torch.bfloat16) * 0.8)(B, S, D)),
            "v": _w((lambda *s: torch.randn(*s, dtype=torch.bfloat16) * 0.8)(B, S, D)),
            "shift_k": _w((lambda *s: torch.randn(*s, dtype=torch.bfloat16) * 0.8)(D, ks) * 0.5),
            "ssm_k": _w((lambda *s: torch.randn(*s, dtype=torch.bfloat16) * 3.2)(D, S) * (1.0 / S ** 0.5)),
            "ssm_D": _w((lambda *s: torch.randn(*s, dtype=torch.bfloat16) * 0.8)(D)),
        }
    _B_in = _B_setup()

    return {
        "A_x": _A_in["x"],
        "A_wqkv": _A_in["wqkv"],
        "A_wo": _A_in["wo"],
        "B_q": _B_in["q"],
        "B_k": _B_in["k"],
        "B_v": _B_in["v"],
        "B_shift_k": _B_in["shift_k"],
        "B_ssm_k": _B_in["ssm_k"],
        "B_ssm_D": _B_in["ssm_D"],
    }

def reference(A_x, A_wqkv, A_wo, B_q, B_k, B_v, B_shift_k, B_ssm_k, B_ssm_D):
    def _A_ref(x, wqkv, wo):
        B, S, D = x.shape
        H = 32; Dh = D // H
        W, dil = 8, 2
        qkv = (lambda x, *d: x.permute(*d))((x @ wqkv).reshape(B, S, 3, H, Dh), 2, 0, 3, 1, 4)
        q, k, v = qkv[0], qkv[1], qkv[2]
        scale = Dh ** -0.5
        pos = torch.arange(S)
        off = torch.arange(W) * dil
        idx = pos[:, None] - off[None, :]
        mask = idx >= 0
        idx_c = torch.where(mask, idx, 0).reshape(-1)
        kg = k[:, :, idx_c, :].reshape(B, H, S, W, Dh)
        vg = v[:, :, idx_c, :].reshape(B, H, S, W, Dh)
        logits = torch.einsum("bhsd,bhswd->bhsw", q, kg) * scale
        logits = torch.where(mask[None, None, :, :], logits, -1e9)
        attn = _softmax(logits, axis=-1)
        o = torch.einsum("bhsw,bhswd->bhsd", attn, vg)
        return (lambda x, *d: x.permute(*d))(o, 0, 2, 1, 3).reshape(B, S, D) @ wo

    def _B_ref(q, k, v, shift_k, ssm_k, ssm_D):
        B, S, D = q.shape
        ks = shift_k.shape[-1]
        k_pad = _pad(k, ((0, 0), (ks - 1, 0), (0, 0)))
        k_shift = torch.zeros_like(k)
        for t in range(ks):
            k_shift = k_shift + k_pad[:, t:t + S, :] * shift_k[:, ks - 1 - t][None, None, :]
        kv = k_shift * v
        L = 2 * S
        kv_pad = _pad(kv, ((0, 0), (0, S), (0, 0)))
        kf_pad = _pad(ssm_k, ((0, 0), (0, S)))
        kv_f = (lambda x, axes=(-2,-1): torch.fft.rfft2(x.float(), dim=axes))(kv_pad[:, None, :, :], axes=(1, 2))
        kf_f = (lambda x, axes=(-2,-1): torch.fft.rfft2(x.float(), dim=axes))(kf_pad[:, None, :], axes=(1, 2))
        kf_f = (lambda x, *d: x.permute(*d))(kf_f, 1, 2, 0)
        y_f = kv_f * kf_f[None, :, :, :]
        y = (lambda x, s, axes=(-2,-1): torch.fft.irfft2(x, s=s, dim=axes).to(torch.bfloat16))(y_f, (1, L), axes=(1, 2))[:, 0, :S, :]
        y = y + kv * ssm_D[None, None, :]
        return q * y

    a = _A_ref(A_x, A_wqkv, A_wo)
    b = _B_ref(B_q, B_k, B_v, B_shift_k, B_ssm_k, B_ssm_D)
    return torch.concatenate([a, b], axis=-1)
```

```cpp
#include <hip/hip_runtime.h>
#include <hip/hip_bf16.h>
typedef unsigned short u16;
static constexpr int S=2048, D=576, H=32, DH=18;
__device__ __forceinline__ float b2f(u16 v){ return __uint_as_float(((unsigned)v)<<16); }
__device__ __forceinline__ u16 f2b(float f){ __hip_bfloat16 b=__float2bfloat16(f); return *reinterpret_cast<u16*>(&b); }
__device__ __forceinline__ float rb(float f){ return b2f(f2b(f)); }

__global__ void k_gemm(const u16* A,const u16* B,u16* C,int M,int N,int K,int ldc){
  int n=blockIdx.x*blockDim.x+threadIdx.x, m=blockIdx.y;
  if(n>=N) return;
  float acc=0;
  for(int k=0;k<K;k++) acc+=b2f(A[m*K+k])*b2f(B[k*N+n]);
  C[m*ldc+n]=f2b(acc);
}
__global__ void k_attn(const u16* qkv,u16* o){
  int idx=blockIdx.x*blockDim.x+threadIdx.x; int s=idx/H,h=idx%H;
  if(s>=S) return;
  const float scale=rb(0.23570226039551584f);
  float lg[8]; float mx=-1e30f;
  #pragma unroll
  for(int w=0;w<8;w++){ int p=s-2*w; float a=0;
    if(p>=0){ for(int d=0;d<DH;d++) a+=b2f(qkv[s*3*D+h*DH+d])*b2f(qkv[p*3*D+D+h*DH+d]); a=rb(rb(a)*0.23570226039551584f);} else a=-1e9f;
    lg[w]=a; mx=fmaxf(mx,a);}
  float sum=0;
  #pragma unroll
  for(int w=0;w<8;w++){ lg[w]=__expf(lg[w]-mx); sum+=lg[w]; }
  #pragma unroll
  for(int w=0;w<8;w++) lg[w]=rb(lg[w]/sum);
  for(int d=0;d<DH;d++){ float a=0;
    #pragma unroll
    for(int w=0;w<8;w++){ int p=s-2*w; if(p<0)p=0; a+=lg[w]*b2f(qkv[p*3*D+2*D+h*DH+d]); }
    o[s*D+h*DH+d]=f2b(a);}
}
__global__ void k_kv(const u16* k,const u16* v,const u16* sk,u16* kv){
  int idx=blockIdx.x*blockDim.x+threadIdx.x; int s=idx/D,d=idx%D; if(s>=S) return;
  float acc=0;
  for(int t=0;t<4;t++){ int p=s+t-3; float kk=p>=0?b2f(k[p*D+d]):0.f; acc=rb(acc+rb(kk*b2f(sk[d*4+3-t]))); }
  kv[idx]=f2b(acc*b2f(v[idx]));
}
__global__ void k_conv(const u16* kv,const u16* h,const u16* q,const u16* Dv,u16* out){
  int idx=blockIdx.x*blockDim.x+threadIdx.x; int s=idx/D,d=idx%D; if(s>=S) return;
  float acc=0;
  for(int j=0;j<=s;j++) acc+=b2f(h[d*S+j])*b2f(kv[(s-j)*D+d]);
  float y=rb(acc); y=rb(y+rb(b2f(kv[idx])*b2f(Dv[d])));
  out[s*2*D+D+d]=f2b(b2f(q[idx])*y);
}
extern "C" void kernel_launch(void* const* d_in, const int* in_sizes, int n_in, void* d_out, int out_size, void* d_ws, size_t ws_size, hipStream_t stream){
  const u16* x=(const u16*)d_in[0]; const u16* wqkv=(const u16*)d_in[1]; const u16* wo=(const u16*)d_in[2];
  const u16* q=(const u16*)d_in[3]; const u16* k=(const u16*)d_in[4]; const u16* v=(const u16*)d_in[5];
  const u16* sk=(const u16*)d_in[6]; const u16* h=(const u16*)d_in[7]; const u16* Dv=(const u16*)d_in[8];
  u16* out=(u16*)d_out; u16* qkv=(u16*)d_ws; u16* o=qkv+S*3*D; u16* kv=o+S*D;
  k_gemm<<<dim3((3*D+63)/64,S),64,0,stream>>>(x,wqkv,qkv,S,3*D,D,3*D);
  k_attn<<<S*H/256,256,0,stream>>>(qkv,o);
  k_gemm<<<dim3(D/64,S),64,0,stream>>>(o,wo,out,S,D,D,2*D);
  k_kv<<<S*D/256,256,0,stream>>>(k,v,sk,kv);
  k_conv<<<S*D/256,256,0,stream>>>(kv,h,q,Dv,out);
}
```

```cpp
#define PROBE 0
#include <hip/hip_runtime.h>
#include <hip/hip_bf16.h>
typedef unsigned short u16;
static constexpr int S=2048, D=576, H=32, DH=18;
__device__ __forceinline__ float b2f(u16 v){ return __uint_as_float(((unsigned)v)<<16); }
__device__ __forceinline__ u16 f2b(float f){ __hip_bfloat16 b=__float2bfloat16(f); return *reinterpret_cast<u16*>(&b); }
__device__ __forceinline__ float rb(float f){ return b2f(f2b(f)); }

__global__ void k_gemm(const u16* A,const u16* B,u16* C,int M,int N,int K,int ldc){
  int n=blockIdx.x*blockDim.x+threadIdx.x, m=blockIdx.y;
  if(n>=N) return;
  float acc=0;
  for(int k=0;k<K;k++) acc+=b2f(A[m*K+k])*b2f(B[k*N+n]);
  C[m*ldc+n]=f2b(acc);
}
__global__ void k_attn(const u16* qkv,u16* o){
  int idx=blockIdx.x*blockDim.x+threadIdx.x; int s=idx/H,h=idx%H;
  if(s>=S) return;
  const float scale=rb(0.23570226039551584f);
  float lg[8]; float mx=-1e30f;
  #pragma unroll
  for(int w=0;w<8;w++){ int p=s-2*w; float a=0;
    if(p>=0){ for(int d=0;d<DH;d++) a+=b2f(qkv[s*3*D+h*DH+d])*b2f(qkv[p*3*D+D+h*DH+d]); a=rb(rb(a)*0.23570226039551584f);} else a=-1e9f;
    lg[w]=a; mx=fmaxf(mx,a);}
  float sum=0;
  #pragma unroll
  for(int w=0;w<8;w++){ lg[w]=__expf(lg[w]-mx); sum+=lg[w]; }
  #pragma unroll
  for(int w=0;w<8;w++) lg[w]=rb(lg[w]/sum);
  for(int d=0;d<DH;d++){ float a=0;
    #pragma unroll
    for(int w=0;w<8;w++){ int p=s-2*w; if(p<0)p=0; a+=lg[w]*b2f(qkv[p*3*D+2*D+h*DH+d]); }
    o[s*D+h*DH+d]=f2b(a);}
}
__global__ void k_kv(const u16* k,const u16* v,const u16* sk,u16* kv){
  int idx=blockIdx.x*blockDim.x+threadIdx.x; int s=idx/D,d=idx%D; if(s>=S) return;
  float acc=0;
  for(int t=0;t<4;t++){ int p=s+t-3; float kk=p>=0?b2f(k[p*D+d]):0.f; acc=rb(acc+rb(kk*b2f(sk[d*4+3-t]))); }
  kv[idx]=f2b(acc*b2f(v[idx]));
}
__global__ void k_conv(const u16* kv,const u16* h,const u16* q,const u16* Dv,u16* out){
  int idx=blockIdx.x*blockDim.x+threadIdx.x; int s=idx/D,d=idx%D; if(s>=S) return;
  float acc=0;
  for(int j=0;j<=s;j++) acc+=b2f(h[d*S+j])*b2f(kv[(s-j)*D+d]);
  float y=rb(acc); y=rb(y+rb(b2f(kv[idx])*b2f(Dv[d])));
  out[s*2*D+D+d]=f2b(b2f(q[idx])*y);
}

typedef unsigned long long u64;
typedef unsigned v4u __attribute__((ext_vector_type(4)));
#define LAS __attribute__((address_space(3)))
#define R4(x) x x x x
#define R16(x) R4(R4(x))
#define R256(x) R16(R16(x))
#define R1024(x) R4(R256(x))
__global__ __launch_bounds__(256) void k_probe(const char* p0,const char* p1,const char* p2,const char* p3,const char* p4,const char* p5,const char* p6,u64* st){
  extern __shared__ __attribute__((aligned(16))) char sm[];
  u64 t0=__builtin_amdgcn_s_memrealtime();
  int bid=blockIdx.x, tid=threadIdx.x, wave=__builtin_amdgcn_readfirstlane(tid>>6);
  u64 t1;
  t1=t0;
  if(tid==0){ st[bid*2]=t0; st[bid*2+1]=t1; }
}
__global__ void k_spin(const u64* st){
  u64 mn=~0ull,mx=0,sum=0;
  for(int b=0;b<256;b++){ u64 a=st[b*2],c=st[b*2+1]; mn=a<mn?a:mn; mx=c>mx?c:mx; sum+=c-a; }
  u64 d = (PROBE==2||PROBE==4||PROBE==3)? sum*1000/256 : (mx-mn)*1000;
  if(d>3000000) d=3000000;
  u64 s=__builtin_amdgcn_s_memrealtime();
  while(__builtin_amdgcn_s_memrealtime()-s<d) __builtin_amdgcn_s_sleep(8);
}
extern "C" void kernel_launch(void* const* d_in, const int* in_sizes, int n_in, void* d_out, int out_size, void* d_ws, size_t ws_size, hipStream_t stream){
  const u16* x=(const u16*)d_in[0]; const u16* wqkv=(const u16*)d_in[1]; const u16* wo=(const u16*)d_in[2];
  const u16* q=(const u16*)d_in[3]; const u16* k=(const u16*)d_in[4]; const u16* v=(const u16*)d_in[5];
  const u16* sk=(const u16*)d_in[6]; const u16* h=(const u16*)d_in[7]; const u16* Dv=(const u16*)d_in[8];
  u16* out=(u16*)d_out; u16* qkv=(u16*)d_ws; u16* o=qkv+S*3*D; u16* kv=o+S*D;
  u64* st=(u64*)((char*)d_ws+(64<<20)); hipFuncSetAttribute((void*)k_probe,hipFuncAttributeMaxDynamicSharedMemorySize,131072); k_probe<<<256,256,131072,stream>>>((const char*)x,(const char*)wqkv,(const char*)wo,(const char*)q,(const char*)k,(const char*)v,(const char*)h,st);
  k_gemm<<<dim3((3*D+63)/64,S),64,0,stream>>>(x,wqkv,qkv,S,3*D,D,3*D);
  k_attn<<<S*H/256,256,0,stream>>>(qkv,o);
  k_gemm<<<dim3(D/64,S),64,0,stream>>>(o,wo,out,S,D,D,2*D);
  k_kv<<<S*D/256,256,0,stream>>>(k,v,sk,kv);
  k_conv<<<S*D/256,256,0,stream>>>(kv,h,q,Dv,out);
  k_spin<<<1,1,0,stream>>>(st);
}
```

```cpp
#include <hip/hip_runtime.h>

typedef unsigned short u16;
typedef unsigned long long u64;
typedef short s4 __attribute__((ext_vector_type(4)));
typedef short s8 __attribute__((ext_vector_type(8)));
typedef __bf16 bf2 __attribute__((ext_vector_type(2)));
typedef __bf16 bf8 __attribute__((ext_vector_type(8)));
typedef float f2 __attribute__((ext_vector_type(2)));
typedef float f4 __attribute__((ext_vector_type(4)));
typedef unsigned u4 __attribute__((ext_vector_type(4)));
typedef unsigned u2 __attribute__((ext_vector_type(2)));
typedef __amdgpu_buffer_rsrc_t rsrc_t;
#define LAS __attribute__((address_space(3)))
#define GAS __attribute__((address_space(1)))
#define RLX_AGENT __ATOMIC_RELAXED, __HIP_MEMORY_SCOPE_AGENT
#define DEV __device__ __forceinline__

constexpr int NB_ACT = 2048 * 576 * 2;
constexpr int NB_W = 576 * 1728 * 2;
constexpr int NB_WO = 576 * 576 * 2;
constexpr unsigned OOB = 0x80000000u;
constexpr int SC1 = 16;

constexpr int WS_FLAG = 4096;
constexpr int F_U = 0, F_O = 144, F_Y = 256;
constexpr int WS_U = 1 << 20;
constexpr int WS_Y = WS_U + (3 << 20);
constexpr int WS_O = WS_Y + (3 << 20);
constexpr int WS_DBG = 16 << 20;

constexpr int LDS_BYTES = 163840;
constexpr int SLOT = 49152;
constexpr int L_WO = 0;
constexpr int L_QKV = 92160;
constexpr int PS = 161 * 16;
constexpr int L_RED = 83968;
constexpr int L_H = 83968;
constexpr int H_CP = 4160, H_CH = 8448;
constexpr int L_T = L_H + 4 * H_CH;
constexpr int US = 286 * 16;
constexpr int L_YT = L_T + 4 * US + 128;

#define STAMP(i) do {} while (0)
#define CSTAMP(i) do {} while (0)

DEV rsrc_t mkrs(const void* p, int bytes) { return __builtin_amdgcn_make_buffer_rsrc((void*)p, 0, bytes, 0x00020000); }
DEV u4 mkdesc(const void* p, int bytes) {
  u64 a = (u64)p;
  return u4{(unsigned)a, (unsigned)(a >> 32) & 0xffffu, (unsigned)bytes, 0x00020000u};
}
DEV void dma16(u4 r, char* lds, unsigned voff, unsigned soff) {
  asm volatile("s_mov_b32 m0, %0\n\ts_nop 0\n\tbuffer_load_dwordx4 %1, %2, %3 offen lds"
               :: "s"((unsigned)(size_t)(LAS char*)lds), "v"(voff), "s"(r), "s"(soff) : "memory");
}
DEV s4 trread(const char* p) { return __builtin_amdgcn_ds_read_tr16_b64_v4i16((LAS s4*)p); }
DEV bf8 cat8(s4 a, s4 b) { return __builtin_bit_cast(bf8, __builtin_shufflevector(a, b, 0, 1, 2, 3, 4, 5, 6, 7)); }
DEV f4 mfma(bf8 a, bf8 b, f4 c) { return __builtin_amdgcn_mfma_f32_16x16x32_bf16(a, b, c, 0, 0, 0); }
DEV unsigned pk(float lo, float hi) { f2 v = {lo, hi}; return __builtin_bit_cast(unsigned, __builtin_convertvector(v, bf2)); }
DEV float lo16(unsigned x) { return __builtin_bit_cast(float, x << 16); }
DEV float hi16(unsigned x) { return __builtin_bit_cast(float, x & 0xffff0000u); }
DEV float dot2(unsigned a, unsigned b, float c) {
  return __builtin_amdgcn_fdot2_f32_bf16(__builtin_bit_cast(bf2, a), __builtin_bit_cast(bf2, b), c, false);
}
DEV unsigned alignb(unsigned hi, unsigned lo) { return __builtin_amdgcn_alignbit(hi, lo, 16); }
#define WAITV(n) asm volatile("s_waitcnt vmcnt(" #n ")" ::: "memory")
#define WAITL0() asm volatile("s_waitcnt lgkmcnt(0)" ::: "memory")
DEV void bar() { WAITL0(); __builtin_amdgcn_s_barrier(); asm volatile("" ::: "memory"); }
DEV int unperm(int p) { int u = (p & 15) >> 1; return (p & ~15) + ((u >> 2) << 3) + ((p & 1) << 2) + (u & 3); }

DEV u64 mktag(u64 e) { asm volatile("" : "+v"(e)); return e * 6364136223846793005ull + 1442695040888963407ull; }
DEV void post_flag(char* ws, int idx, u64 e) { u64 tag = mktag(e); __hip_atomic_store((GAS u64*)(ws + WS_FLAG + idx * 128), tag, RLX_AGENT); }
DEV void wait_flags(char* ws, int idx, u64 e) {
  const u64 tag = mktag(e);
#pragma unroll 1
  for (int spin = 0; spin < (1 << 20); ++spin) {
    u64 v = tag;
    if (idx >= 0) v = __hip_atomic_load((GAS u64*)(ws + WS_FLAG + idx * 128), RLX_AGENT);
    if (__all(v == tag)) break;
    __builtin_amdgcn_s_sleep(1);
  }
  __builtin_amdgcn_fence(__ATOMIC_ACQUIRE, "wavefront");
}

DEV void wo_dma(u4 rwo, char* sm, int ct, int i0, int i1, int w, int nw, int lane) {
#pragma unroll 1
  for (int i = i0 + w; i < i1; i += nw) {
    int c = i * 64 + lane, p = c / 9, ch = c - p * 9;
    dma16(rwo, sm + L_WO + i * 1024, unperm(p) * 1152 + ct * 144 + ch * 16, 0);
  }
}

template <int NN>
DEV void g1_loop(char* sm, f4 (&acc)[5][4], u4 rx, u4 rw, u4 rwo, const unsigned (&vo)[6], int w, int lane,
                 int m0, int n0, int rot, int ct, char* ws) {
  const int r = lane & 15, g = lane >> 4;
  const int xa0 = (m0 * 16 + r) * 128 + ((g ^ ((r >> 1) & 7)) << 4);
  const int xa1 = xa0 ^ 64;
  const int wa = 20480 + (((g >> 1) << 4) + ((g & 1) << 3) + ((r >> 2) << 1)) * 432 + (lane & 3) * 8 + n0 * 32;
  bf8 xf[2][5], wf[2][NN];
  auto issue = [&](int st) {
    int kt = st + rot; if (kt >= 9) kt -= 9;
    char* d = sm + (st % 3) * SLOT + w * 1024;
    unsigned sx = kt * 128, sw = kt * (64 * 3456);
    dma16(rx, d, vo[0], sx);
    dma16(rx, d + 8192, vo[1], sx);
    dma16(w < 4 ? rx : rw, d + 16384, vo[2], w < 4 ? sx : sw);
    dma16(rw, d + 24576, vo[3], sw);
    dma16(rw, d + 32768, vo[4], sw);
    dma16(rw, d + 40960, vo[5], sw);
  };
  auto rd = [&](const char* s, int ks, bf8 (&x)[5], bf8 (&wv)[NN]) {
#pragma unroll
    for (int n = 0; n < NN; ++n)
      wv[n] = cat8(trread(s + wa + ks * 32 * 432 + n * 32), trread(s + wa + ks * 32 * 432 + 432 + n * 32));
#pragma unroll
    for (int m = 0; m < 5; ++m) x[m] = *(const LAS bf8*)(s + (ks ? xa1 : xa0) + m * 2048);
  };
  auto mm = [&](bf8 (&x)[5], bf8 (&wv)[NN]) {
#pragma unroll
    for (int m = 0; m < 5; ++m)
#pragma unroll
      for (int n = 0; n < NN; ++n) acc[m][n] = mfma(wv[n], x[m], acc[m][n]);
  };
  STAMP(16);
  issue(0); issue(1); issue(2);
  WAITV(12); bar();
  STAMP(17);
  rd(sm, 0, xf[0], wf[0]);
  rd(sm, 1, xf[1], wf[1]);
  mm(xf[0], wf[0]);
  int sl = 1;
#pragma unroll 1
  for (int it = 1; it < 9; ++it) {
    WAITV(6); bar();
    STAMP(17 + it);
    if (it < 7) issue(it + 2);
    else if (it == 7) wo_dma(rwo, sm, ct, 0, 48, w, 8, lane);
    else wo_dma(rwo, sm, ct, 48, 81, w, 8, lane);
    const char* s = sm + sl * SLOT;
    rd(s, 0, xf[0], wf[0]);
    mm(xf[1], wf[1]);
    rd(s, 1, xf[1], wf[1]);
    mm(xf[0], wf[0]);
    sl = sl == 2 ? 0 : sl + 1;
  }
  mm(xf[1], wf[1]);
}

DEV float asf(unsigned u) { return __builtin_bit_cast(float, u); }
DEV unsigned asu(float f) { return __builtin_bit_cast(unsigned, f); }
DEV float xmax(float x) {
  auto a = __builtin_amdgcn_permlane16_swap(asu(x), asu(x), false, false);
  x = fmaxf(asf(a[0]), asf(a[1]));
  auto b = __builtin_amdgcn_permlane32_swap(asu(x), asu(x), false, false);
  return fmaxf(asf(b[0]), asf(b[1]));
}
DEV float xsum(float x) {
  auto a = __builtin_amdgcn_permlane16_swap(asu(x), asu(x), false, false);
  x = asf(a[0]) + asf(a[1]);
  auto b = __builtin_amdgcn_permlane32_swap(asu(x), asu(x), false, false);
  return asf(b[0]) + asf(b[1]);
}

DEV void role_a(char* sm, char* ws, const u16* x, const u16* wqkv, u4 rwo, u64 tag, int xcd, int j, int w, int lane) {
  const int tid = threadIdx.x;
  const int rt = (xcd >> 2) * 7 + (j >> 1), hq = (xcd & 3) * 2 + (j & 1);
  const int row0 = rt * 146;
  const u4 rx = mkdesc(x, NB_ACT), rw = mkdesc(wqkv, NB_W);
  unsigned vo[6];
#pragma unroll
  for (int i = 0; i < 6; ++i) {
    int s = w + 8 * i;
    int xr = s * 8 + (lane >> 3);
    unsigned ox = (row0 + xr) * 1152 + (((lane & 7) ^ ((xr >> 1) & 7)) << 4);
    int c = (s - 20) * 64 + lane, p = c / 27, cc = c - p * 27, part = cc / 9, ch = cc - part * 9;
    unsigned ow = unperm(p) * 3456 + part * 1152 + hq * 144 + ch * 16;
    vo[i] = s < 20 ? ox : (s < 47 ? ow : OOB);
  }
  f4 acc[5][4];
#pragma unroll
  for (int m = 0; m < 5; ++m)
#pragma unroll
    for (int n = 0; n < 4; ++n) acc[m][n] = f4{0.f, 0.f, 0.f, 0.f};
  const int wm = w >> 2, nr = wm ? 3 - (w & 3) : (w & 3);
  const int n0 = nr < 2 ? nr * 4 : 2 + nr * 3, m0 = wm * 5;
  const int rot = 0;
  if (nr < 2) g1_loop<4>(sm, acc, rx, rw, rwo, vo, w, lane, m0, n0, rot, j & 7, ws);
  else g1_loop<3>(sm, acc, rx, rw, rwo, vo, w, lane, m0, n0, rot, j & 7, ws);
  STAMP(1);
  bar();
  const int r = lane & 15, g = lane >> 4;
  char* qkv = sm + L_QKV;
  {
    char* d = qkv + (2 * n0 + (g >> 1)) * PS + (m0 * 16 + r) * 16 + (g & 1) * 8;
#pragma unroll
    for (int n = 0; n < 4; ++n) {
      if (n < 3 || nr < 2) {
        if (2 * (n0 + n) + (g >> 1) < 27) {
#pragma unroll
          for (int m = 0; m < 5; ++m)
            *(LAS u2*)(d + n * 2 * PS + m * 256) = u2{pk(acc[m][n][0], acc[m][n][1]), pk(acc[m][n][2], acc[m][n][3])};
        }
      }
    }
    if (tid < 27) *(LAS u4*)(qkv + tid * PS + 160 * 16) = u4{0, 0, 0, 0};
  }
  bar();
  STAMP(2);
  {
    const int h = w & 3, par = w >> 2, odd = h & 1;
    const char* qa = qkv + (2 * h + g) * PS + (par + 2 * r) * 16;
    const char* ka = qa + 9 * PS - 256;
    const int d0 = 18 * h - 2 * odd, q4 = r >> 2, p4 = lane & 3;
    const int vc0 = d0 + 4 * p4, vc1 = d0 + 16;
    const int kr0 = par - 16 + 2 * (4 * g + q4), kr1 = kr0 + (g >= 2 ? 16 : 32);
    const char* v0 = qkv + (18 + (vc0 >> 3)) * PS + ((vc0 >> 2) & 1) * 8;
    const char* v1 = qkv + (18 + (vc1 >> 3)) * PS + ((vc1 >> 2) & 1) * 8;
    u4 qm;
    bool c0[4], c1[4];
#pragma unroll
    for (int d = 0; d < 4; ++d) {
      int e = 8 * g + 2 * d - 2 * h;
      qm[d] = (e >= 0 && e < 18) ? 0xffffffffu : 0u;
      int kk = 4 * g + d - r;
      c0[d] = kk >= 1 && kk <= 8;
      c1[d] = kk + 16 <= 8;
    }
    const unsigned n01 = (c1[0] ? 0xffffu : 0u) | (c1[1] ? 0xffff0000u : 0u);
    const unsigned n23 = (c1[2] ? 0xffffu : 0u) | (c1[3] ? 0xffff0000u : 0u);
    const bool sp = g == 0 && odd;
    const int ca = sp ? d0 + 16 : d0 + 4 * g, cb = odd ? d0 + 2 : d0 + 16;
    char* oa = qkv + (ca >> 3) * PS + (par + 2 * r) * 16 + (ca & 7) * 2;
    char* ob = qkv + (cb >> 3) * PS + (par + 2 * r) * 16 + (cb & 7) * 2;
    const float cs = 0.23570226039551584f * 1.4426950408889634f;
    const float ninf = -__builtin_inff();
#pragma unroll
    for (int bq = 0; bq < 5; ++bq) {
      u4 qf = *(const LAS u4*)(qa + bq * 512) & qm;
      bf8 k0 = *(const LAS bf8*)(ka + bq * 512), k1 = *(const LAS bf8*)(ka + bq * 512 + 512);
      bf8 vt0 = cat8(trread(v0 + kr0 * 16 + bq * 512), trread(v0 + kr1 * 16 + bq * 512));
      bf8 vt1 = cat8(trread(v1 + kr0 * 16 + bq * 512), trread(v1 + kr1 * 16 + bq * 512));
      const f4 z = {0.f, 0.f, 0.f, 0.f};
      f4 s0 = mfma(k0, __builtin_bit_cast(bf8, qf), z), s1 = mfma(k1, __builtin_bit_cast(bf8, qf), z);
      float val[4];
#pragma unroll
      for (int d = 0; d < 4; ++d) {
        bool a0 = bq == 0 ? (c0[d] && g >= 2) : c0[d];
        val[d] = c1[d] ? s1[d] : (a0 ? s0[d] : ninf);
      }
      float mx = xmax(fmaxf(fmaxf(val[0], val[1]), fmaxf(val[2], val[3])));
      float mc = -mx * cs;
#pragma unroll
      for (int d = 0; d < 4; ++d) val[d] = __builtin_amdgcn_exp2f(__builtin_fmaf(val[d], cs, mc));
      float inv = __builtin_amdgcn_rcpf(xsum((val[0] + val[1]) + (val[2] + val[3])));
      unsigned p01 = pk(val[0] * inv, val[1] * inv), p23 = pk(val[2] * inv, val[3] * inv);
      u4 pf = {p01 & ~n01, p23 & ~n23, p01 & n01, p23 & n23};
      f4 o0 = mfma(vt0, __builtin_bit_cast(bf8, pf), z), o1 = mfma(vt1, __builtin_bit_cast(bf8, pf), z);
      unsigned a0 = pk(o0[0], o0[1]), a1 = pk(o0[2], o0[3]), b0 = pk(o1[0], o1[1]), b1 = pk(o1[2], o1[3]);
      *(LAS u2*)(oa + bq * 512) = sp ? u2{b0, b1} : u2{a0, a1};
      if (g == 0) *(LAS unsigned*)(ob + bq * 512) = odd ? a1 : b0;
    }
  }
  bar();
  STAMP(3);
  rsrc_t ro = mkrs(ws + WS_O, NB_ACT);
#pragma unroll
  for (int i = 0; i < 3; ++i) {
    int it = tid + 512 * i, c = it / 160, row = it - c * 160, gr = row0 + row;
    if (it < 1440) {
      u4 v = *(const LAS u4*)(qkv + c * PS + row * 16);
      unsigned off = ((gr >> 6) * 72 + hq * 9 + c) * 1024 + (gr & 63) * 16;
      if ((row < 14 && rt > 0) || gr >= 2048) off = OOB;
      __builtin_amdgcn_raw_buffer_store_b128(v, ro, off, 0, SC1);
    }
  }
  WAITV(0);
  bar();
  if (tid == 448) post_flag(ws, F_O + rt * 8 + hq, tag);
  STAMP(4);
}

DEV void role_b(char* sm, char* ws, const u16* q, const u16* k, const u16* v, const u16* shk, const u16* hk,
                const u16* dv, u16* out, u4 rwo, u64 tag, int xcd, int j, int w, int lane) {
  const int tid = threadIdx.x;
  const int jb = j - 14;
  const int grp = jb < 16 ? xcd : 8, idx = jb < 16 ? jb : xcd * 2 + (jb - 16);
  char* kt = sm + L_T;
  char* vt = kt + 136 * 128;
  {
    const u4 rk = mkdesc(k, NB_ACT), rv = mkdesc(v, NB_ACT);
    const int rr = w * 8 + (lane >> 3);
    const int f = ((rr >> 1) & 1) | (((rr >> 3) & 3) << 1);
    const unsigned off = (idx * 128 + rr) * 1152 + grp * 128 + (((lane & 7) ^ f) << 4);
    dma16(rk, kt + w * 1024, off - 8 * 1152, 0);
    dma16(rk, kt + w * 1024 + 8192, off + 56 * 1152, 0);
    if (w == 0) dma16(rk, kt + 16384, off + 120 * 1152, 0);
    dma16(rv, vt + w * 1024, off, 0);
    dma16(rv, vt + w * 1024 + 8192, off + 64 * 1152, 0);
  }
  const int G = lane >> 4, i16 = lane & 15;
  const u2 wk = *(const GAS u2*)((const char*)shk + (grp * 64 + w * 8 + (G & 1) * 4 + (i16 & 3)) * 8);
  const int ch0 = grp * 64 + idx * 4;
  rsrc_t rh = mkrs(hk, NB_ACT);
  u4 ha[2]; unsigned hp[2];
#pragma unroll
  for (int i = 0; i < 2; ++i) {
    int it = tid + 512 * i;
    unsigned off = ch0 * 4096 + it * 16;
    ha[i] = __builtin_amdgcn_raw_buffer_load_b128(rh, off, 0, 0);
    hp[i] = __builtin_amdgcn_raw_buffer_load_b32(rh, (it & 255) ? off - 4 : OOB, 0, 0);
  }
  const unsigned dval = dv[ch0 + (w & 3)];
  WAITV(0);
  bar();
  STAMP(1);
  rsrc_t ru = mkrs(ws + WS_U, NB_ACT);
  {
    const unsigned wA = alignb(wk[1], wk[1]), wB = alignb(wk[0], wk[0]);
    const int q4 = i16 >> 2, p4 = lane & 3;
#pragma unroll
    for (int s = 0; s < 2; ++s) {
      const int Y = 2 * s + (G >> 1);
      unsigned K[6], V[4];
#pragma unroll
      for (int T = 0; T < 3; ++T) {
        int rr = 32 * Y + 8 * p4 + 4 + 4 * T + q4;
        int f = ((rr >> 1) & 1) | (((rr >> 3) & 3) << 1);
        u2 t = __builtin_bit_cast(u2, trread(kt + rr * 128 + ((w ^ f) << 4) + (G & 1) * 8));
        K[2 * T] = t[0]; K[2 * T + 1] = t[1];
      }
#pragma unroll
      for (int T = 0; T < 2; ++T) {
        int rr = 32 * Y + 8 * p4 + 4 * T + q4;
        int f = ((rr >> 1) & 1) | (((rr >> 3) & 3) << 1);
        u2 t = __builtin_bit_cast(u2, trread(vt + rr * 128 + ((w ^ f) << 4) + (G & 1) * 8));
        V[2 * T] = t[0]; V[2 * T + 1] = t[1];
      }
      unsigned Sh[5];
#pragma unroll
      for (int d = 0; d < 5; ++d) Sh[d] = alignb(K[d + 1], K[d]);
      u4 o;
#pragma unroll
      for (int d = 0; d < 4; ++d) {
        float ev = dot2(Sh[d + 1], wB, dot2(Sh[d], wA, 0.f)) * lo16(V[d]);
        float od = dot2(K[d + 2], wB, dot2(K[d + 1], wA, 0.f)) * hi16(V[d]);
        o[3 - d] = pk(od, ev);
      }
      unsigned off = ((grp * 16 + 2 * w + (G & 1)) * 256 + idx * 16 + 4 * Y) * 64 + i16 * 16;
      __builtin_amdgcn_raw_buffer_store_b128(o, ru, off, 0, SC1);
    }
  }
#pragma unroll
  for (int i = 0; i < 2; ++i) {
    int it = tid + 512 * i;
    char* d = sm + L_H + (it >> 8) * H_CH + 32 + (it & 255) * 16;
    *(LAS u4*)(d + H_CP) = ha[i];
    *(LAS u4*)d = u4{alignb(ha[i][0], hp[i]), alignb(ha[i][1], ha[i][0]), alignb(ha[i][2], ha[i][1]), alignb(ha[i][3], ha[i][2])};
  }
  if (tid < 16) *(LAS u4*)(sm + L_H + (tid >> 2) * H_CH + ((tid >> 1) & 1) * H_CP + (tid & 1) * 16) = u4{0, 0, 0, 0};
  WAITV(0);
  bar();
  if (tid == 448) post_flag(ws, F_U + grp * 16 + idx, tag);
  STAMP(2);
  char* ui = sm + L_T;
  if (tid >= 128 && tid < 248) { int t = tid - 128, c = t / 30; *(LAS u4*)(ui + c * US + (t - c * 30) * 16) = u4{0, 0, 0, 0}; }
  if (w == 0) wait_flags(ws, lane < 16 ? F_U + grp * 16 + lane : -1, tag);
  bar();
  STAMP(3);
  {
    u4 uv[2];
#pragma unroll
    for (int i = 0; i < 2; ++i) uv[i] = __builtin_amdgcn_raw_buffer_load_b128(ru, (grp * 16 + idx) * 16384 + (tid + 512 * i) * 16, 0, SC1);
#pragma unroll
    for (int i = 0; i < 2; ++i) { int it = tid + 512 * i; *(LAS u4*)(ui + (it & 3) * US + (30 + (it >> 2)) * 16) = uv[i]; }
  }
  u4 qv[2];
  {
    rsrc_t rq = mkrs(q, NB_ACT);
#pragma unroll
    for (int i = 0; i < 2; ++i) { int it = tid + 512 * i; qv[i] = __builtin_amdgcn_raw_buffer_load_b128(rq, (idx * 128 + (it >> 3)) * 1152 + grp * 128 + (it & 7) * 16, 0, 0); }
  }
  bar();
  STAMP(4);
  rsrc_t ry = mkrs(ws + WS_Y, NB_ACT);
  if (w >= 4) {
    wo_dma(rwo, sm, j & 7, 0, 81, w - 4, 4, lane);
  } else {
    const int r = lane & 15, g = lane >> 4;
    const char* aa = sm + L_H + w * H_CH + (r & 1) * H_CP + 4 + 4 * (4 * g + (r >> 1));
    const char* ba = ui + w * US + 16 * (31 + 2 * r - g);
    f4 acc[8];
    bf8 A[2][8], B[2][8];
    auto rd = [&](int dd, bf8 (&a)[8], bf8 (&b)[8]) {
#pragma unroll
      for (int i = 0; i < 8; ++i) {
        b[i] = *(const LAS bf8*)(ba - dd * 64 + i * 512);
        const LAS unsigned* p = (const LAS unsigned*)(aa + dd * 64 + i * 512);
        a[i] = __builtin_bit_cast(bf8, u4{p[0], p[1], p[2], p[3]});
      }
    };
    auto mm = [&](bf8 (&a)[8], bf8 (&b)[8]) {
#pragma unroll
      for (int i = 0; i < 8; ++i)
#pragma unroll
        for (int jj = 0; jj + i < 8; ++jj) acc[i + jj] = mfma(a[i], b[jj], acc[i + jj]);
    };
    rd(0, A[0], B[0]);
    {
      const int x = 15 - r - 8 * g;
      u4 ad;
#pragma unroll
      for (int d = 0; d < 4; ++d) ad[d] = x == 2 * d ? dval : (x == 2 * d + 1 ? dval << 16 : 0u);
#pragma unroll
      for (int n = 0; n < 8; ++n) acc[n] = mfma(__builtin_bit_cast(bf8, ad), B[0][n], f4{0.f, 0.f, 0.f, 0.f});
    }
#pragma unroll 1
    for (int dd = 0; dd < 8; dd += 2) {
      rd(dd + 1, A[1], B[1]);
      mm(A[0], B[0]);
      rd(dd + 2 < 8 ? dd + 2 : 0, A[0], B[0]);
      mm(A[1], B[1]);
    }
#pragma unroll
    for (int a = 0; a < 4; ++a) {
      unsigned x0 = pk(acc[2 * a][0], acc[2 * a][1]), x1 = pk(acc[2 * a][2], acc[2 * a][3]);
      unsigned y0 = pk(acc[2 * a + 1][0], acc[2 * a + 1][1]), y1 = pk(acc[2 * a + 1][2], acc[2 * a + 1][3]);
      auto s0 = __builtin_amdgcn_permlane16_swap(x0, y0, false, false);
      auto s1 = __builtin_amdgcn_permlane16_swap(x1, y1, false, false);
      int n = 2 * a + (g & 1);
      unsigned off = ((grp * 16 + 2 * n + (r >> 3)) * 64 + idx * 4 + w) * 256 + ((g >> 1) * 8 + (r & 7)) * 16;
      __builtin_amdgcn_raw_buffer_store_b128(u4{s0[0], s1[0], s0[1], s1[1]}, ry, off, 0, SC1);
    }
  }
  STAMP(5);
  WAITV(0);
  bar();
  if (tid == 448) post_flag(ws, F_Y + grp * 16 + idx, tag);
  STAMP(6);
  if (w == 0) wait_flags(ws, lane < 16 ? F_Y + grp * 16 + lane : -1, tag);
  bar();
  STAMP(7);
  char* yt = sm + L_YT;
  {
    u4 yv[2];
#pragma unroll
    for (int i = 0; i < 2; ++i) yv[i] = __builtin_amdgcn_raw_buffer_load_b128(ry, (grp * 16 + idx) * 16384 + (tid + 512 * i) * 16, 0, SC1);
#pragma unroll
    for (int i = 0; i < 2; ++i) {
      int it = tid + 512 * i, c = it >> 4, pos = it & 15;
      *(LAS u4*)(yt + (c >> 3) * 2064 + (c & 7) * 256 + (2 * (pos & 7) + (pos >> 3)) * 16) = yv[i];
    }
  }
  bar();
#pragma unroll
  for (int i = 0; i < 2; ++i) {
    int it = tid + 512 * i, row = it >> 3, c = it & 7;
    const LAS u16* p = (const LAS u16*)(yt + c * 2064 + row * 2);
    u4 o;
#pragma unroll
    for (int d = 0; d < 4; ++d)
      o[d] = pk(lo16(p[d * 256]) * lo16(qv[i][d]), lo16(p[d * 256 + 128]) * hi16(qv[i][d]));
    *(GAS u4*)((char*)out + (idx * 128 + row) * 2304 + 1152 + grp * 128 + c * 16) = o;
  }
  STAMP(8);
}

__global__ __launch_bounds__(512, 2) void fused(const u16* x, const u16* wqkv, const u16* wo, const u16* q, const u16* k,
                                                const u16* v, const u16* shk, const u16* hk, const u16* dv, u16* out,
                                                char* ws) {
  extern __shared__ __attribute__((aligned(16))) char sm[];
  const int tid = threadIdx.x, lane = tid & 63, w = __builtin_amdgcn_readfirstlane(tid >> 6);
  const int bid = blockIdx.x, xcd = bid & 7, j = bid >> 3;
  const u64 tag = __hip_atomic_load((GAS u64*)ws, RLX_AGENT);
  const u4 rwo = mkdesc(wo, NB_WO);
  if (j < 14) role_a(sm, ws, x, wqkv, rwo, tag, xcd, j, w, lane);
  else role_b(sm, ws, q, k, v, shk, hk, dv, out, rwo, tag, xcd, j, w, lane);

  const int rt = xcd * 4 + (j >> 3), ct = j & 7;
  if (w == 0) {
    if (bid == 0) {
#pragma unroll 1
      for (int i = 0; i < 4; ++i) wait_flags(ws, i * 64 + lane, tag);
      if (lane == 0) __hip_atomic_store((GAS u64*)ws, mktag(tag), RLX_AGENT);
    } else {
      int ra = rt * 64 + (lane < 8 ? 0 : 63);
      int ti = ra < 14 ? 0 : (ra - 14) / 146;
      wait_flags(ws, lane < 16 ? F_O + ti * 8 + (lane & 7) : -1, tag);
    }
  }
  WAITV(0);
  bar();
  STAMP(9);
  const int r = lane & 15, g = lane >> 4, m = w & 3, kh = w >> 2;
  rsrc_t ro = mkrs(ws + WS_O, NB_ACT);
  u4 of[9];
#pragma unroll
  for (int i = 0; i < 9; ++i)
    of[i] = __builtin_amdgcn_raw_buffer_load_b128(ro, (rt * 72 + kh * 36 + g) * 1024 + (m * 16 + r) * 16, i * 4096, SC1);
  __builtin_amdgcn_sched_barrier(0);
  const char* wa = sm + L_WO + (kh * 288 + ((g >> 1) << 4) + ((g & 1) << 3) + ((r >> 2) << 1)) * 144 + (lane & 3) * 8;
  f4 acc[5];
#pragma unroll
  for (int n = 0; n < 5; ++n) acc[n] = f4{0.f, 0.f, 0.f, 0.f};
#pragma unroll
  for (int i = 0; i < 9; ++i)
#pragma unroll
    for (int n = 0; n < 5; ++n)
      acc[n] = mfma(cat8(trread(wa + i * 32 * 144 + n * 32), trread(wa + i * 32 * 144 + 144 + n * 32)),
                    __builtin_bit_cast(bf8, of[i]), acc[n]);
  STAMP(10);
  char* red = sm + L_RED;
  if (kh) {
#pragma unroll
    for (int n = 0; n < 5; ++n) *(LAS f4*)(red + ((m * 5 + n) * 64 + lane) * 16) = acc[n];
  }
  bar();
  char* ot = red + 20480;
  if (!kh) {
#pragma unroll
    for (int n = 0; n < 5; ++n) {
      f4 t = acc[n] + *(const LAS f4*)(red + ((m * 5 + n) * 64 + lane) * 16);
      if (n < 4 || g < 2) *(LAS u2*)(ot + (m * 16 + r) * 144 + n * 32 + g * 8) = u2{pk(t[0], t[1]), pk(t[2], t[3])};
    }
  }
  bar();
#pragma unroll
  for (int i = 0; i < 2; ++i) {
    int it = tid + 512 * i, row = it / 9, c = it - row * 9;
    if (it < 576) *(GAS u4*)((char*)out + (rt * 64 + row) * 2304 + ct * 144 + c * 16) = *(const LAS u4*)(ot + it * 16);
  }
  STAMP(11); CSTAMP(1);
}

extern "C" void kernel_launch(void* const* d_in, const int* in_sizes, int n_in, void* d_out, int out_size, void* d_ws,
                              size_t ws_size, hipStream_t stream) {
  (void)hipFuncSetAttribute((const void*)fused, hipFuncAttributeMaxDynamicSharedMemorySize, LDS_BYTES);
  fused<<<256, 512, LDS_BYTES, stream>>>((const u16*)d_in[0], (const u16*)d_in[1], (const u16*)d_in[2], (const u16*)d_in[3],
                                         (const u16*)d_in[4], (const u16*)d_in[5], (const u16*)d_in[6], (const u16*)d_in[7],
                                         (const u16*)d_in[8], (u16*)d_out, (char*)d_ws);
}
```

```cpp
#define STAMPS
#include <hip/hip_runtime.h>

typedef unsigned short u16;
typedef unsigned long long u64;
typedef short s4 __attribute__((ext_vector_type(4)));
typedef short s8 __attribute__((ext_vector_type(8)));
typedef __bf16 bf2 __attribute__((ext_vector_type(2)));
typedef __bf16 bf8 __attribute__((ext_vector_type(8)));
typedef float f2 __attribute__((ext_vector_type(2)));
typedef float f4 __attribute__((ext_vector_type(4)));
typedef unsigned u4 __attribute__((ext_vector_type(4)));
typedef unsigned u2 __attribute__((ext_vector_type(2)));
typedef __amdgpu_buffer_rsrc_t rsrc_t;
#define LAS __attribute__((address_space(3)))
#define GAS __attribute__((address_space(1)))
#define RLX_AGENT __ATOMIC_RELAXED, __HIP_MEMORY_SCOPE_AGENT
#define DEV __device__ __forceinline__

constexpr int NB_ACT = 2048 * 576 * 2;
constexpr int NB_W = 576 * 1728 * 2;
constexpr int NB_WO = 576 * 576 * 2;
constexpr unsigned OOB = 0x80000000u;
constexpr int SC1 = 16;

constexpr int WS_FLAG = 4096;
constexpr int F_U = 0, F_O = 144, F_Y = 256;
constexpr int WS_U = 1 << 20;
constexpr int WS_Y = WS_U + (3 << 20);
constexpr int WS_O = WS_Y + (3 << 20);
constexpr int WS_DBG = 16 << 20;

constexpr int LDS_BYTES = 163840;
constexpr int SLOT = 49152;
constexpr int L_WO = 0;
constexpr int L_QKV = 92160;
constexpr int PS = 161 * 16;
constexpr int L_RED = 83968;
constexpr int L_H = 83968;
constexpr int H_CP = 4160, H_CH = 8448;
constexpr int L_T = L_H + 4 * H_CH;
constexpr int US = 286 * 16;
constexpr int L_YT = L_T + 4 * US + 128;

#define STAMP(i) do { if (threadIdx.x == 0) ((GAS u64*)(ws + WS_DBG))[blockIdx.x * 32 + (i)] = __builtin_amdgcn_s_memtime(); } while (0)
#define CSTAMP(i) do { if (threadIdx.x == 0) ((GAS u64*)(ws + WS_DBG + 65536))[blockIdx.x * 2 + (i)] = __builtin_amdgcn_s_memrealtime(); } while (0)

DEV rsrc_t mkrs(const void* p, int bytes) { return __builtin_amdgcn_make_buffer_rsrc((void*)p, 0, bytes, 0x00020000); }
DEV u4 mkdesc(const void* p, int bytes) {
  u64 a = (u64)p;
  return u4{(unsigned)a, (unsigned)(a >> 32) & 0xffffu, (unsigned)bytes, 0x00020000u};
}
DEV void dma16(u4 r, char* lds, unsigned voff, unsigned soff) {
  asm volatile("s_mov_b32 m0, %0\n\ts_nop 0\n\tbuffer_load_dwordx4 %1, %2, %3 offen lds"
               :: "s"((unsigned)(size_t)(LAS char*)lds), "v"(voff), "s"(r), "s"(soff) : "memory");
}
DEV s4 trread(const char* p) { return __builtin_amdgcn_ds_read_tr16_b64_v4i16((LAS s4*)p); }
DEV bf8 cat8(s4 a, s4 b) { return __builtin_bit_cast(bf8, __builtin_shufflevector(a, b, 0, 1, 2, 3, 4, 5, 6, 7)); }
DEV f4 mfma(bf8 a, bf8 b, f4 c) { return __builtin_amdgcn_mfma_f32_16x16x32_bf16(a, b, c, 0, 0, 0); }
DEV unsigned pk(float lo, float hi) { f2 v = {lo, hi}; return __builtin_bit_cast(unsigned, __builtin_convertvector(v, bf2)); }
DEV float lo16(unsigned x) { return __builtin_bit_cast(float, x << 16); }
DEV float hi16(unsigned x) { return __builtin_bit_cast(float, x & 0xffff0000u); }
DEV float dot2(unsigned a, unsigned b, float c) {
  return __builtin_amdgcn_fdot2_f32_bf16(__builtin_bit_cast(bf2, a), __builtin_bit_cast(bf2, b), c, false);
}
DEV unsigned alignb(unsigned hi, unsigned lo) { return __builtin_amdgcn_alignbit(hi, lo, 16); }
#define WAITV(n) asm volatile("s_waitcnt vmcnt(" #n ")" ::: "memory")
#define WAITL0() asm volatile("s_waitcnt lgkmcnt(0)" ::: "memory")
DEV void bar() { WAITL0(); __builtin_amdgcn_s_barrier(); asm volatile("" ::: "memory"); }
DEV int unperm(int p) { int u = (p & 15) >> 1; return (p & ~15) + ((u >> 2) << 3) + ((p & 1) << 2) + (u & 3); }

DEV u64 mktag(u64 e) { asm volatile("" : "+v"(e)); return e * 6364136223846793005ull + 1442695040888963407ull; }
DEV void post_flag(char* ws, int idx, u64 e) { u64 tag = mktag(e); __hip_atomic_store((GAS u64*)(ws + WS_FLAG + idx * 128), tag, RLX_AGENT); }
DEV void wait_flags(char* ws, int idx, u64 e) {
  const u64 tag = mktag(e);
#pragma unroll 1
  for (int spin = 0; spin < (1 << 20); ++spin) {
    u64 v = tag;
    if (idx >= 0) v = __hip_atomic_load((GAS u64*)(ws + WS_FLAG + idx * 128), RLX_AGENT);
    if (__all(v == tag)) break;
    __builtin_amdgcn_s_sleep(1);
  }
  __builtin_amdgcn_fence(__ATOMIC_ACQUIRE, "wavefront");
}

DEV void wo_dma(u4 rwo, char* sm, int ct, int i0, int i1, int w, int nw, int lane) {
#pragma unroll 1
  for (int i = i0 + w; i < i1; i += nw) {
    int c = i * 64 + lane, p = c / 9, ch = c - p * 9;
    dma16(rwo, sm + L_WO + i * 1024, unperm(p) * 1152 + ct * 144 + ch * 16, 0);
  }
}

template <int NN>
DEV void g1_loop(char* sm, f4 (&acc)[5][4], u4 rx, u4 rw, u4 rwo, const unsigned (&vo)[6], int w, int lane,
                 int m0, int n0, int rot, int ct, char* ws) {
  const int r = lane & 15, g = lane >> 4;
  const int xa0 = (m0 * 16 + r) * 128 + ((g ^ ((r >> 1) & 7)) << 4);
  const int xa1 = xa0 ^ 64;
  const int wa = 20480 + (((g >> 1) << 4) + ((g & 1) << 3) + ((r >> 2) << 1)) * 432 + (lane & 3) * 8 + n0 * 32;
  bf8 xf[2][5], wf[2][NN];
  auto issue = [&](int st) {
    int kt = st + rot; if (kt >= 9) kt -= 9;
    char* d = sm + (st % 3) * SLOT + w * 1024;
    unsigned sx = kt * 128, sw = kt * (64 * 3456);
    dma16(rx, d, vo[0], sx);
    dma16(rx, d + 8192, vo[1], sx);
    dma16(w < 4 ? rx : rw, d + 16384, vo[2], w < 4 ? sx : sw);
    dma16(rw, d + 24576, vo[3], sw);
    dma16(rw, d + 32768, vo[4], sw);
    dma16(rw, d + 40960, vo[5], sw);
  };
  auto rd = [&](const char* s, int ks, bf8 (&x)[5], bf8 (&wv)[NN]) {
#pragma unroll
    for (int n = 0; n < NN; ++n)
      wv[n] = cat8(trread(s + wa + ks * 32 * 432 + n * 32), trread(s + wa + ks * 32 * 432 + 432 + n * 32));
#pragma unroll
    for (int m = 0; m < 5; ++m) x[m] = *(const LAS bf8*)(s + (ks ? xa1 : xa0) + m * 2048);
  };
  auto mm = [&](bf8 (&x)[5], bf8 (&wv)[NN]) {
#pragma unroll
    for (int m = 0; m < 5; ++m)
#pragma unroll
      for (int n = 0; n < NN; ++n) acc[m][n] = mfma(wv[n], x[m], acc[m][n]);
  };
  STAMP(16);
  issue(0); issue(1); issue(2);
  WAITV(12); bar();
  STAMP(17);
  rd(sm, 0, xf[0], wf[0]);
  rd(sm, 1, xf[1], wf[1]);
  mm(xf[0], wf[0]);
  int sl = 1;
#pragma unroll 1
  for (int it = 1; it < 9; ++it) {
    WAITV(6); bar();
    STAMP(17 + it);
    if (it < 7) issue(it + 2);
    else if (it == 7) wo_dma(rwo, sm, ct, 0, 48, w, 8, lane);
    else wo_dma(rwo, sm, ct, 48, 81, w, 8, lane);
    const char* s = sm + sl * SLOT;
    rd(s, 0, xf[0], wf[0]);
    mm(xf[1], wf[1]);
    rd(s, 1, xf[1], wf[1]);
    mm(xf[0], wf[0]);
    sl = sl == 2 ? 0 : sl + 1;
  }
  mm(xf[1], wf[1]);
}

DEV float asf(unsigned u) { return __builtin_bit_cast(float, u); }
DEV unsigned asu(float f) { return __builtin_bit_cast(unsigned, f); }
DEV float xmax(float x) {
  auto a = __builtin_amdgcn_permlane16_swap(asu(x), asu(x), false, false);
  x = fmaxf(asf(a[0]), asf(a[1]));
  auto b = __builtin_amdgcn_permlane32_swap(asu(x), asu(x), false, false);
  return fmaxf(asf(b[0]), asf(b[1]));
}
DEV float xsum(float x) {
  auto a = __builtin_amdgcn_permlane16_swap(asu(x), asu(x), false, false);
  x = asf(a[0]) + asf(a[1]);
  auto b = __builtin_amdgcn_permlane32_swap(asu(x), asu(x), false, false);
  return asf(b[0]) + asf(b[1]);
}

DEV void role_a(char* sm, char* ws, const u16* x, const u16* wqkv, u4 rwo, u64 tag, int xcd, int j, int w, int lane) {
  const int tid = threadIdx.x;
  const int rt = (xcd >> 2) * 7 + (j >> 1), hq = (xcd & 3) * 2 + (j & 1);
  const int row0 = rt * 146;
  const u4 rx = mkdesc(x, NB_ACT), rw = mkdesc(wqkv, NB_W);
  unsigned vo[6];
#pragma unroll
  for (int i = 0; i < 6; ++i) {
    int s = w + 8 * i;
    int xr = s * 8 + (lane >> 3);
    unsigned ox = (row0 + xr) * 1152 + (((lane & 7) ^ ((xr >> 1) & 7)) << 4);
    int c = (s - 20) * 64 + lane, p = c / 27, cc = c - p * 27, part = cc / 9, ch = cc - part * 9;
    unsigned ow = unperm(p) * 3456 + part * 1152 + hq * 144 + ch * 16;
    vo[i] = s < 20 ? ox : (s < 47 ? ow : OOB);
  }
  f4 acc[5][4];
#pragma unroll
  for (int m = 0; m < 5; ++m)
#pragma unroll
    for (int n = 0; n < 4; ++n) acc[m][n] = f4{0.f, 0.f, 0.f, 0.f};
  const int wm = w >> 2, nr = wm ? 3 - (w & 3) : (w & 3);
  const int n0 = nr < 2 ? nr * 4 : 2 + nr * 3, m0 = wm * 5;
  const int rot = 0;
  if (nr < 2) g1_loop<4>(sm, acc, rx, rw, rwo, vo, w, lane, m0, n0, rot, j & 7, ws);
  else g1_loop<3>(sm, acc, rx, rw, rwo, vo, w, lane, m0, n0, rot, j & 7, ws);
  STAMP(1);
  bar();
  const int r = lane & 15, g = lane >> 4;
  char* qkv = sm + L_QKV;
  {
    char* d = qkv + (2 * n0 + (g >> 1)) * PS + (m0 * 16 + r) * 16 + (g & 1) * 8;
#pragma unroll
    for (int n = 0; n < 4; ++n) {
      if (n < 3 || nr < 2) {
        if (2 * (n0 + n) + (g >> 1) < 27) {
#pragma unroll
          for (int m = 0; m < 5; ++m)
            *(LAS u2*)(d + n * 2 * PS + m * 256) = u2{pk(acc[m][n][0], acc[m][n][1]), pk(acc[m][n][2], acc[m][n][3])};
        }
      }
    }
    if (tid < 27) *(LAS u4*)(qkv + tid * PS + 160 * 16) = u4{0, 0, 0, 0};
  }
  bar();
  STAMP(2);
  {
    const int h = w & 3, par = w >> 2, odd = h & 1;
    const char* qa = qkv + (2 * h + g) * PS + (par + 2 * r) * 16;
    const char* ka = qa + 9 * PS - 256;
    const int d0 = 18 * h - 2 * odd, q4 = r >> 2, p4 = lane & 3;
    const int vc0 = d0 + 4 * p4, vc1 = d0 + 16;
    const int kr0 = par - 16 + 2 * (4 * g + q4), kr1 = kr0 + (g >= 2 ? 16 : 32);
    const char* v0 = qkv + (18 + (vc0 >> 3)) * PS + ((vc0 >> 2) & 1) * 8;
    const char* v1 = qkv + (18 + (vc1 >> 3)) * PS + ((vc1 >> 2) & 1) * 8;
    u4 qm;
    bool c0[4], c1[4];
#pragma unroll
    for (int d = 0; d < 4; ++d) {
      int e = 8 * g + 2 * d - 2 * h;
      qm[d] = (e >= 0 && e < 18) ? 0xffffffffu : 0u;
      int kk = 4 * g + d - r;
      c0[d] = kk >= 1 && kk <= 8;
      c1[d] = kk + 16 <= 8;
    }
    const unsigned n01 = (c1[0] ? 0xffffu : 0u) | (c1[1] ? 0xffff0000u : 0u);
    const unsigned n23 = (c1[2] ? 0xffffu : 0u) | (c1[3] ? 0xffff0000u : 0u);
    const bool sp = g == 0 && odd;
    const int ca = sp ? d0 + 16 : d0 + 4 * g, cb = odd ? d0 + 2 : d0 + 16;
    char* oa = qkv + (ca >> 3) * PS + (par + 2 * r) * 16 + (ca & 7) * 2;
    char* ob = qkv + (cb >> 3) * PS + (par + 2 * r) * 16 + (cb & 7) * 2;
    const float cs = 0.23570226039551584f * 1.4426950408889634f;
    const float ninf = -__builtin_inff();
#pragma unroll
    for (int bq = 0; bq < 5; ++bq) {
      u4 qf = *(const LAS u4*)(qa + bq * 512) & qm;
      bf8 k0 = *(const LAS bf8*)(ka + bq * 512), k1 = *(const LAS bf8*)(ka + bq * 512 + 512);
      bf8 vt0 = cat8(trread(v0 + kr0 * 16 + bq * 512), trread(v0 + kr1 * 16 + bq * 512));
      bf8 vt1 = cat8(trread(v1 + kr0 * 16 + bq * 512), trread(v1 + kr1 * 16 + bq * 512));
      const f4 z = {0.f, 0.f, 0.f, 0.f};
      f4 s0 = mfma(k0, __builtin_bit_cast(bf8, qf), z), s1 = mfma(k1, __builtin_bit_cast(bf8, qf), z);
      float val[4];
#pragma unroll
      for (int d = 0; d < 4; ++d) {
        bool a0 = bq == 0 ? (c0[d] && g >= 2) : c0[d];
        val[d] = c1[d] ? s1[d] : (a0 ? s0[d] : ninf);
      }
      float mx = xmax(fmaxf(fmaxf(val[0], val[1]), fmaxf(val[2], val[3])));
      float mc = -mx * cs;
#pragma unroll
      for (int d = 0; d < 4; ++d) val[d] = __builtin_amdgcn_exp2f(__builtin_fmaf(val[d], cs, mc));
      float inv = __builtin_amdgcn_rcpf(xsum((val[0] + val[1]) + (val[2] + val[3])));
      unsigned p01 = pk(val[0] * inv, val[1] * inv), p23 = pk(val[2] * inv, val[3] * inv);
      u4 pf = {p01 & ~n01, p23 & ~n23, p01 & n01, p23 & n23};
      f4 o0 = mfma(vt0, __builtin_bit_cast(bf8, pf), z), o1 = mfma(vt1, __builtin_bit_cast(bf8, pf), z);
      unsigned a0 = pk(o0[0], o0[1]), a1 = pk(o0[2], o0[3]), b0 = pk(o1[0], o1[1]), b1 = pk(o1[2], o1[3]);
      *(LAS u2*)(oa + bq * 512) = sp ? u2{b0, b1} : u2{a0, a1};
      if (g == 0) *(LAS unsigned*)(ob + bq * 512) = odd ? a1 : b0;
    }
  }
  bar();
  STAMP(3);
  rsrc_t ro = mkrs(ws + WS_O, NB_ACT);
#pragma unroll
  for (int i = 0; i < 3; ++i) {
    int it = tid + 512 * i, c = it / 160, row = it - c * 160, gr = row0 + row;
    if (it < 1440) {
      u4 v = *(const LAS u4*)(qkv + c * PS + row * 16);
      unsigned off = ((gr >> 6) * 72 + hq * 9 + c) * 1024 + (gr & 63) * 16;
      if ((row < 14 && rt > 0) || gr >= 2048) off = OOB;
      __builtin_amdgcn_raw_buffer_store_b128(v, ro, off, 0, SC1);
    }
  }
  WAITV(0);
  bar();
  if (tid == 448) post_flag(ws, F_O + rt * 8 + hq, tag);
  STAMP(4);
}

DEV void role_b(char* sm, char* ws, const u16* q, const u16* k, const u16* v, const u16* shk, const u16* hk,
                const u16* dv, u16* out, u4 rwo, u64 tag, int xcd, int j, int w, int lane) {
  const int tid = threadIdx.x;
  const int jb = j - 14;
  const int grp = jb < 16 ? xcd : 8, idx = jb < 16 ? jb : xcd * 2 + (jb - 16);
  char* kt = sm + L_T;
  char* vt = kt + 136 * 128;
  {
    const u4 rk = mkdesc(k, NB_ACT), rv = mkdesc(v, NB_ACT);
    const int rr = w * 8 + (lane >> 3);
    const int f = ((rr >> 1) & 1) | (((rr >> 3) & 3) << 1);
    const unsigned off = (idx * 128 + rr) * 1152 + grp * 128 + (((lane & 7) ^ f) << 4);
    dma16(rk, kt + w * 1024, off - 8 * 1152, 0);
    dma16(rk, kt + w * 1024 + 8192, off + 56 * 1152, 0);
    if (w == 0) dma16(rk, kt + 16384, off + 120 * 1152, 0);
    dma16(rv, vt + w * 1024, off, 0);
    dma16(rv, vt + w * 1024 + 8192, off + 64 * 1152, 0);
  }
  const int G = lane >> 4, i16 = lane & 15;
  const u2 wk = *(const GAS u2*)((const char*)shk + (grp * 64 + w * 8 + (G & 1) * 4 + (i16 & 3)) * 8);
  const int ch0 = grp * 64 + idx * 4;
  rsrc_t rh = mkrs(hk, NB_ACT);
  u4 ha[2]; unsigned hp[2];
#pragma unroll
  for (int i = 0; i < 2; ++i) {
    int it = tid + 512 * i;
    unsigned off = ch0 * 4096 + it * 16;
    ha[i] = __builtin_amdgcn_raw_buffer_load_b128(rh, off, 0, 0);
    hp[i] = __builtin_amdgcn_raw_buffer_load_b32(rh, (it & 255) ? off - 4 : OOB, 0, 0);
  }
  const unsigned dval = dv[ch0 + (w & 3)];
  WAITV(0);
  bar();
  STAMP(1);
  rsrc_t ru = mkrs(ws + WS_U, NB_ACT);
  {
    const unsigned wA = alignb(wk[1], wk[1]), wB = alignb(wk[0], wk[0]);
    const int q4 = i16 >> 2, p4 = lane & 3;
#pragma unroll
    for (int s = 0; s < 2; ++s) {
      const int Y = 2 * s + (G >> 1);
      unsigned K[6], V[4];
#pragma unroll
      for (int T = 0; T < 3; ++T) {
        int rr = 32 * Y + 8 * p4 + 4 + 4 * T + q4;
        int f = ((rr >> 1) & 1) | (((rr >> 3) & 3) << 1);
        u2 t = __builtin_bit_cast(u2, trread(kt + rr * 128 + ((w ^ f) << 4) + (G & 1) * 8));
        K[2 * T] = t[0]; K[2 * T + 1] = t[1];
      }
#pragma unroll
      for (int T = 0; T < 2; ++T) {
        int rr = 32 * Y + 8 * p4 + 4 * T + q4;
        int f = ((rr >> 1) & 1) | (((rr >> 3) & 3) << 1);
        u2 t = __builtin_bit_cast(u2, trread(vt + rr * 128 + ((w ^ f) << 4) + (G & 1) * 8));
        V[2 * T] = t[0]; V[2 * T + 1] = t[1];
      }
      unsigned Sh[5];
#pragma unroll
      for (int d = 0; d < 5; ++d) Sh[d] = alignb(K[d + 1], K[d]);
      u4 o;
#pragma unroll
      for (int d = 0; d < 4; ++d) {
        float ev = dot2(Sh[d + 1], wB, dot2(Sh[d], wA, 0.f)) * lo16(V[d]);
        float od = dot2(K[d + 2], wB, dot2(K[d + 1], wA, 0.f)) * hi16(V[d]);
        o[3 - d] = pk(od, ev);
      }
      unsigned off = ((grp * 16 + 2 * w + (G & 1)) * 256 + idx * 16 + 4 * Y) * 64 + i16 * 16;
      __builtin_amdgcn_raw_buffer_store_b128(o, ru, off, 0, SC1);
    }
  }
#pragma unroll
  for (int i = 0; i < 2; ++i) {
    int it = tid + 512 * i;
    char* d = sm + L_H + (it >> 8) * H_CH + 32 + (it & 255) * 16;
    *(LAS u4*)(d + H_CP) = ha[i];
    *(LAS u4*)d = u4{alignb(ha[i][0], hp[i]), alignb(ha[i][1], ha[i][0]), alignb(ha[i][2], ha[i][1]), alignb(ha[i][3], ha[i][2])};
  }
  if (tid < 16) *(LAS u4*)(sm + L_H + (tid >> 2) * H_CH + ((tid >> 1) & 1) * H_CP + (tid & 1) * 16) = u4{0, 0, 0, 0};
  WAITV(0);
  bar();
  if (tid == 448) post_flag(ws, F_U + grp * 16 + idx, tag);
  STAMP(2);
  char* ui = sm + L_T;
  if (tid >= 128 && tid < 248) { int t = tid - 128, c = t / 30; *(LAS u4*)(ui + c * US + (t - c * 30) * 16) = u4{0, 0, 0, 0}; }
  if (w == 0) wait_flags(ws, lane < 16 ? F_U + grp * 16 + lane : -1, tag);
  bar();
  STAMP(3);
  {
    u4 uv[2];
#pragma unroll
    for (int i = 0; i < 2; ++i) uv[i] = __builtin_amdgcn_raw_buffer_load_b128(ru, (grp * 16 + idx) * 16384 + (tid + 512 * i) * 16, 0, SC1);
#pragma unroll
    for (int i = 0; i < 2; ++i) { int it = tid + 512 * i; *(LAS u4*)(ui + (it & 3) * US + (30 + (it >> 2)) * 16) = uv[i]; }
  }
  u4 qv[2];
  {
    rsrc_t rq = mkrs(q, NB_ACT);
#pragma unroll
    for (int i = 0; i < 2; ++i) { int it = tid + 512 * i; qv[i] = __builtin_amdgcn_raw_buffer_load_b128(rq, (idx * 128 + (it >> 3)) * 1152 + grp * 128 + (it & 7) * 16, 0, 0); }
  }
  bar();
  STAMP(4);
  rsrc_t ry = mkrs(ws + WS_Y, NB_ACT);
  if (w >= 4) {
    wo_dma(rwo, sm, j & 7, 0, 81, w - 4, 4, lane);
  } else {
    const int r = lane & 15, g = lane >> 4;
    const char* aa = sm + L_H + w * H_CH + (r & 1) * H_CP + 4 + 4 * (4 * g + (r >> 1));
    const char* ba = ui + w * US + 16 * (31 + 2 * r - g);
    f4 acc[8];
    bf8 A[2][8], B[2][8];
    auto rd = [&](int dd, bf8 (&a)[8], bf8 (&b)[8]) {
#pragma unroll
      for (int i = 0; i < 8; ++i) {
        b[i] = *(const LAS bf8*)(ba - dd * 64 + i * 512);
        const LAS unsigned* p = (const LAS unsigned*)(aa + dd * 64 + i * 512);
        a[i] = __builtin_bit_cast(bf8, u4{p[0], p[1], p[2], p[3]});
      }
    };
    auto mm = [&](bf8 (&a)[8], bf8 (&b)[8]) {
#pragma unroll
      for (int i = 0; i < 8; ++i)
#pragma unroll
        for (int jj = 0; jj + i < 8; ++jj) acc[i + jj] = mfma(a[i], b[jj], acc[i + jj]);
    };
    rd(0, A[0], B[0]);
    {
      const int x = 15 - r - 8 * g;
      u4 ad;
#pragma unroll
      for (int d = 0; d < 4; ++d) ad[d] = x == 2 * d ? dval : (x == 2 * d + 1 ? dval << 16 : 0u);
#pragma unroll
      for (int n = 0; n < 8; ++n) acc[n] = mfma(__builtin_bit_cast(bf8, ad), B[0][n], f4{0.f, 0.f, 0.f, 0.f});
    }
#pragma unroll 1
    for (int dd = 0; dd < 8; dd += 2) {
      rd(dd + 1, A[1], B[1]);
      mm(A[0], B[0]);
      rd(dd + 2 < 8 ? dd + 2 : 0, A[0], B[0]);
      mm(A[1], B[1]);
    }
#pragma unroll
    for (int a = 0; a < 4; ++a) {
      unsigned x0 = pk(acc[2 * a][0], acc[2 * a][1]), x1 = pk(acc[2 * a][2], acc[2 * a][3]);
      unsigned y0 = pk(acc[2 * a + 1][0], acc[2 * a + 1][1]), y1 = pk(acc[2 * a + 1][2], acc[2 * a + 1][3]);
      auto s0 = __builtin_amdgcn_permlane16_swap(x0, y0, false, false);
      auto s1 = __builtin_amdgcn_permlane16_swap(x1, y1, false, false);
      int n = 2 * a + (g & 1);
      unsigned off = ((grp * 16 + 2 * n + (r >> 3)) * 64 + idx * 4 + w) * 256 + ((g >> 1) * 8 + (r & 7)) * 16;
      __builtin_amdgcn_raw_buffer_store_b128(u4{s0[0], s1[0], s0[1], s1[1]}, ry, off, 0, SC1);
    }
  }
  STAMP(5);
  WAITV(0);
  bar();
  if (tid == 448) post_flag(ws, F_Y + grp * 16 + idx, tag);
  STAMP(6);
  if (w == 0) wait_flags(ws, lane < 16 ? F_Y + grp * 16 + lane : -1, tag);
  bar();
  STAMP(7);
  char* yt = sm + L_YT;
  {
    u4 yv[2];
#pragma unroll
    for (int i = 0; i < 2; ++i) yv[i] = __builtin_amdgcn_raw_buffer_load_b128(ry, (grp * 16 + idx) * 16384 + (tid + 512 * i) * 16, 0, SC1);
#pragma unroll
    for (int i = 0; i < 2; ++i) {
      int it = tid + 512 * i, c = it >> 4, pos = it & 15;
      *(LAS u4*)(yt + (c >> 3) * 2064 + (c & 7) * 256 + (2 * (pos & 7) + (pos >> 3)) * 16) = yv[i];
    }
  }
  bar();
#pragma unroll
  for (int i = 0; i < 2; ++i) {
    int it = tid + 512 * i, row = it >> 3, c = it & 7;
    const LAS u16* p = (const LAS u16*)(yt + c * 2064 + row * 2);
    u4 o;
#pragma unroll
    for (int d = 0; d < 4; ++d)
      o[d] = pk(lo16(p[d * 256]) * lo16(qv[i][d]), lo16(p[d * 256 + 128]) * hi16(qv[i][d]));
    *(GAS u4*)((char*)out + (idx * 128 + row) * 2304 + 1152 + grp * 128 + c * 16) = o;
  }
  STAMP(8);
}

__global__ __launch_bounds__(512, 2) void fused(const u16* x, const u16* wqkv, const u16* wo, const u16* q, const u16* k,
                                                const u16* v, const u16* shk, const u16* hk, const u16* dv, u16* out,
                                                char* ws) {
  extern __shared__ __attribute__((aligned(16))) char sm[];
  const int tid = threadIdx.x, lane = tid & 63, w = __builtin_amdgcn_readfirstlane(tid >> 6);
  const int bid = blockIdx.x, xcd = bid & 7, j = bid >> 3;
  u64 rt0, ct0;
  asm volatile("s_memrealtime %0\n\ts_memtime %1" : "=s"(rt0), "=s"(ct0));
  const u64 tag = __hip_atomic_load((GAS u64*)ws, RLX_AGENT);
  const u4 rwo = mkdesc(wo, NB_WO);
  if (j < 14) role_a(sm, ws, x, wqkv, rwo, tag, xcd, j, w, lane);
  else role_b(sm, ws, q, k, v, shk, hk, dv, out, rwo, tag, xcd, j, w, lane);

  const int rt = xcd * 4 + (j >> 3), ct = j & 7;
  if (w == 0) {
    if (bid == 0) {
#pragma unroll 1
      for (int i = 0; i < 4; ++i) wait_flags(ws, i * 64 + lane, tag);
      if (lane == 0) __hip_atomic_store((GAS u64*)ws, mktag(tag), RLX_AGENT);
    } else {
      int ra = rt * 64 + (lane < 8 ? 0 : 63);
      int ti = ra < 14 ? 0 : (ra - 14) / 146;
      wait_flags(ws, lane < 16 ? F_O + ti * 8 + (lane & 7) : -1, tag);
    }
  }
  WAITV(0);
  bar();
  STAMP(9);
  const int r = lane & 15, g = lane >> 4, m = w & 3, kh = w >> 2;
  rsrc_t ro = mkrs(ws + WS_O, NB_ACT);
  u4 of[9];
#pragma unroll
  for (int i = 0; i < 9; ++i)
    of[i] = __builtin_amdgcn_raw_buffer_load_b128(ro, (rt * 72 + kh * 36 + g) * 1024 + (m * 16 + r) * 16, i * 4096, SC1);
  __builtin_amdgcn_sched_barrier(0);
  const char* wa = sm + L_WO + (kh * 288 + ((g >> 1) << 4) + ((g & 1) << 3) + ((r >> 2) << 1)) * 144 + (lane & 3) * 8;
  f4 acc[5];
#pragma unroll
  for (int n = 0; n < 5; ++n) acc[n] = f4{0.f, 0.f, 0.f, 0.f};
#pragma unroll
  for (int i = 0; i < 9; ++i)
#pragma unroll
    for (int n = 0; n < 5; ++n)
      acc[n] = mfma(cat8(trread(wa + i * 32 * 144 + n * 32), trread(wa + i * 32 * 144 + 144 + n * 32)),
                    __builtin_bit_cast(bf8, of[i]), acc[n]);
  STAMP(10);
  char* red = sm + L_RED;
  if (kh) {
#pragma unroll
    for (int n = 0; n < 5; ++n) *(LAS f4*)(red + ((m * 5 + n) * 64 + lane) * 16) = acc[n];
  }
  bar();
  char* ot = red + 20480;
  if (!kh) {
#pragma unroll
    for (int n = 0; n < 5; ++n) {
      f4 t = acc[n] + *(const LAS f4*)(red + ((m * 5 + n) * 64 + lane) * 16);
      if (n < 4 || g < 2) *(LAS u2*)(ot + (m * 16 + r) * 144 + n * 32 + g * 8) = u2{pk(t[0], t[1]), pk(t[2], t[3])};
    }
  }
  bar();
#pragma unroll
  for (int i = 0; i < 2; ++i) {
    int it = tid + 512 * i, row = it / 9, c = it - row * 9;
    if (it < 576) *(GAS u4*)((char*)out + (rt * 64 + row) * 2304 + ct * 144 + c * 16) = *(const LAS u4*)(ot + it * 16);
  }
  STAMP(11); CSTAMP(1);
  if (threadIdx.x == 0) { ((GAS u64*)(ws + WS_DBG + 65536))[blockIdx.x * 2] = rt0; ((GAS u64*)(ws + WS_DBG))[blockIdx.x * 32] = ct0; }
}

__global__ void k_spin(const char* ws){
  const u64* st=(const u64*)(ws+WS_DBG); const u64* rt=(const u64*)(ws+WS_DBG+65536);
  u64 sum=0; for(int b=0;b<256;b++) if((b>>3)<14) sum+=st[b*32+16]-st[b*32+0]; u64 d=sum*10/112;
  if(d>1000000) d=1000000;
  u64 s=__builtin_amdgcn_s_memrealtime();
  while(__builtin_amdgcn_s_memrealtime()-s<d) __builtin_amdgcn_s_sleep(8);
}
extern "C" void kernel_launch(void* const* d_in, const int* in_sizes, int n_in, void* d_out, int out_size, void* d_ws,
                              size_t ws_size, hipStream_t stream) {
  (void)hipFuncSetAttribute((const void*)fused, hipFuncAttributeMaxDynamicSharedMemorySize, LDS_BYTES);
  fused<<<256, 512, LDS_BYTES, stream>>>((const u16*)d_in[0], (const u16*)d_in[1], (const u16*)d_in[2], (const u16*)d_in[3],
                                         (const u16*)d_in[4], (const u16*)d_in[5], (const u16*)d_in[6], (const u16*)d_in[7],
                                         (const u16*)d_in[8], (u16*)d_out, (char*)d_ws);
  k_spin<<<1,1,0,stream>>>((const char*)d_ws);
}
```

```cpp
#include <hip/hip_runtime.h>

typedef unsigned short u16;
typedef unsigned long long u64;
typedef short s4 __attribute__((ext_vector_type(4)));
typedef short s8 __attribute__((ext_vector_type(8)));
typedef __bf16 bf2 __attribute__((ext_vector_type(2)));
typedef __bf16 bf8 __attribute__((ext_vector_type(8)));
typedef float f2 __attribute__((ext_vector_type(2)));
typedef float f4 __attribute__((ext_vector_type(4)));
typedef unsigned u4 __attribute__((ext_vector_type(4)));
typedef unsigned u2 __attribute__((ext_vector_type(2)));
typedef __amdgpu_buffer_rsrc_t rsrc_t;
#define LAS __attribute__((address_space(3)))
#define GAS __attribute__((address_space(1)))
#define RLX_AGENT __ATOMIC_RELAXED, __HIP_MEMORY_SCOPE_AGENT
#define DEV __device__ __forceinline__

constexpr int NB_ACT = 2048 * 576 * 2;
constexpr int NB_W = 576 * 1728 * 2;
constexpr int NB_WO = 576 * 576 * 2;
constexpr unsigned OOB = 0x80000000u;
constexpr int SC1 = 16;
#define OUT_AUX 0

constexpr int WS_FLAG = 4096;
constexpr int F_U = 0, F_O = 144, F_Y = 256;
constexpr int WS_U = 1 << 20;
constexpr int WS_Y = WS_U + (3 << 20);
constexpr int WS_O = WS_Y + (3 << 20);
constexpr int WS_DBG = 16 << 20;

constexpr int LDS_BYTES = 163840;
constexpr int SLOT = 49152;
constexpr int L_WO = 0;
constexpr int L_QKV = 92160;
constexpr int PS = 161 * 16;
constexpr int L_RED = 83968;
constexpr int L_H = 83968;
constexpr int H_CP = 4160, H_CH = 8448;
constexpr int L_T = L_H + 4 * H_CH;
constexpr int US = 286 * 16;
constexpr int L_YT = L_T + 4 * US + 128;

#define STAMP(i) do {} while (0)
#define CSTAMP(i) do {} while (0)

DEV rsrc_t mkrs(const void* p, int bytes) { return __builtin_amdgcn_make_buffer_rsrc((void*)p, 0, bytes, 0x00020000); }
DEV u4 mkdesc(const void* p, int bytes) {
  u64 a = (u64)p;
  return u4{(unsigned)a, (unsigned)(a >> 32) & 0xffffu, (unsigned)bytes, 0x00020000u};
}
DEV void dma16(u4 r, char* lds, unsigned voff, unsigned soff) {
  asm volatile("s_mov_b32 m0, %0\n\ts_nop 0\n\tbuffer_load_dwordx4 %1, %2, %3 offen lds"
               :: "s"((unsigned)(size_t)(LAS char*)lds), "v"(voff), "s"(r), "s"(soff) : "memory");
}
DEV s4 trread(const char* p) { return __builtin_amdgcn_ds_read_tr16_b64_v4i16((LAS s4*)p); }
DEV bf8 cat8(s4 a, s4 b) { return __builtin_bit_cast(bf8, __builtin_shufflevector(a, b, 0, 1, 2, 3, 4, 5, 6, 7)); }
DEV f4 mfma(bf8 a, bf8 b, f4 c) { return __builtin_amdgcn_mfma_f32_16x16x32_bf16(a, b, c, 0, 0, 0); }
DEV unsigned pk(float lo, float hi) { f2 v = {lo, hi}; return __builtin_bit_cast(unsigned, __builtin_convertvector(v, bf2)); }
DEV float lo16(unsigned x) { return __builtin_bit_cast(float, x << 16); }
DEV float hi16(unsigned x) { return __builtin_bit_cast(float, x & 0xffff0000u); }
DEV float dot2(unsigned a, unsigned b, float c) {
  return __builtin_amdgcn_fdot2_f32_bf16(__builtin_bit_cast(bf2, a), __builtin_bit_cast(bf2, b), c, false);
}
DEV unsigned alignb(unsigned hi, unsigned lo) { return __builtin_amdgcn_alignbit(hi, lo, 16); }
#define WAITV(n) asm volatile("s_waitcnt vmcnt(" #n ")" ::: "memory")
#define WAITL0() asm volatile("s_waitcnt lgkmcnt(0)" ::: "memory")
DEV void bar() { WAITL0(); __builtin_amdgcn_s_barrier(); asm volatile("" ::: "memory"); }
DEV int unperm(int p) { int u = (p & 15) >> 1; return (p & ~15) + ((u >> 2) << 3) + ((p & 1) << 2) + (u & 3); }

DEV u64 mktag(u64 e) { asm volatile("" : "+v"(e)); return e * 6364136223846793005ull + 1442695040888963407ull; }
DEV void post_flag(char* ws, int idx, u64 e) { u64 tag = mktag(e); __hip_atomic_store((GAS u64*)(ws + WS_FLAG + idx * 128), tag, RLX_AGENT); }
DEV void wait_flags(char* ws, int idx, u64 e) {
  const u64 tag = mktag(e);
#pragma unroll 1
  for (int spin = 0; spin < (1 << 20); ++spin) {
    u64 v = tag;
    if (idx >= 0) v = __hip_atomic_load((GAS u64*)(ws + WS_FLAG + idx * 128), RLX_AGENT);
    if (__all(v == tag)) break;
    __builtin_amdgcn_s_sleep(1);
  }
  __builtin_amdgcn_fence(__ATOMIC_ACQUIRE, "wavefront");
}

DEV void wo_dma(u4 rwo, char* sm, int ct, int i0, int i1, int w, int nw, int lane) {
#pragma unroll 1
  for (int i = i0 + w; i < i1; i += nw) {
    int c = i * 64 + lane, p = c / 9, ch = c - p * 9;
    dma16(rwo, sm + L_WO + i * 1024, unperm(p) * 1152 + ct * 144 + ch * 16, 0);
  }
}

template <int NN>
DEV void g1_loop(char* sm, f4 (&acc)[5][4], u4 rx, u4 rw, u4 rwo, const unsigned (&vo)[6], int w, int lane,
                 int m0, int n0, int rot, int ct, char* ws) {
  const int r = lane & 15, g = lane >> 4;
  const int xa0 = (m0 * 16 + r) * 128 + ((g ^ ((r >> 1) & 7)) << 4);
  const int xa1 = xa0 ^ 64;
  const int wa = 20480 + (((g >> 1) << 4) + ((g & 1) << 3) + ((r >> 2) << 1)) * 432 + (lane & 3) * 8 + n0 * 32;
  bf8 xf[2][5], wf[2][NN];
  auto issue = [&](int st) {
    int kt = st + rot; if (kt >= 9) kt -= 9;
    char* d = sm + (st % 3) * SLOT + w * 1024;
    unsigned sx = kt * 128, sw = kt * (64 * 3456);
    dma16(rx, d, vo[0], sx);
    dma16(rx, d + 8192, vo[1], sx);
    dma16(w < 4 ? rx : rw, d + 16384, vo[2], w < 4 ? sx : sw);
    dma16(rw, d + 24576, vo[3], sw);
    dma16(rw, d + 32768, vo[4], sw);
    dma16(rw, d + 40960, vo[5], sw);
  };
  auto rd = [&](const char* s, int ks, bf8 (&x)[5], bf8 (&wv)[NN]) {
#pragma unroll
    for (int n = 0; n < NN; ++n)
      wv[n] = cat8(trread(s + wa + ks * 32 * 432 + n * 32), trread(s + wa + ks * 32 * 432 + 432 + n * 32));
#pragma unroll
    for (int m = 0; m < 5; ++m) x[m] = *(const LAS bf8*)(s + (ks ? xa1 : xa0) + m * 2048);
  };
  auto mm = [&](bf8 (&x)[5], bf8 (&wv)[NN]) {
#pragma unroll
    for (int m = 0; m < 5; ++m)
#pragma unroll
      for (int n = 0; n < NN; ++n) acc[m][n] = mfma(wv[n], x[m], acc[m][n]);
  };
  STAMP(16);
  issue(0); issue(1); issue(2);
  WAITV(12); bar();
  STAMP(17);
  rd(sm, 0, xf[0], wf[0]);
  rd(sm, 1, xf[1], wf[1]);
  mm(xf[0], wf[0]);
  int sl = 1;
#pragma unroll 1
  for (int it = 1; it < 9; ++it) {
    WAITV(6); bar();
    STAMP(17 + it);
    if (it < 7) issue(it + 2);
    else if (it == 7) wo_dma(rwo, sm, ct, 0, 48, w, 8, lane);
    else wo_dma(rwo, sm, ct, 48, 81, w, 8, lane);
    const char* s = sm + sl * SLOT;
    rd(s, 0, xf[0], wf[0]);
    mm(xf[1], wf[1]);
    rd(s, 1, xf[1], wf[1]);
    mm(xf[0], wf[0]);
    sl = sl == 2 ? 0 : sl + 1;
  }
  mm(xf[1], wf[1]);
}

DEV float asf(unsigned u) { return __builtin_bit_cast(float, u); }
DEV unsigned asu(float f) { return __builtin_bit_cast(unsigned, f); }
DEV float xmax(float x) {
  auto a = __builtin_amdgcn_permlane16_swap(asu(x), asu(x), false, false);
  x = fmaxf(asf(a[0]), asf(a[1]));
  auto b = __builtin_amdgcn_permlane32_swap(asu(x), asu(x), false, false);
  return fmaxf(asf(b[0]), asf(b[1]));
}
DEV float xsum(float x) {
  auto a = __builtin_amdgcn_permlane16_swap(asu(x), asu(x), false, false);
  x = asf(a[0]) + asf(a[1]);
  auto b = __builtin_amdgcn_permlane32_swap(asu(x), asu(x), false, false);
  return asf(b[0]) + asf(b[1]);
}

DEV void role_a(char* sm, char* ws, const u16* x, const u16* wqkv, u4 rwo, u64 tag, int xcd, int j, int w, int lane) {
  const int tid = threadIdx.x;
  const int rt = (xcd >> 2) * 7 + (j >> 1), hq = (xcd & 3) * 2 + (j & 1);
  const int row0 = rt * 146;
  const u4 rx = mkdesc(x, NB_ACT), rw = mkdesc(wqkv, NB_W);
  unsigned vo[6];
#pragma unroll
  for (int i = 0; i < 6; ++i) {
    int s = w + 8 * i;
    int xr = s * 8 + (lane >> 3);
    unsigned ox = (row0 + xr) * 1152 + (((lane & 7) ^ ((xr >> 1) & 7)) << 4);
    int c = (s - 20) * 64 + lane, p = c / 27, cc = c - p * 27, part = cc / 9, ch = cc - part * 9;
    unsigned ow = unperm(p) * 3456 + part * 1152 + hq * 144 + ch * 16;
    vo[i] = s < 20 ? ox : (s < 47 ? ow : OOB);
  }
  f4 acc[5][4];
#pragma unroll
  for (int m = 0; m < 5; ++m)
#pragma unroll
    for (int n = 0; n < 4; ++n) acc[m][n] = f4{0.f, 0.f, 0.f, 0.f};
  const int wm = w >> 2, nr = wm ? 3 - (w & 3) : (w & 3);
  const int n0 = nr < 2 ? nr * 4 : 2 + nr * 3, m0 = wm * 5;
  const int rot = 0;
  if (nr < 2) g1_loop<4>(sm, acc, rx, rw, rwo, vo, w, lane, m0, n0, rot, j & 7, ws);
  else g1_loop<3>(sm, acc, rx, rw, rwo, vo, w, lane, m0, n0, rot, j & 7, ws);
  STAMP(1);
  bar();
  const int r = lane & 15, g = lane >> 4;
  char* qkv = sm + L_QKV;
  {
    char* d = qkv + (2 * n0 + (g >> 1)) * PS + (m0 * 16 + r) * 16 + (g & 1) * 8;
#pragma unroll
    for (int n = 0; n < 4; ++n) {
      if (n < 3 || nr < 2) {
        if (2 * (n0 + n) + (g >> 1) < 27) {
#pragma unroll
          for (int m = 0; m < 5; ++m)
            *(LAS u2*)(d + n * 2 * PS + m * 256) = u2{pk(acc[m][n][0], acc[m][n][1]), pk(acc[m][n][2], acc[m][n][3])};
        }
      }
    }
    if (tid < 27) *(LAS u4*)(qkv + tid * PS + 160 * 16) = u4{0, 0, 0, 0};
  }
  bar();
  STAMP(2);
  {
    const int h = w & 3, par = w >> 2, odd = h & 1;
    const char* qa = qkv + (2 * h + g) * PS + (par + 2 * r) * 16;
    const char* ka = qa + 9 * PS - 256;
    const int d0 = 18 * h - 2 * odd, q4 = r >> 2, p4 = lane & 3;
    const int vc0 = d0 + 4 * p4, vc1 = d0 + 16;
    const int kr0 = par - 16 + 2 * (4 * g + q4), kr1 = kr0 + (g >= 2 ? 16 : 32);
    const char* v0 = qkv + (18 + (vc0 >> 3)) * PS + ((vc0 >> 2) & 1) * 8;
    const char* v1 = qkv + (18 + (vc1 >> 3)) * PS + ((vc1 >> 2) & 1) * 8;
    u4 qm;
    bool c0[4], c1[4];
#pragma unroll
    for (int d = 0; d < 4; ++d) {
      int e = 8 * g + 2 * d - 2 * h;
      qm[d] = (e >= 0 && e < 18) ? 0xffffffffu : 0u;
      int kk = 4 * g + d - r;
      c0[d] = kk >= 1 && kk <= 8;
      c1[d] = kk + 16 <= 8;
    }
    const unsigned n01 = (c1[0] ? 0xffffu : 0u) | (c1[1] ? 0xffff0000u : 0u);
    const unsigned n23 = (c1[2] ? 0xffffu : 0u) | (c1[3] ? 0xffff0000u : 0u);
    const bool sp = g == 0 && odd;
    const int ca = sp ? d0 + 16 : d0 + 4 * g, cb = odd ? d0 + 2 : d0 + 16;
    char* oa = qkv + (ca >> 3) * PS + (par + 2 * r) * 16 + (ca & 7) * 2;
    char* ob = qkv + (cb >> 3) * PS + (par + 2 * r) * 16 + (cb & 7) * 2;
    const float cs = 0.23570226039551584f * 1.4426950408889634f;
    const float ninf = -__builtin_inff();
#pragma unroll
    for (int bq = 0; bq < 5; ++bq) {
      u4 qf = *(const LAS u4*)(qa + bq * 512) & qm;
      bf8 k0 = *(const LAS bf8*)(ka + bq * 512), k1 = *(const LAS bf8*)(ka + bq * 512 + 512);
      bf8 vt0 = cat8(trread(v0 + kr0 * 16 + bq * 512), trread(v0 + kr1 * 16 + bq * 512));
      bf8 vt1 = cat8(trread(v1 + kr0 * 16 + bq * 512), trread(v1 + kr1 * 16 + bq * 512));
      const f4 z = {0.f, 0.f, 0.f, 0.f};
      f4 s0 = mfma(k0, __builtin_bit_cast(bf8, qf), z), s1 = mfma(k1, __builtin_bit_cast(bf8, qf), z);
      float val[4];
#pragma unroll
      for (int d = 0; d < 4; ++d) {
        bool a0 = bq == 0 ? (c0[d] && g >= 2) : c0[d];
        val[d] = c1[d] ? s1[d] : (a0 ? s0[d] : ninf);
      }
      float mx = xmax(fmaxf(fmaxf(val[0], val[1]), fmaxf(val[2], val[3])));
      float mc = -mx * cs;
#pragma unroll
      for (int d = 0; d < 4; ++d) val[d] = __builtin_amdgcn_exp2f(__builtin_fmaf(val[d], cs, mc));
      float inv = __builtin_amdgcn_rcpf(xsum((val[0] + val[1]) + (val[2] + val[3])));
      unsigned p01 = pk(val[0] * inv, val[1] * inv), p23 = pk(val[2] * inv, val[3] * inv);
      u4 pf = {p01 & ~n01, p23 & ~n23, p01 & n01, p23 & n23};
      f4 o0 = mfma(vt0, __builtin_bit_cast(bf8, pf), z), o1 = mfma(vt1, __builtin_bit_cast(bf8, pf), z);
      unsigned a0 = pk(o0[0], o0[1]), a1 = pk(o0[2], o0[3]), b0 = pk(o1[0], o1[1]), b1 = pk(o1[2], o1[3]);
      *(LAS u2*)(oa + bq * 512) = sp ? u2{b0, b1} : u2{a0, a1};
      if (g == 0) *(LAS unsigned*)(ob + bq * 512) = odd ? a1 : b0;
    }
  }
  bar();
  STAMP(3);
  rsrc_t ro = mkrs(ws + WS_O, NB_ACT);
#pragma unroll
  for (int i = 0; i < 3; ++i) {
    int it = tid + 512 * i, c = it / 160, row = it - c * 160, gr = row0 + row;
    if (it < 1440) {
      u4 v = *(const LAS u4*)(qkv + c * PS + row * 16);
      unsigned off = ((gr >> 6) * 72 + hq * 9 + c) * 1024 + (gr & 63) * 16;
      if ((row < 14 && rt > 0) || gr >= 2048) off = OOB;
      __builtin_amdgcn_raw_buffer_store_b128(v, ro, off, 0, SC1);
    }
  }
  WAITV(0);
  bar();
  if (tid == 448) post_flag(ws, F_O + rt * 8 + hq, tag);
  STAMP(4);
}

DEV void role_b(char* sm, char* ws, const u16* q, const u16* k, const u16* v, const u16* shk, const u16* hk,
                const u16* dv, u16* out, u4 rwo, u64 tag, int xcd, int j, int w, int lane) {
  const int tid = threadIdx.x;
  const int jb = j - 14;
  const int grp = jb < 16 ? xcd : 8, idx = jb < 16 ? jb : xcd * 2 + (jb - 16);
  char* kt = sm + L_T;
  char* vt = kt + 136 * 128;
  {
    const u4 rk = mkdesc(k, NB_ACT), rv = mkdesc(v, NB_ACT);
    const int rr = w * 8 + (lane >> 3);
    const int f = ((rr >> 1) & 1) | (((rr >> 3) & 3) << 1);
    const unsigned off = (idx * 128 + rr) * 1152 + grp * 128 + (((lane & 7) ^ f) << 4);
    dma16(rk, kt + w * 1024, off - 8 * 1152, 0);
    dma16(rk, kt + w * 1024 + 8192, off + 56 * 1152, 0);
    if (w == 0) dma16(rk, kt + 16384, off + 120 * 1152, 0);
    dma16(rv, vt + w * 1024, off, 0);
    dma16(rv, vt + w * 1024 + 8192, off + 64 * 1152, 0);
  }
  const int G = lane >> 4, i16 = lane & 15;
  const u2 wk = *(const GAS u2*)((const char*)shk + (grp * 64 + w * 8 + (G & 1) * 4 + (i16 & 3)) * 8);
  const int ch0 = grp * 64 + idx * 4;
  rsrc_t rh = mkrs(hk, NB_ACT);
  u4 ha[2]; unsigned hp[2];
#pragma unroll
  for (int i = 0; i < 2; ++i) {
    int it = tid + 512 * i;
    unsigned off = ch0 * 4096 + it * 16;
    ha[i] = __builtin_amdgcn_raw_buffer_load_b128(rh, off, 0, 0);
    hp[i] = __builtin_amdgcn_raw_buffer_load_b32(rh, (it & 255) ? off - 4 : OOB, 0, 0);
  }
  const unsigned dval = dv[ch0 + (w & 3)];
  WAITV(0);
  bar();
  STAMP(1);
  rsrc_t ru = mkrs(ws + WS_U, NB_ACT);
  {
    const unsigned wA = alignb(wk[1], wk[1]), wB = alignb(wk[0], wk[0]);
    const int q4 = i16 >> 2, p4 = lane & 3;
#pragma unroll
    for (int s = 0; s < 2; ++s) {
      const int Y = 2 * s + (G >> 1);
      unsigned K[6], V[4];
#pragma unroll
      for (int T = 0; T < 3; ++T) {
        int rr = 32 * Y + 8 * p4 + 4 + 4 * T + q4;
        int f = ((rr >> 1) & 1) | (((rr >> 3) & 3) << 1);
        u2 t = __builtin_bit_cast(u2, trread(kt + rr * 128 + ((w ^ f) << 4) + (G & 1) * 8));
        K[2 * T] = t[0]; K[2 * T + 1] = t[1];
      }
#pragma unroll
      for (int T = 0; T < 2; ++T) {
        int rr = 32 * Y + 8 * p4 + 4 * T + q4;
        int f = ((rr >> 1) & 1) | (((rr >> 3) & 3) << 1);
        u2 t = __builtin_bit_cast(u2, trread(vt + rr * 128 + ((w ^ f) << 4) + (G & 1) * 8));
        V[2 * T] = t[0]; V[2 * T + 1] = t[1];
      }
      unsigned Sh[5];
#pragma unroll
      for (int d = 0; d < 5; ++d) Sh[d] = alignb(K[d + 1], K[d]);
      u4 o;
#pragma unroll
      for (int d = 0; d < 4; ++d) {
        float ev = dot2(Sh[d + 1], wB, dot2(Sh[d], wA, 0.f)) * lo16(V[d]);
        float od = dot2(K[d + 2], wB, dot2(K[d + 1], wA, 0.f)) * hi16(V[d]);
        o[3 - d] = pk(od, ev);
      }
      unsigned off = ((grp * 16 + 2 * w + (G & 1)) * 256 + idx * 16 + 4 * Y) * 64 + i16 * 16;
      __builtin_amdgcn_raw_buffer_store_b128(o, ru, off, 0, SC1);
    }
  }
#pragma unroll
  for (int i = 0; i < 2; ++i) {
    int it = tid + 512 * i;
    char* d = sm + L_H + (it >> 8) * H_CH + 32 + (it & 255) * 16;
    *(LAS u4*)(d + H_CP) = ha[i];
    *(LAS u4*)d = u4{alignb(ha[i][0], hp[i]), alignb(ha[i][1], ha[i][0]), alignb(ha[i][2], ha[i][1]), alignb(ha[i][3], ha[i][2])};
  }
  if (tid < 16) *(LAS u4*)(sm + L_H + (tid >> 2) * H_CH + ((tid >> 1) & 1) * H_CP + (tid & 1) * 16) = u4{0, 0, 0, 0};
  WAITV(0);
  bar();
  if (tid == 448) post_flag(ws, F_U + grp * 16 + idx, tag);
  STAMP(2);
  char* ui = sm + L_T;
  if (tid >= 128 && tid < 248) { int t = tid - 128, c = t / 30; *(LAS u4*)(ui + c * US + (t - c * 30) * 16) = u4{0, 0, 0, 0}; }
  if (w == 0) wait_flags(ws, lane < 16 ? F_U + grp * 16 + lane : -1, tag);
  bar();
  STAMP(3);
  {
    u4 uv[2];
#pragma unroll
    for (int i = 0; i < 2; ++i) uv[i] = __builtin_amdgcn_raw_buffer_load_b128(ru, (grp * 16 + idx) * 16384 + (tid + 512 * i) * 16, 0, SC1);
#pragma unroll
    for (int i = 0; i < 2; ++i) { int it = tid + 512 * i; *(LAS u4*)(ui + (it & 3) * US + (30 + (it >> 2)) * 16) = uv[i]; }
  }
  u4 qv[2];
  {
    rsrc_t rq = mkrs(q, NB_ACT);
#pragma unroll
    for (int i = 0; i < 2; ++i) { int it = tid + 512 * i; qv[i] = __builtin_amdgcn_raw_buffer_load_b128(rq, (idx * 128 + (it >> 3)) * 1152 + grp * 128 + (it & 7) * 16, 0, 0); }
  }
  bar();
  STAMP(4);
  rsrc_t ry = mkrs(ws + WS_Y, NB_ACT);
  if (w >= 4) {
    wo_dma(rwo, sm, j & 7, 0, 81, w - 4, 4, lane);
  } else {
    const int r = lane & 15, g = lane >> 4;
    const char* aa = sm + L_H + w * H_CH + (r & 1) * H_CP + 4 + 4 * (4 * g + (r >> 1));
    const char* ba = ui + w * US + 16 * (31 + 2 * r - g);
    f4 acc[8];
    bf8 A[2][8], B[2][8];
    auto rd = [&](int dd, bf8 (&a)[8], bf8 (&b)[8]) {
#pragma unroll
      for (int i = 0; i < 8; ++i) {
        b[i] = *(const LAS bf8*)(ba - dd * 64 + i * 512);
        const LAS unsigned* p = (const LAS unsigned*)(aa + dd * 64 + i * 512);
        a[i] = __builtin_bit_cast(bf8, u4{p[0], p[1], p[2], p[3]});
      }
    };
    auto mm = [&](bf8 (&a)[8], bf8 (&b)[8]) {
#pragma unroll
      for (int i = 0; i < 8; ++i)
#pragma unroll
        for (int jj = 0; jj + i < 8; ++jj) acc[i + jj] = mfma(a[i], b[jj], acc[i + jj]);
    };
    rd(0, A[0], B[0]);
    {
      const int x = 15 - r - 8 * g;
      u4 ad;
#pragma unroll
      for (int d = 0; d < 4; ++d) ad[d] = x == 2 * d ? dval : (x == 2 * d + 1 ? dval << 16 : 0u);
#pragma unroll
      for (int n = 0; n < 8; ++n) acc[n] = mfma(__builtin_bit_cast(bf8, ad), B[0][n], f4{0.f, 0.f, 0.f, 0.f});
    }
#pragma unroll 1
    for (int dd = 0; dd < 8; dd += 2) {
      rd(dd + 1, A[1], B[1]);
      mm(A[0], B[0]);
      rd(dd + 2 < 8 ? dd + 2 : 0, A[0], B[0]);
      mm(A[1], B[1]);
    }
#pragma unroll
    for (int a = 0; a < 4; ++a) {
      unsigned x0 = pk(acc[2 * a][0], acc[2 * a][1]), x1 = pk(acc[2 * a][2], acc[2 * a][3]);
      unsigned y0 = pk(acc[2 * a + 1][0], acc[2 * a + 1][1]), y1 = pk(acc[2 * a + 1][2], acc[2 * a + 1][3]);
      auto s0 = __builtin_amdgcn_permlane16_swap(x0, y0, false, false);
      auto s1 = __builtin_amdgcn_permlane16_swap(x1, y1, false, false);
      int n = 2 * a + (g & 1);
      unsigned off = ((grp * 16 + 2 * n + (r >> 3)) * 64 + idx * 4 + w) * 256 + ((g >> 1) * 8 + (r & 7)) * 16;
      __builtin_amdgcn_raw_buffer_store_b128(u4{s0[0], s1[0], s0[1], s1[1]}, ry, off, 0, SC1);
    }
  }
  STAMP(5);
  WAITV(0);
  bar();
  if (tid == 448) post_flag(ws, F_Y + grp * 16 + idx, tag);
  STAMP(6);
  if (w == 0) wait_flags(ws, lane < 16 ? F_Y + grp * 16 + lane : -1, tag);
  bar();
  STAMP(7);
  char* yt = sm + L_YT;
  {
    u4 yv[2];
#pragma unroll
    for (int i = 0; i < 2; ++i) yv[i] = __builtin_amdgcn_raw_buffer_load_b128(ry, (grp * 16 + idx) * 16384 + (tid + 512 * i) * 16, 0, SC1);
#pragma unroll
    for (int i = 0; i < 2; ++i) {
      int it = tid + 512 * i, c = it >> 4, pos = it & 15;
      *(LAS u4*)(yt + (c >> 3) * 2064 + (c & 7) * 256 + (2 * (pos & 7) + (pos >> 3)) * 16) = yv[i];
    }
  }
  bar();
#pragma unroll
  for (int i = 0; i < 2; ++i) {
    int it = tid + 512 * i, row = it >> 3, c = it & 7;
    const LAS u16* p = (const LAS u16*)(yt + c * 2064 + row * 2);
    u4 o;
#pragma unroll
    for (int d = 0; d < 4; ++d)
      o[d] = pk(lo16(p[d * 256]) * lo16(qv[i][d]), lo16(p[d * 256 + 128]) * hi16(qv[i][d]));
    __builtin_amdgcn_raw_buffer_store_b128(o, mkrs(out, 2 * NB_ACT), (idx * 128 + row) * 2304 + 1152 + grp * 128 + c * 16, 0, OUT_AUX);
  }
  STAMP(8);
}

__global__ __launch_bounds__(512, 2) void fused(const u16* x, const u16* wqkv, const u16* wo, const u16* q, const u16* k,
                                                const u16* v, const u16* shk, const u16* hk, const u16* dv, u16* out,
                                                char* ws) {
  extern __shared__ __attribute__((aligned(16))) char sm[];
  const int tid = threadIdx.x, lane = tid & 63, w = __builtin_amdgcn_readfirstlane(tid >> 6);
  const int bid = blockIdx.x, xcd = bid & 7, j = bid >> 3;
  const u64 tag = __hip_atomic_load((GAS u64*)ws, RLX_AGENT);
  const u4 rwo = mkdesc(wo, NB_WO);
  if (j < 14) role_a(sm, ws, x, wqkv, rwo, tag, xcd, j, w, lane);
  else role_b(sm, ws, q, k, v, shk, hk, dv, out, rwo, tag, xcd, j, w, lane);

  const int rt = xcd * 4 + (j >> 3), ct = j & 7;
  if (w == 0) {
    if (bid == 0) {
#pragma unroll 1
      for (int i = 0; i < 4; ++i) wait_flags(ws, i * 64 + lane, tag);
      if (lane == 0) __hip_atomic_store((GAS u64*)ws, mktag(tag), RLX_AGENT);
    } else {
      int ra = rt * 64 + (lane < 8 ? 0 : 63);
      int ti = ra < 14 ? 0 : (ra - 14) / 146;
      wait_flags(ws, lane < 16 ? F_O + ti * 8 + (lane & 7) : -1, tag);
    }
  }
  WAITV(0);
  bar();
  STAMP(9);
  const int r = lane & 15, g = lane >> 4, m = w & 3, kh = w >> 2;
  rsrc_t ro = mkrs(ws + WS_O, NB_ACT);
  u4 of[9];
#pragma unroll
  for (int i = 0; i < 9; ++i)
    of[i] = __builtin_amdgcn_raw_buffer_load_b128(ro, (rt * 72 + kh * 36 + g) * 1024 + (m * 16 + r) * 16, i * 4096, SC1);
  __builtin_amdgcn_sched_barrier(0);
  const char* wa = sm + L_WO + (kh * 288 + ((g >> 1) << 4) + ((g & 1) << 3) + ((r >> 2) << 1)) * 144 + (lane & 3) * 8;
  f4 acc[5];
#pragma unroll
  for (int n = 0; n < 5; ++n) acc[n] = f4{0.f, 0.f, 0.f, 0.f};
#pragma unroll
  for (int i = 0; i < 9; ++i)
#pragma unroll
    for (int n = 0; n < 5; ++n)
      acc[n] = mfma(cat8(trread(wa + i * 32 * 144 + n * 32), trread(wa + i * 32 * 144 + 144 + n * 32)),
                    __builtin_bit_cast(bf8, of[i]), acc[n]);
  STAMP(10);
  char* red = sm + L_RED;
  if (kh) {
#pragma unroll
    for (int n = 0; n < 5; ++n) *(LAS f4*)(red + ((m * 5 + n) * 64 + lane) * 16) = acc[n];
  }
  bar();
  char* ot = red + 20480;
  if (!kh) {
#pragma unroll
    for (int n = 0; n < 5; ++n) {
      f4 t = acc[n] + *(const LAS f4*)(red + ((m * 5 + n) * 64 + lane) * 16);
      if (n < 4 || g < 2) *(LAS u2*)(ot + (m * 16 + r) * 144 + n * 32 + g * 8) = u2{pk(t[0], t[1]), pk(t[2], t[3])};
    }
  }
  bar();
#pragma unroll
  for (int i = 0; i < 2; ++i) {
    int it = tid + 512 * i, row = it / 9, c = it - row * 9;
    if (it < 576) __builtin_amdgcn_raw_buffer_store_b128(*(const LAS u4*)(ot + it * 16), mkrs(out, 2 * NB_ACT), (rt * 64 + row) * 2304 + ct * 144 + c * 16, 0, OUT_AUX);
  }
  STAMP(11); CSTAMP(1);
}

extern "C" void kernel_launch(void* const* d_in, const int* in_sizes, int n_in, void* d_out, int out_size, void* d_ws,
                              size_t ws_size, hipStream_t stream) {
  (void)hipFuncSetAttribute((const void*)fused, hipFuncAttributeMaxDynamicSharedMemorySize, LDS_BYTES);
  fused<<<256, 512, LDS_BYTES, stream>>>((const u16*)d_in[0], (const u16*)d_in[1], (const u16*)d_in[2], (const u16*)d_in[3],
                                         (const u16*)d_in[4], (const u16*)d_in[5], (const u16*)d_in[6], (const u16*)d_in[7],
                                         (const u16*)d_in[8], (u16*)d_out, (char*)d_ws);
}
```

```cpp
#include <hip/hip_runtime.h>

typedef unsigned short u16;
typedef unsigned long long u64;
typedef short s4 __attribute__((ext_vector_type(4)));
typedef __bf16 bf2 __attribute__((ext_vector_type(2)));
typedef __bf16 bf8 __attribute__((ext_vector_type(8)));
typedef float f2 __attribute__((ext_vector_type(2)));
typedef float f4 __attribute__((ext_vector_type(4)));
typedef unsigned u4 __attribute__((ext_vector_type(4)));
typedef unsigned u2 __attribute__((ext_vector_type(2)));
typedef __amdgpu_buffer_rsrc_t rsrc_t;
#define LAS __attribute__((address_space(3)))
#define GAS __attribute__((address_space(1)))
typedef LAS char* lptr;
#define RLX_AGENT __ATOMIC_RELAXED, __HIP_MEMORY_SCOPE_AGENT
#define DEV __device__ __forceinline__

constexpr int NB_ACT = 2048 * 576 * 2;
constexpr int NB_W = 576 * 1728 * 2;
constexpr int NB_WO = 576 * 576 * 2;
constexpr unsigned OOB = 0x80000000u;
constexpr unsigned NOTYET = 0xffffffffu;
constexpr int SC1 = 16;
#define ROTK 2
#define PFS 12
#define TDELAY 0
#define YSH 0
#define XSH 1
#define OSH 2
#define PSLEEP 4
constexpr int SPIN_MAX = 1 << 15;

constexpr int WS_FLAG = 4096;
constexpr int F_A = 0, F_P = 128, F_Y = 256, F_Q = 512, F_W = 640, F_G = 896;
constexpr int WS_U = 1 << 20;
constexpr int WS_Y = WS_U + (3 << 20);
constexpr int WS_O = WS_Y + (3 << 20);
constexpr int WS_WO = WS_O + (3 << 20);
constexpr int WS_DBG = 16 << 20;

constexpr int LDS_BYTES = 163840;
constexpr int XB = 144 * 128;
constexpr int SLOT = XB + 64 * 432;
constexpr int PS = 1160, HS = 32640;
constexpr int L_WO = 65152;
constexpr int L_OT = L_WO + 83200;
constexpr int H_CP = 4160, U_OFF = 8448, CH_SZ = 13024;
constexpr int L_TAG = L_OT + 10240;
constexpr int L_CNT = L_TAG + 16;
constexpr int L_YA = L_TAG + 128;
constexpr int L_PF = LDS_BYTES - 256;

#define STAMPW(i, wv) do {} while (0)
#define STAMP(i) STAMPW(i, 0)
#define STAMPL(i) STAMPW(i, 4)

DEV rsrc_t mkrs(const void* p, int bytes) { return __builtin_amdgcn_make_buffer_rsrc((void*)p, 0, bytes, 0x00020000); }
DEV u4 mkdesc(const void* p, int bytes) {
  u64 a = (u64)p;
  return u4{(unsigned)a, (unsigned)(a >> 32) & 0xffffu, (unsigned)bytes, 0x00020000u};
}
DEV void dma16(u4 r, unsigned lds, unsigned voff, unsigned soff) {
  asm volatile("s_mov_b32 m0, %0\n\ts_nop 0\n\tbuffer_load_dwordx4 %1, %2, %3 offen lds"
               :: "s"(lds), "v"(voff), "s"(r), "s"(soff) : "memory");
}
DEV void dma16c(u4 r, unsigned lds, unsigned voff, unsigned soff) {
  asm volatile("s_mov_b32 m0, %0\n\ts_nop 0\n\tbuffer_load_dwordx4 %1, %2, %3 offen sc1 lds"
               :: "s"(lds), "v"(voff), "s"(r), "s"(soff) : "memory");
}
DEV void dma16m(u4 r, unsigned lds, unsigned voff, unsigned soff, u64 mask) {
  asm volatile("s_mov_b32 m0, %0\n\ts_mov_b64 exec, %4\n\tbuffer_load_dwordx4 %1, %2, %3 offen lds\n\ts_mov_b64 exec, -1"
               :: "s"(lds), "v"(voff), "s"(r), "s"(soff), "s"(mask) : "memory");
}
template <int AUX> DEV void st16(u4 v, rsrc_t r, unsigned off) { __builtin_amdgcn_raw_buffer_store_b128(v, r, off, 0, AUX); }
template <int AUX> DEV u4 ld16(rsrc_t r, unsigned off, unsigned soff) { return __builtin_amdgcn_raw_buffer_load_b128(r, off, soff, AUX); }
DEV s4 trread(lptr p) { return __builtin_amdgcn_ds_read_tr16_b64_v4i16((LAS s4*)p); }
DEV bf8 cat8(s4 a, s4 b) { return __builtin_bit_cast(bf8, __builtin_shufflevector(a, b, 0, 1, 2, 3, 4, 5, 6, 7)); }
DEV bf8 asb(u4 v) { return __builtin_bit_cast(bf8, v); }
DEV f4 mfma(bf8 a, bf8 b, f4 c) { return __builtin_amdgcn_mfma_f32_16x16x32_bf16(a, b, c, 0, 0, 0); }
DEV unsigned pk(float lo, float hi) { f2 v = {lo, hi}; return __builtin_bit_cast(unsigned, __builtin_convertvector(v, bf2)); }
DEV float asf(unsigned u) { return __builtin_bit_cast(float, u); }
DEV unsigned asu(float f) { return __builtin_bit_cast(unsigned, f); }
DEV float lo16(unsigned x) { return asf(x << 16); }
DEV float hi16(unsigned x) { return asf(x & 0xffff0000u); }
DEV float dot2(unsigned a, unsigned b, float c) {
  return __builtin_amdgcn_fdot2_f32_bf16(__builtin_bit_cast(bf2, a), __builtin_bit_cast(bf2, b), c, false);
}
DEV unsigned alignb(unsigned hi, unsigned lo) { return __builtin_amdgcn_alignbit(hi, lo, 16); }
DEV unsigned umax(unsigned a, unsigned b) { return a > b ? a : b; }
DEV unsigned max4(u4 v, unsigned m) { return umax(umax(umax(v[0], v[1]), m), umax(v[2], v[3])); }
DEV float fmax2(float a, float b) { return __builtin_elementwise_maximum(a, b); }
#define WAITV(n) asm volatile("s_waitcnt vmcnt(" #n ")" ::: "memory")
#define WAITL0() asm volatile("s_waitcnt lgkmcnt(0)" ::: "memory")
#define SB() __builtin_amdgcn_sched_barrier(0)
#define SGB(m, n) __builtin_amdgcn_sched_group_barrier(m, n, 0)
DEV void bar() { WAITL0(); __builtin_amdgcn_s_barrier(); asm volatile("" ::: "memory"); }
template <class T> DEV void pin(T& x) { asm volatile("" : "+v"(x)); }

DEV u64 get_tag(char* ws) {
  u64 e;
  asm volatile("s_load_dwordx2 %0, %1, 0x0\n\ts_waitcnt lgkmcnt(0)" : "=s"(e) : "s"(ws) : "memory");
  return e * 6364136223846793005ull + 1442695040888963407ull;
}
DEV void post_flag(char* ws, int idx, u64 tag) { __hip_atomic_store((GAS u64*)(ws + WS_FLAG + idx * 128), tag, RLX_AGENT); }
DEV void wait_flags(char* ws, int idx, u64 tag) {
  GAS u64* p = (GAS u64*)(ws + WS_FLAG + idx * 128);
#pragma unroll 1
  for (int spin = 0; spin < SPIN_MAX; ++spin) {
    if (__all(__hip_atomic_load(p, RLX_AGENT) == tag)) break;
    __builtin_amdgcn_s_sleep(PSLEEP);
  }
  __builtin_amdgcn_fence(__ATOMIC_ACQUIRE, "wavefront");
}

DEV void arrive(unsigned word) {
  unsigned one = 1;
  asm volatile("s_waitcnt lgkmcnt(0)\n\ts_mov_b64 exec, 1\n\tds_add_u32 %0, %1\n\ts_mov_b64 exec, -1"
               :: "v"(word), "v"(one) : "memory");
}
DEV void reach(unsigned word, unsigned n) {
#pragma unroll 1
  for (int spin = 0; spin < (1 << 22); ++spin) {
    unsigned v;
    asm volatile("ds_read_b32 %0, %1\n\ts_waitcnt lgkmcnt(0)" : "=v"(v) : "v"(word) : "memory");
    if ((unsigned)__builtin_amdgcn_readfirstlane(v) >= n) break;
    __builtin_amdgcn_s_sleep(1);
  }
}

DEV u4 ld2(lptr p) {
  const u2 a = *(const LAS u2*)p, b = *(const LAS u2*)(p + HS);
  return u4{a[0], a[1], b[0], b[1]};
}
DEV void att_consts(lptr sm, int lane, u4 (&tc)[9], int first) {
  const LAS u4* tb = (const LAS u4*)(sm + L_OT) + lane;
#pragma unroll
  for (int i = 0; i < 9; ++i) tc[i] = tb[(i < 4 ? i : i == 4 ? 4 + first : i + 1) * 64];
}

template <int HI>
DEV void g1_compute(lptr sm, int n0, int lane, u4 (&tc)[9], int first) {
  const int r = lane & 15, g = lane >> 4;
  lptr xa0 = sm + r * 128 + ((g ^ ((r >> 1) & 7)) << 4);
  lptr xa1 = (lptr)((size_t)xa0 ^ 64);
  lptr wa = sm + XB + (((g >> 1) << 4) + ((g & 1) << 3) + ((r >> 2) << 1)) * 432 + (lane & 3) * 8 + n0 * 32;
#define USED(m, n) (HI ? ((n) > 0 || (m) >= 5) : ((n) < 3 || (m) < 5))
  f4 acc[9][4];
#pragma unroll
  for (int m = 0; m < 9; ++m)
#pragma unroll
    for (int n = 0; n < 4; ++n) acc[m][n] = f4{0.f, 0.f, 0.f, 0.f};
  bf8 xf[9], wf[2][4];
  auto mm = [&](bf8 (&wv)[4], int m) {
#pragma unroll
    for (int n = 0; n < 4; ++n)
      if (USED(m, n)) acc[m][n] = mfma(wv[n], xf[m], acc[m][n]);
  };
  auto rdx = [&](lptr xp, int m) { xf[m] = *(const LAS bf8*)(xp + m * 2048); };
  auto rdw = [&](lptr wp, bf8 (&wv)[4], int n) { wv[n] = cat8(trread(wp + n * 32), trread(wp + 432 + n * 32)); };
  auto mix = [&]() {
#pragma unroll
    for (int i = 0; i < 4; ++i) { SGB(0x8, 1); SGB(0x100, 1); }
    SB();
  };
  auto step = [&](bf8 (&wc)[4], bf8 (&wn)[4], lptr xp, lptr wp, int m0) {
#pragma unroll
    for (int m = m0; m < 9; ++m) {
      mm(wc, m);
      if (m - m0 < 4) rdw(wp, wn, m - m0);
      if (m0 == 0) { if (m > 0) rdx(xp, m - 1); }
      else if (m - m0 < 4) rdx(xp, m - m0);
      else { rdx(xp, 2 * (m - m0) - 4); if (2 * (m - m0) - 3 < m) rdx(xp, 2 * (m - m0) - 3); }
      mix();
    }
#pragma unroll
    for (int m = (m0 == 0 ? 8 : 2 * (9 - m0) - 4); m < 9; ++m) rdx(xp, m);
    SB();
  };
  constexpr int MS = 3;
  bar();
#pragma unroll
  for (int n = 0; n < 4; ++n) rdw(wa, wf[0], n);
#pragma unroll
  for (int m = 0; m < 9; ++m) rdx(xa0, m);
  SB();
  int so = 0;
#pragma unroll 1
  for (int k = 0; k < 8; ++k) {
    step(wf[0], wf[1], xa1 + so, wa + so + 32 * 432, 0);
    so = so == 2 * SLOT ? 0 : so + SLOT;
#pragma unroll
    for (int m = 0; m < MS; ++m) mm(wf[1], m);
    SB();
    bar();
    step(wf[1], wf[0], xa0 + so, wa + so, MS);
  }
  xa1 += 2 * SLOT; wa += 2 * SLOT + 32 * 432;
  pin(xa1); pin(wa);
  STAMP(1);
  lptr d = sm + (2 * n0 + (g >> 1)) * PS + r * 8 + (g & 1) * HS;
  bf8 xg[9];
#pragma unroll
  for (int m = 0; m < 11; ++m) {
    if (m < 9) mm(wf[0], m);
    if (m < 2) { rdw(wa, wf[1], 2 * m); rdw(wa, wf[1], 2 * m + 1); }
    if (m < 3) {
#pragma unroll
      for (int i = 3 * m; i < 3 * m + 3; ++i) xg[i] = *(const LAS bf8*)(xa1 + i * 2048);
    }
    if (m == 4) arrive(L_CNT + 12);
    if (m >= 2) {
#pragma unroll
      for (int n = 0; n < 4; ++n)
        if (USED(m - 2, n)) acc[m - 2][n] = mfma(wf[1][n], xg[m - 2], acc[m - 2][n]);
    }
    if (m == 7) att_consts(sm, lane, tc, first);
    if (m == 8) arrive(L_CNT + 8);
#pragma unroll
    for (int o = m - 3; o < (m == 10 ? 9 : m - 2); ++o) {
      if (o >= 0) {
#pragma unroll
        for (int n = 0; n < 4; ++n)
          if (USED(o, n))
            *(LAS u2*)(d + n * 2 * PS + o * 128) = u2{pk(acc[o][n][0], acc[o][n][1]), pk(acc[o][n][2], acc[o][n][3])};
      }
    }
    if (m < 3) {
#pragma unroll
      for (int i = 0; i < 4; ++i) { SGB(0x8, 1); SGB(0x100, 2); }
    } else {
      SGB(0x8, 1); SGB(0x100, 1);
#pragma unroll
      for (int i = 0; i < 4; ++i) { SGB(0x8, 1); SGB(0x2, 2); SGB(0x8, 1); SGB(0x200, 1); }
    }
    SB();
  }
}

DEV void role_a(lptr sm, char* ws, const u16* x, const u16* wqkv, int bid, int w, int lane) {
  const int xcd = bid & 7, j = bid >> 3;
  const int rt = (xcd >> 1) * 4 + (j >> 2), hq = (xcd & 1) * 4 + (j & 3);
  const u4 rp = mkdesc(ws + WS_WO + (j & 7) * 82944, 82944);
  u64 tag;
  u4 tc[9];
  const int aw = w ^ 4;
  const int first = rt + (aw >> 1) == 0;
  if (__builtin_expect(w >= 4, 1)) {
    const int lw = w - 4;
    const u4 rx = mkdesc(x, NB_ACT), rw = mkdesc(wqkv, NB_W);
    const int t27 = lane >= 27, cc = lane - 27 * t27, part = (cc >= 9) + (cc >= 18);
    const unsigned vw = t27 * (4 * 3456) + part * (1152 - 144) + cc * 16 + hq * 144;
    auto wpiece = [&](unsigned d, int kt, int i) {
      dma16m(rw, d + XB + (lw * 8 + i) * 864, vw, (kt * 64 + lw * 16 + 8 * (i >> 2) + (i & 3)) * 3456, (1ull << 54) - 1);
    };
    auto tailp = [&](unsigned d, int kt) {
#pragma unroll
      for (int i = 2; i < 8; ++i) wpiece(d, kt, i);
    };
    STAMPL(16);
    tailp(0, 0);
    SB();
    unsigned vx[6];
#pragma unroll
    for (int i = 0; i < 6; ++i) {
      const int xr = lw * 36 + i * 6 + (lane >> 3);
      vx[i] = __umul24(rt * 128 + xr, 1152) + (((lane & 7) ^ ((xr >> 1) & 7)) << 4) - 16 * 1152;
    }
    auto headp = [&](unsigned d, int kt) {
#pragma unroll
      for (int i = 0; i < 6; ++i) dma16m(rx, d + (lw * 6 + i) * 768, vx[i], kt * 128, (1ull << 48) - 1);
#pragma unroll
      for (int i = 0; i < 2; ++i) wpiece(d, kt, i);
    };
    headp(0, 0);
    unsigned d = SLOT;
#pragma unroll 1
    for (int k = 1; k < 9; ++k) {
      headp(d, k);
      WAITV(8);
      bar();
      STAMPL(16 + k);
      tailp(d, k);
      d = d == 2 * SLOT ? 0 : d + SLOT;
    }
    STAMPL(26);
    tag = *(LAS u64*)(sm + L_TAG);
    att_consts(sm, lane, tc, first);
    if (lw == 3) wait_flags(ws, F_W + (j & 7) * 32 + (lane < 18 ? lane : 0), tag);
    WAITV(0);
    bar();
    STAMPL(27);
#pragma unroll 1
    for (int i = lw - 8; i < 73; i += 4) {
      if (i == lw + 24) reach(L_CNT + 12, 4);
      const int ii = i < 0 ? i + 81 : i;
      dma16c(rp, L_WO + ii * 1024, lane * 16, ii * 1024);
    }
    if (lw == 0) {
      wait_flags(ws, F_Q + rt * 8 + (lane & 7), tag);
      arrive(L_CNT + 4);
    }
    STAMPL(28);
  } else {
    tag = get_tag(ws);
    if (threadIdx.x == 0) { *(LAS u64*)(sm + L_TAG) = tag; *(LAS u4*)(sm + L_CNT) = u4{0, 0, 0, 0}; *(LAS u4*)(sm + L_CNT + 16) = u4{0, 0, 0, 0}; *(LAS u4*)(sm + L_CNT + 32) = u4{0, 0, 0, 0}; }
    {
      const int r = lane & 15, g = lane >> 4, q4 = r >> 2, p4 = lane & 3;
      LAS u4* tb = (LAS u4*)(sm + L_OT) + lane;
      u4 t0, t1;
      if (w < 2) {
#pragma unroll
        for (int dd = 0; dd < 4; ++dd) {
          t0[dd] = __builtin_amdgcn_sbfe((int)(0x1ffu << (2 * w)), 4 * g + dd, 1);
          t1[dd] = __builtin_amdgcn_sbfe((int)(0x3feu << (2 * w)), 4 * g + dd, 1);
        }
      } else if (w == 2) {
#pragma unroll
        for (int dd = 0; dd < 4; ++dd) {
          const int kk = 4 * g + dd - r;
          t0[dd] = (kk >= 1 && kk <= 8) ? 0u : 0xff800000u;
          t1[dd] = (kk >= 1 && kk <= 8 && g >= 2) ? 0u : 0xff800000u;
        }
      } else {
#pragma unroll
        for (int dd = 0; dd < 4; ++dd) t0[dd] = 4 * g + dd - r <= -8 ? 0u : 0xff800000u;
        t1[0] = (t0[0] ? 0u : 0xffffu) | (t0[1] ? 0u : 0xffff0000u);
        t1[1] = (t0[2] ? 0u : 0xffffu) | (t0[3] ? 0u : 0xffff0000u);
        t1[2] = g * PS + r * 16;
        t1[3] = g * PS + (r & 7) * 16;
        const int kr0 = (4 * g + q4) * 16, kr1 = kr0 + (g >= 2 ? 128 : 256);
        const int ve = (p4 >> 1) * PS + (p4 & 1) * HS, vo = ((p4 + 1) >> 1) * PS + ((p4 + 1) & 1) * HS;
        tb[8 * 64] = u4{(unsigned)(kr0 + ve), (unsigned)(kr1 + ve), (unsigned)(kr0 + vo), (unsigned)(kr1 + vo)};
        const unsigned os = (rt * 144 + hq * 9 + g) * 1024 + r * 16;
        tb[9 * 64] = u4{(unsigned)kr0, (unsigned)kr1, os, g ? OOB : os + 8192};
      }
      tb[2 * w * 64] = t0;
      tb[(2 * w + 1) * 64] = t1;
    }
    if (w & 1) g1_compute<1>(sm, 3 + (w >> 1) * 7, lane, tc, first);
    else g1_compute<0>(sm, (w >> 1) * 7, lane, tc, first);
  }
  {
    const unsigned rb = (32 * (aw >> 1) + (aw & 1)) * 8;
    const u4 qm[4] = {tc[0], tc[1], tc[2], tc[3]}, u0 = tc[4], u1 = tc[5], c0 = tc[6], c1 = tc[7], c2 = tc[8];
    const f4 b0 = __builtin_bit_cast(f4, u0), b1 = __builtin_bit_cast(f4, u1);
    const unsigned m01 = c0[0], m23 = c0[1];
    lptr qa = sm + rb + c0[2], k1 = sm + rb + c0[3];
    lptr ve0 = sm + rb + c1[0], ve1 = sm + rb + c1[1], vo0 = sm + rb + c1[2], vo1 = sm + rb + c1[3];
    lptr vk0 = sm + rb + c2[0], vk1 = sm + rb + c2[1];
    const unsigned ow = (aw >> 2) * 73728 + (aw & 3) * 256;
    unsigned os0 = c2[2] + ow, os1 = os0 + 4096, os2 = c2[3] + ow;
    const bool g0 = lane < 16;
    rsrc_t ro = mkrs(ws + WS_O, NB_ACT);
    u4 ones = {0x3f803f80u, 0x3f803f80u, 0x3f803f80u, 0x3f803f80u};
    pin(qa); pin(k1); pin(ve0); pin(ve1); pin(vo0); pin(vo1); pin(vk0); pin(vk1); pin(os0); pin(os1); pin(os2); pin(ones);
    STAMP(14);
    if (w < 4) arrive(L_CNT);
    reach(w < 4 ? L_CNT : L_CNT + 8, 4);
    STAMP(2);
    STAMPL(29);
    const float cs = 0.23570226039551584f * 1.4426950408889634f;
    const f4 z = {0.f, 0.f, 0.f, 0.f};
    u4 qf[4];
    bf8 ka[4], kb[4], vt0[4], vt1[4];
#pragma unroll
    for (int h = 0; h < 4; ++h) {
      qf[h] = ld2(qa + 2 * h * PS + 128);
      ka[h] = asb(ld2(qa + (9 + 2 * h) * PS));
      kb[h] = asb(ld2(k1 + (9 + 2 * h) * PS + 256));
      SB();
    }
#pragma unroll
    for (int h = 0; h < 4; ++h) {
      vt0[h] = cat8(trread((h < 2 ? ve0 : vo0) + (18 + 2 * h) * PS), trread((h < 2 ? ve1 : vo1) + (18 + 2 * h) * PS));
      vt1[h] = cat8(trread(vk0 + (20 + 2 * h) * PS + (h < 2 ? 0 : HS)), trread(vk1 + (20 + 2 * h) * PS + (h < 2 ? 0 : HS)));
    }
    SB();
    f4 s0[4], s1[4], s[4];
    float mx[4];
#pragma unroll
    for (int h = 0; h < 4; ++h) {
      bf8 qq = asb(qf[h] & qm[h]);
      s0[h] = mfma(ka[h], qq, b0);
      s1[h] = mfma(kb[h], qq, b1);
      SB();
    }
#pragma unroll
    for (int h = 0; h < 4; ++h) {
#pragma unroll
      for (int dd = 0; dd < 4; ++dd) s[h][dd] = fmax2(s0[h][dd], s1[h][dd]);
      mx[h] = fmax2(fmax2(fmax2(s[h][0], s[h][1]), s[h][2]), s[h][3]);
      SB();
    }
    {
      auto a = __builtin_amdgcn_permlane16_swap(asu(mx[0]), asu(mx[1]), false, false);
      auto b = __builtin_amdgcn_permlane16_swap(asu(mx[2]), asu(mx[3]), false, false);
      auto c = __builtin_amdgcn_permlane32_swap(asu(fmax2(asf(a[0]), asf(a[1]))), asu(fmax2(asf(b[0]), asf(b[1]))), false, false);
      unsigned m = asu(-cs * fmax2(asf(c[0]), asf(c[1])));
      auto e = __builtin_amdgcn_permlane32_swap(m, m, false, false);
      unsigned e0 = e[0], e1 = e[1];
      auto f0 = __builtin_amdgcn_permlane16_swap(e0, e0, false, false);
      auto f1 = __builtin_amdgcn_permlane16_swap(e1, e1, false, false);
      mx[0] = asf(f0[0]); mx[1] = asf(f0[1]); mx[2] = asf(f1[0]); mx[3] = asf(f1[1]);
    }
    SB();
    f4 sum[4], o0[4], o1[4];
#pragma unroll
    for (int h = 0; h < 4; ++h) {
      float p[4];
#pragma unroll
      for (int dd = 0; dd < 4; ++dd) p[dd] = __builtin_amdgcn_exp2f(__builtin_fmaf(s[h][dd], cs, mx[h]));
      unsigned p01 = pk(p[0], p[1]), p23 = pk(p[2], p[3]);
      bf8 pf = asb(u4{p01 & ~m01, p23 & ~m23, p01 & m01, p23 & m23});
      sum[h] = mfma(asb(ones), pf, z);
      o0[h] = mfma(vt0[h], pf, z);
      o1[h] = mfma(vt1[h], pf, z);
      SB();
    }
    if (w >= 4) { WAITV(0); arrive(L_CNT + 20); }
    reach(L_CNT + 4, 1);
    u2 gr[4];
    u4 left;
#pragma unroll
    for (int h = 0; h < 4; ++h) {
      const float inv = __builtin_amdgcn_rcpf(sum[h][0]);
      const unsigned a0 = pk(o0[h][0] * inv, o0[h][1] * inv), a1 = pk(o0[h][2] * inv, o0[h][3] * inv);
      const unsigned e0 = pk(o1[h][0] * inv, o1[h][1] * inv);
      if (h & 1) {
        const unsigned e1 = pk(o1[h][2] * inv, o1[h][3] * inv);
        left[h] = a1;
        gr[h] = u2{g0 ? e0 : a0, g0 ? e1 : a1};
        st16<SC1>(u4{gr[h - 1][0], gr[h - 1][1], gr[h][0], gr[h][1]}, ro, h == 1 ? os0 : os1);
        SB();
      } else {
        left[h] = e0;
        gr[h] = u2{a0, a1};
      }
    }
    st16<SC1>(left, ro, os2);
  }
  if (threadIdx.x == 192) post_flag(ws, F_A + rt * 8 + hq, tag);
  STAMP(3);
  STAMPL(4);
}

DEV void role_b(lptr sm, char* ws, const u16* x, const u16* wqkv, const u16* wo, const u16* k, const u16* v,
                const u16* shk, const u16* hk, const u16* dv, int bid, int w, int lane) {
  const int tid = threadIdx.x, xcd = bid & 7, jb = (bid >> 3) - 16, me = xcd * 16 + jb;
  const int G = lane >> 4, i16 = lane & 15, w4 = w & 3;
  rsrc_t ru = mkrs(ws + WS_U, NB_ACT);
  const int xc = (xcd + XSH) & 7, cm = xc * 16 + jb;
  const int c5 = 512 + xcd * 8 + (jb & 7);
  const unsigned tb = L_WO + w * 9216;
  const u4 ny = {NOTYET, NOTYET, NOTYET, NOTYET};
  rsrc_t ry = mkrs(ws + WS_Y, NB_ACT);
  u64 tag;
  if (w < 4 || (w == 4 && jb < 8)) {
    const int grp = w < 4 ? xcd : 8, un = w < 4 ? jb * 4 + w : xcd * 8 + jb;
    const u4 rk = mkdesc(k, NB_ACT), rv = mkdesc(v, NB_ACT);
    const unsigned o0 = (un * 32 + (lane >> 3)) * 1152 + grp * 128 + (((lane & 7) ^ ((lane >> 4) & 1)) << 4);
    if (TDELAY) __builtin_amdgcn_s_sleep(TDELAY);
    dma16(rk, tb, o0 - 8 * 1152, 0);
#pragma unroll
    for (int i = 0; i < 4; ++i) {
      dma16(rk, tb + 1024 + i * 1024, o0 ^ (((i + 1) & 3) << 5), i * 8 * 1152);
      dma16(rv, tb + 5120 + i * 1024, o0 ^ (i << 5), i * 8 * 1152);
    }
    u2 wk[4];
#pragma unroll
    for (int s = 0; s < 4; ++s) wk[s] = *(const GAS u2*)((const char*)shk + (grp * 64 + G * 4 + (i16 & 3)) * 8 + s * 128);
    const unsigned off = ((grp * 16 + G) * 256 + 4 * un) * 64 + i16 * 16;
    tag = get_tag(ws);
    if (tid == 0) { *(LAS u64*)(sm + L_TAG) = tag; *(LAS u4*)(sm + L_CNT) = u4{0, 0, 0, 0}; *(LAS u4*)(sm + L_CNT + 16) = u4{0, 0, 0, 0}; *(LAS u4*)(sm + L_CNT + 32) = u4{0, 0, 0, 0}; }
    bar();
    const int q4 = i16 >> 2, p4 = lane & 3;
    const int f0 = (q4 >> 1) | (p4 << 1), f1 = (q4 >> 1) | (((p4 + 1) & 3) << 1);
    const unsigned a0 = tb + (8 * p4 + q4) * 128 + (((G >> 1) ^ f0) << 4) + (G & 1) * 8;
    const unsigned a1 = tb + (8 * p4 + q4) * 128 + (((G >> 1) ^ f1) << 4) + (G & 1) * 8;
    asm volatile("s_waitcnt vmcnt(0)" : "+v"(wk[0]), "+v"(wk[1]), "+v"(wk[2]), "+v"(wk[3]) :: "memory");
    arrive(L_CNT);
    STAMP(1);
    u2 K[4][3], V[4][2];
#pragma unroll
    for (int s = 0; s < 4; ++s) {
      lptr b0 = (lptr)(size_t)(a0 ^ (s << 5)), b1 = (lptr)(size_t)(a1 ^ (s << 5));
      K[s][0] = __builtin_bit_cast(u2, trread(b0 + 512));
      K[s][1] = __builtin_bit_cast(u2, trread(b1 + 1024));
      K[s][2] = __builtin_bit_cast(u2, trread(b1 + 1536));
      V[s][0] = __builtin_bit_cast(u2, trread(b0 + 5120));
      V[s][1] = __builtin_bit_cast(u2, trread(b0 + 5120 + 512));
      SB();
    }
#pragma unroll
    for (int s = 0; s < 4; ++s) {
      const unsigned wA = alignb(wk[s][1], wk[s][1]), wB = alignb(wk[s][0], wk[s][0]);
      unsigned kk[6] = {K[s][0][0], K[s][0][1], K[s][1][0], K[s][1][1], K[s][2][0], K[s][2][1]};
      unsigned vv[4] = {V[s][0][0], V[s][0][1], V[s][1][0], V[s][1][1]};
      unsigned sh[5];
#pragma unroll
      for (int d = 0; d < 5; ++d) sh[d] = alignb(kk[d + 1], kk[d]);
      u4 o;
#pragma unroll
      for (int d = 0; d < 4; ++d) {
        float ev = dot2(sh[d + 1], wB, dot2(sh[d], wA, 0.f)) * lo16(vv[d]);
        float od = dot2(kk[d + 2], wB, dot2(kk[d + 1], wA, 0.f)) * hi16(vv[d]);
        o[3 - d] = pk(od, ev);
      }
      st16<SC1>(o, ru, off + s * 65536);
    }
    STAMP(2);
    WAITV(0);
    if (w < 4) arrive(L_CNT + 36);
    else if (lane == 0) post_flag(ws, F_G + un, tag);
  } else if (w >= 6) {
    const u4 rpf = w == 6 ? mkdesc(x, NB_ACT) : mkdesc(wqkv, NB_W);
    const int kr = (lane * 4370) >> 16, cc = lane - kr * 15, part = (cc * 52) >> 8;
    const unsigned fw = lane < 60 ? (jb * 4 + kr) * 3456 + part * (1152 - 640) + ((xcd & 1) * 4 + cc) * 128 : OOB;
    const unsigned fx = lane < 33 ? ((xcd >> 1) * 512 - 16 + jb * 33 + lane) * 1152 : OOB;
    const unsigned fo = w == 6 ? fx : fw, fs = w == 6 ? 128 : 64 * 3456;
    auto touch = [&](int st) {
      asm volatile("s_mov_b32 m0, %0\n\ts_nop 0\n\tbuffer_load_dword %1, %2, %3 offen lds"
                   :: "s"(L_PF), "v"(fo), "s"(rpf), "s"(st * fs) : "memory");
    };
    tag = get_tag(ws);
    bar();
    touch(1); touch(2);
    if (lane < 34) {
#pragma unroll
      for (int i = 0; i < 3; ++i)
        *(LAS u4*)(sm + ((w & 1) * 2 + i) * CH_SZ + (lane < 30 ? U_OFF + lane * 16 : ((lane >> 1) & 1) * H_CP + (lane & 1) * 16)) = u4{0, 0, 0, 0};
    }
    __builtin_amdgcn_s_sleep(PFS);
    touch(3);
    reach(L_CNT, 4);
    rsrc_t rh = mkrs(hk, NB_ACT);
    u4 ha[10];
    unsigned hp[10];
    const unsigned ho = (tid & 127) * 16;
#pragma unroll
    for (int i = 0; i < 10; ++i) {
      const unsigned on = (i >= 8 && jb < 8) ? OOB : 0;
      const unsigned so = (i < 8 ? cm * 4 + (i >> 1) : c5) * 4096;
      ha[i] = ld16<0>(rh, (ho | on) + (i & 1) * 2048, so);
      hp[i] = __builtin_amdgcn_raw_buffer_load_b32(rh, ((i & 1) ? ho + 2044 : ho ? ho - 4 : OOB) | on, so, 0);
    }
    SB();
    touch(4);
#pragma unroll
    for (int i = 0; i < 10; ++i) {
      lptr hb = sm + (i >> 1) * CH_SZ + 32 + ho + (i & 1) * 2048;
      *(LAS u4*)(hb + H_CP) = ha[i];
      *(LAS u4*)hb =
          u4{alignb(ha[i][0], hp[i]), alignb(ha[i][1], ha[i][0]), alignb(ha[i][2], ha[i][1]), alignb(ha[i][3], ha[i][2])};
    }
    arrive(L_CNT + 8);
    STAMPW(18, 6);
    touch(5);
    __builtin_amdgcn_s_sleep(PFS);
    touch(6);
    reach(L_CNT + 4, 1);
    __builtin_amdgcn_s_setprio(2);
    touch(7);
    rsrc_t ro = mkrs(ws + WS_O, NB_ACT);
#pragma unroll 1
    for (int i = 0; i < 9; ++i) st16<SC1>(ny, ro, (me * 18 + (w & 1) * 9 + i) * 1024 + lane * 16);
    touch(8);
    WAITV(0);
    arrive(L_CNT + 12);
    if (w == 7) {
      reach(L_CNT + 12, 2);
      if (lane == 0) post_flag(ws, F_Q + me, tag);
      STAMPW(22, 7);
    }
  } else {
    const int slot = w == 5 ? jb : 2 + jb;
    tag = get_tag(ws);
    bar();
    reach(L_CNT, 3);
    if (w == 5 || jb >= 14) {
      const u4 rwo = mkdesc(wo, NB_WO);
      const int e4 = (lane * 57) >> 9;
      const unsigned vb = (e4 >> 1) * 1152 + xcd * 144 + (lane - e4 * 9) * 16, vt = (e4 & 1) * 1152;
      int ks = slot + ROTK * xcd;
      if (ks >= 18) ks -= 18;
#pragma unroll 1
      for (int i = 0; i < 8; ++i) {
        const int ci = 4 * ks + (i >> 1), h = (ci * 57) >> 9, c = ci - 9 * h;
        const int row = h * 72 + (c == 8 ? 16 : 36 * (c >> 2) + 4 * (c & 3)) + 2 * (i & 1);
        dma16m(rwo, tb + i * 576, vb + __umul24(vt, c == 8 ? 36 : (c & 3) ? 16 : 32), row * 1152, (1ull << 36) - 1);
      }
      WAITV(0);
      rsrc_t rpw = mkrs(ws + WS_WO, NB_WO);
      lptr p = (lptr)(size_t)tb + (G * 8 + ((i16 >> 2) << 1)) * 144 + (lane & 3) * 8;
      const unsigned po = (xcd * 18 + slot) * 4608;
      u4 f[5];
#pragma unroll
      for (int n = 0; n < 5; ++n) f[n] = __builtin_bit_cast(u4, cat8(trread(p + n * 32), trread(p + 144 + n * 32)));
#pragma unroll
      for (int n = 0; n < 4; ++n) st16<SC1>(f[n], rpw, po + n * 1024 + lane * 16);
      st16<SC1>(f[4], rpw, i16 < 8 ? po + 4096 + (G * 8 + i16) * 16 : OOB);
      WAITV(0);
      if (lane == 0) post_flag(ws, F_W + xcd * 32 + slot, tag);
      STAMPW(21, 5);
    }
  }
  const int r = lane & 15, g = lane >> 4;
  if (w >= 4) {
    if (w < 6) {
      const int ct = (bid >> 3) & 7;
      __builtin_amdgcn_s_setprio(2);
      if (w == 5) {
        __builtin_amdgcn_s_sleep(8);
        wait_flags(ws, F_W + ct * 32 + (lane < 18 ? lane : 0), tag);
      } else if (jb >= 8) {
        reach(L_CNT + 4, 1);
        wait_flags(ws, F_G + lane, tag);
        u4 u5[4];
#pragma unroll
        for (int i = 0; i < 4; ++i) u5[i] = ld16<SC1>(ru, lane * 64 + (c5 & 3) * 16, (c5 >> 2) * 16384 + i * 4096);
#pragma unroll
        for (int i = 0; i < 4; ++i) *(LAS u4*)(sm + U_OFF + 480 + 4 * CH_SZ + lane * 16 + i * 1024) = u5[i];
      }
      arrive(L_CNT + 32);
      reach(L_CNT + 32, 2);
      reach(L_CNT + 8, 6);
      STAMPL(16);
      const u4 rp = mkdesc(ws + WS_WO + ct * 82944, 82944);
#pragma unroll 1
      for (int i = (w & 1) * 1024; i < 81 * 1024; i += 2048) dma16c(rp, L_WO + i, lane * 16, i);
      WAITV(0);
      STAMPL(17);
    }
    arrive(L_CNT + 20);
    __builtin_amdgcn_s_setprio(0);
    return;
  }
  typedef const __attribute__((address_space(4))) unsigned* cptr;
  const unsigned dv0 = ((cptr)dv)[cm * 2 + (w >> 1)], dv1 = ((cptr)dv)[c5 >> 1];
  if (w == 3) {
    reach(L_CNT + 36, 4);
    if (lane == 0) post_flag(ws, F_P + me, tag);
    STAMPW(20, 3);
  }
  STAMP(3);
  if (w == 0) {
    __builtin_amdgcn_s_sleep(4);
    wait_flags(ws, F_P + xc * 16 + i16, tag);
    arrive(L_CNT + 4);
  }
  reach(L_CNT + 4, 1);
  STAMP(4);
  {
    u4 uv[4];
#pragma unroll
    for (int i = 0; i < 4; ++i) uv[i] = ld16<SC1>(ru, tid * 16, cm * 16384 + i * 4096);
#pragma unroll
    for (int i = 0; i < 4; ++i) *(LAS u4*)(sm + U_OFF + 480 + (tid & 3) * CH_SZ + (tid >> 2) * 16 + i * 1024) = uv[i];
  }
  arrive(L_CNT + 8);
  const int xd = 15 - r - 8 * g;
#pragma unroll 1
  for (int rnd = 0; rnd < (jb >= 8 ? 2 : 1); ++rnd) {
    const int dd0 = rnd ? 2 * w : 0, ch = rnd ? c5 : cm * 4 + w;
    const unsigned dval = dd0 == 0 ? ((rnd ? dv1 : dv0) >> ((ch & 1) * 16)) & 0xffffu : 0;
    lptr cb = sm + (rnd ? 4 : w) * CH_SZ;
    lptr aa[4];
#pragma unroll
    for (int i = 0; i < 4; ++i) { aa[i] = cb + (r & 1) * H_CP + 4 + 4 * (4 * g + (r >> 1)) + dd0 * 64 + i * 1024; pin(aa[i]); }
    lptr ba = cb + U_OFF + 16 * (31 + 2 * r - g) - 128 - dd0 * 64;
    pin(ba);
    u4 ad;
#pragma unroll
    for (int d = 0; d < 4; ++d) ad[d] = (xd >> 1) == d ? dval << ((xd & 1) * 16) : 0u;
    pin(ad);
    reach(rnd ? L_CNT + 32 : L_CNT + 8, rnd ? 2 : 6);
    STAMP(rnd ? 19 : 5);
    f4 acc[8];
    bf8 A[2][8], B[2][8];
    auto rd = [&](int dd, bf8 (&a)[8], bf8 (&b)[8]) {
#pragma unroll
      for (int i = 0; i < 8; ++i) {
        const LAS unsigned* p = (const LAS unsigned*)(aa[i >> 1] + dd * 64 + (i & 1) * 512);
        a[i] = asb(u4{p[0], p[1], p[2], p[3]});
        b[i] = *(const LAS bf8*)(ba + 128 - dd * 64 + i * 512);
      }
    };
    auto mm = [&](bf8 (&a)[8], bf8 (&b)[8]) {
#pragma unroll
      for (int i = 0; i < 8; ++i)
#pragma unroll
        for (int jj = 0; jj + i < 8; ++jj) acc[i + jj] = mfma(a[i], b[jj], acc[i + jj]);
#pragma unroll
      for (int i = 0; i < 24; ++i) { SGB(0x8, 1); SGB(0x100, 1); }
      SGB(0x8, 12);
      SB();
    };
    rd(0, A[0], B[0]);
#pragma unroll
    for (int n = 0; n < 8; ++n) acc[n] = mfma(asb(ad), B[0][n], f4{0.f, 0.f, 0.f, 0.f});
    SB();
#pragma unroll 1
    for (int it = rnd * 3; it < 4; ++it) {
      rd(1, A[1], B[1]);
      mm(A[0], B[0]);
      rd(2, A[0], B[0]);
      mm(A[1], B[1]);
#pragma unroll
      for (int i = 0; i < 4; ++i) aa[i] += 128;
      ba -= 128;
    }
    STAMP(6 + rnd);
    const unsigned yo = (((ch >> 6) * 16 + 2 * (g & 1) + (r >> 3)) * 4 + ((r >> 2) & 1) + 2 * (g >> 1)) * 4096 + (ch & 63) * 64 + (r & 3) * 16;
    auto put = [&](f4 x, f4 y, int a) {
      auto s0 = __builtin_amdgcn_permlane16_swap(pk(x[0], x[1]), pk(y[0], y[1]), false, false);
      auto s1 = __builtin_amdgcn_permlane16_swap(pk(x[2], x[3]), pk(y[2], y[3]), false, false);
      st16<SC1>(u4{s0[0], s1[0], s0[1], s1[1]}, ry, yo + a * 65536);
    };
    if (rnd == 0) {
#pragma unroll
      for (int a = 0; a < 4; ++a) put(acc[2 * a], acc[2 * a + 1], a);
      if (jb >= 8) continue;
    } else {
      LAS f4* mine = (LAS f4*)(sm + w * CH_SZ + lane * 16);
#pragma unroll
      for (int n = 0; n < 8; ++n) mine[n * 64] = acc[n];
    }
    WAITV(0);
    arrive(L_CNT + 16);
    if (rnd || w == 1) reach(L_CNT + 16, 4);
    if (tid == 64) post_flag(ws, F_Y + cm, tag);
    if (rnd) {
      constexpr int CS = CH_SZ / 16;
      LAS f4* p = (LAS f4*)(sm + lane * 16) + w * 128;
      put((p[0] + p[CS]) + (p[2 * CS] + p[3 * CS]), (p[64] + p[CS + 64]) + (p[2 * CS + 64] + p[3 * CS + 64]), w);
      WAITV(0);
      arrive(L_CNT + 16);
      if (w == 1) {
        reach(L_CNT + 16, 8);
        if (lane == 0) post_flag(ws, F_Y + 128 + (c5 & 63), tag);
      }
    }
  }
  STAMP(8);
}

DEV void finish(lptr sm, lptr yt, char* ws, const u16* q, u16* out, int grp, int idx, int w4, int lane) {
  rsrc_t ry = mkrs(ws + WS_Y, NB_ACT), rq = mkrs(q, NB_ACT), rout = mkrs(out, 2 * NB_ACT);
  const int orow = idx * 128 + 64 * (w4 & 1) + 8 * (w4 >> 1) + (lane >> 3);
  u4 qv[4], yv[4];
#pragma unroll
  for (int i = 0; i < 4; ++i) qv[i] = ld16<0>(rq, orow * 1152 + grp * 128 + (lane & 7) * 16, i * 16 * 1152);
  if (grp == 8 || w4 == 0) wait_flags(ws, F_Y + (grp == 8 ? 128 + lane : grp * 16 + (lane & 15)), *(LAS u64*)(sm + L_TAG));
  if (grp < 8) {
    if (w4 == 0) arrive(L_CNT + 24);
    reach(L_CNT + 24, 1);
  }
#pragma unroll
  for (int i = 0; i < 4; ++i) yv[i] = ld16<SC1>(ry, (grp * 16 + idx) * 16384 + w4 * 4096 + lane * 16, i * 1024);
  STAMP(12);
#pragma unroll
  for (int i = 0; i < 4; ++i) *(LAS u4*)(yt + (lane >> 5) * 528 + (lane & 31) * 16 + i * 1056) = yv[i];
  WAITL0();
  const LAS u16* p = (const LAS u16*)(yt + (lane & 7) * 528 + (lane >> 3) * 2);
  unsigned yy[4][8];
#pragma unroll
  for (int i = 0; i < 4; ++i)
#pragma unroll
    for (int d = 0; d < 8; ++d) yy[i][d] = p[i * 8 + d * 32];
  SB();
#pragma unroll
  for (int i = 0; i < 4; ++i) {
    u4 o;
#pragma unroll
    for (int d = 0; d < 4; ++d) o[d] = pk(lo16(yy[i][2 * d]) * lo16(qv[i][d]), lo16(yy[i][2 * d + 1]) * hi16(qv[i][d]));
    st16<0>(o, rout, orow * 2304 + 1152 + grp * 128 + (lane & 7) * 16 + i * 16 * 2304);
  }
}

DEV void out_proj(lptr sm, char* ws, u16* out, int bid, int lw, int lane) {
  const int rt = ((bid + OSH) & 7) * 4 + (bid >> 6), ct = (bid >> 3) & 7;
  const int r = lane & 15, g = lane >> 4;
  lptr wa[2] = {sm + L_WO + lane * 16, sm + L_WO + lane * 16 + 9 * 4608};
  lptr wb[2] = {sm + L_WO + 4096 + (g * 8 + (r & 7)) * 16, sm + L_WO + 4096 + (g * 8 + (r & 7)) * 16 + 9 * 4608};
  pin(wa[0]); pin(wa[1]); pin(wb[0]); pin(wb[1]);
  rsrc_t ro = mkrs(ws + WS_O, NB_ACT);
  const unsigned ob = rt * 72 * 1024 + g * 1024 + lw * 256 + r * 16;
  const u64 tag = *(LAS u64*)(sm + L_TAG);
  unsigned oo[3];
#pragma unroll
  for (int i = 0; i < 3; ++i) {
    const int it = lane + 64 * i, row = (it * 57) >> 9, c = it - row * 9;
    oo[i] = (i == 2 && lane >= 16) ? OOB : (rt * 64 + 32 * (lw >> 1) + (lw & 1) + 2 * row) * 2304 + ct * 144 + c * 16;
    pin(oo[i]);
  }
  lptr ot = sm + L_OT + lw * 2304 + lane * 16, ow = sm + L_OT + lw * 2304 + r * 144 + g * 8;
  pin(ot); pin(ow);
  reach(L_CNT + 20, 4);
  f4 acc[5];
#pragma unroll 1
  for (int att = 0; att < 4096; ++att) {
    constexpr int PRE = 7, AHEAD = 9;
    bf8 wf[PRE][5];
    auto rdw = [&](int i) {
#pragma unroll
      for (int n = 0; n < 5; ++n)
        wf[i % PRE][n] = *(const LAS bf8*)((n < 4 ? wa[i / 9] + n * 1024 : wb[i / 9]) + (i % 9) * 4608);
    };
#pragma unroll
    for (int i = 0; i < PRE - 1; ++i) rdw(i);
    SB();
    if (lw == 0 && att == 0) {
      wait_flags(ws, F_A + (rt >> 1) * 8 + (lane & 7), tag);
      arrive(L_CNT + 28);
    }
    reach(L_CNT + 28, 1);
    STAMPL(9);
    u4 of[18];
    auto ldo = [&](int i) { of[i] = ld16<SC1>(ro, ob, (i + ROTK * ct < 18 ? i + ROTK * ct : i + ROTK * ct - 18) * 4096); };
#pragma unroll
    for (int i = 0; i < AHEAD; ++i) { ldo(i); SB(); }
#pragma unroll
    for (int n = 0; n < 5; ++n) acc[n] = f4{0.f, 0.f, 0.f, 0.f};
    unsigned mx = 0;
#pragma unroll
    for (int i = 0; i < 18; ++i) {
      if (i + PRE - 1 < 18) rdw(i + PRE - 1);
#pragma unroll
      for (int n = 0; n < 5; ++n) acc[n] = mfma(wf[i % PRE][n], asb(of[i]), acc[n]);
      mx = max4(of[i], mx);
      if (i + AHEAD < 18) ldo(i + AHEAD);
      SGB(0x8, 1); SGB(0x20, 1); SGB(0x100, 1);
#pragma unroll
      for (int n = 1; n < 5; ++n) { SGB(0x8, 1); SGB(0x100, 1); SGB(0x2, 1); }
      SB();
    }
    if (__all(mx != NOTYET)) break;
    asm volatile("" ::: "memory");
  }
  STAMPL(10);
#pragma unroll
  for (int n = 0; n < 5; ++n)
    if (n < 4 || g < 2) *(LAS u2*)(ow + n * 32) = u2{pk(acc[n][0], acc[n][1]), pk(acc[n][2], acc[n][3])};
  WAITL0();
  rsrc_t rout = mkrs(out, 2 * NB_ACT);
#pragma unroll
  for (int i = 0; i < 3; ++i) st16<0>(*(const LAS u4*)(ot + i * 1024), rout, oo[i]);
  STAMPL(15);
}

__global__ __launch_bounds__(512) __attribute__((amdgpu_waves_per_eu(2, 2))) void fused(
    const u16* x, const u16* wqkv, const u16* wo, const u16* q, const u16* k, const u16* v, const u16* shk,
    const u16* hk, const u16* dv, u16* out, char* ws) {
  extern __shared__ __attribute__((aligned(16))) char smem[];
  lptr sm = (lptr)smem;
  const int tid = threadIdx.x, lane = tid & 63, w = __builtin_amdgcn_readfirstlane(tid >> 6);
  const int bid = blockIdx.x;
  if (bid < 128 && tid < 15) {
    u64 pc;
    asm volatile("s_getpc_b64 %0" : "=s"(pc));
    asm volatile("s_mov_b32 m0, %0\n\ts_nop 0\n\tglobal_load_lds_dword %1, %2"
                 :: "s"(L_PF), "v"(((bid >> 3) + 16 * tid) * 128), "s"(pc & ~127ull) : "memory");
  }
  if (__builtin_expect(bid < 128, 1)) role_a(sm, ws, x, wqkv, bid, w, lane);
  else role_b(sm, ws, x, wqkv, wo, k, v, shk, hk, dv, bid, w, lane);
  if (w >= 4) {
    out_proj(sm, ws, out, bid, w & 3, lane);
  } else if (bid < 128 || (bid < 192 && w == 0)) {
    const bool g8 = bid >= 128;
    if (!g8) { arrive(L_CNT + 16); reach(L_CNT + 16, 4); reach(L_CNT + 20, 4); }
    finish(sm, sm + (g8 ? U_OFF : w * 4224), ws, q, out, g8 ? 8 : (bid + YSH) & 7, g8 ? (bid - 128) >> 2 : bid >> 3, g8 ? bid & 3 : w, lane);
    STAMP(13);
  } else if (bid == 128 && w == 2) {
    const u64 tag = *(LAS u64*)(sm + L_TAG);
#pragma unroll 1
    for (int i = 0; i < 4; ++i) wait_flags(ws, i * 64 + lane, tag);
    if (lane == 0) __hip_atomic_store((GAS u64*)ws, tag, RLX_AGENT);
  }
}

extern "C" void kernel_launch(void* const* d_in, const int* in_sizes, int n_in, void* d_out, int out_size, void* d_ws,
                              size_t ws_size, hipStream_t stream) {
  (void)hipFuncSetAttribute((const void*)fused, hipFuncAttributeMaxDynamicSharedMemorySize, LDS_BYTES);
  fused<<<256, 512, LDS_BYTES, stream>>>((const u16*)d_in[0], (const u16*)d_in[1], (const u16*)d_in[2], (const u16*)d_in[3],
                                         (const u16*)d_in[4], (const u16*)d_in[5], (const u16*)d_in[6], (const u16*)d_in[7],
                                         (const u16*)d_in[8], (u16*)d_out, (char*)d_ws);
}
```

```cpp
#define STAMPS
#include <hip/hip_runtime.h>

typedef unsigned short u16;
typedef unsigned long long u64;
typedef short s4 __attribute__((ext_vector_type(4)));
typedef __bf16 bf2 __attribute__((ext_vector_type(2)));
typedef __bf16 bf8 __attribute__((ext_vector_type(8)));
typedef float f2 __attribute__((ext_vector_type(2)));
typedef float f4 __attribute__((ext_vector_type(4)));
typedef unsigned u4 __attribute__((ext_vector_type(4)));
typedef unsigned u2 __attribute__((ext_vector_type(2)));
typedef __amdgpu_buffer_rsrc_t rsrc_t;
#define LAS __attribute__((address_space(3)))
#define GAS __attribute__((address_space(1)))
typedef LAS char* lptr;
#define RLX_AGENT __ATOMIC_RELAXED, __HIP_MEMORY_SCOPE_AGENT
#define DEV __device__ __forceinline__

constexpr int NB_ACT = 2048 * 576 * 2;
constexpr int NB_W = 576 * 1728 * 2;
constexpr int NB_WO = 576 * 576 * 2;
constexpr unsigned OOB = 0x80000000u;
constexpr unsigned NOTYET = 0xffffffffu;
constexpr int SC1 = 16;
#define ROTK 2
#define PFS 12
#define TDELAY 0
#define YSH 0
#define PF0 2
#define XSH 1
#define OSH 2
#define PSLEEP 4
constexpr int SPIN_MAX = 1 << 15;

constexpr int WS_FLAG = 4096;
constexpr int F_A = 0, F_P = 128, F_Y = 256, F_Q = 512, F_W = 640, F_G = 896;
constexpr int WS_U = 1 << 20;
constexpr int WS_Y = WS_U + (3 << 20);
constexpr int WS_O = WS_Y + (3 << 20);
constexpr int WS_WO = WS_O + (3 << 20);
constexpr int WS_DBG = 16 << 20;

constexpr int LDS_BYTES = 163840;
constexpr int XB = 144 * 128;
constexpr int SLOT = XB + 64 * 432;
constexpr int PS = 1160, HS = 32640;
constexpr int L_WO = 65152;
constexpr int L_OT = L_WO + 83200;
constexpr int H_CP = 4160, U_OFF = 8448, CH_SZ = 13024;
constexpr int L_TAG = L_OT + 10240;
constexpr int L_CNT = L_TAG + 16;
constexpr int L_YA = L_TAG + 128;
constexpr int L_PF = LDS_BYTES - 256;

#define STAMPW(i, wv) do { if (threadIdx.x == (wv) * 64) *(LAS u64*)(size_t)(L_PF - 256 + (i) * 8) = __builtin_amdgcn_s_memtime(); } while (0)
#define STAMP(i) STAMPW(i, 0)
#define STAMPL(i) STAMPW(i, 4)

DEV rsrc_t mkrs(const void* p, int bytes) { return __builtin_amdgcn_make_buffer_rsrc((void*)p, 0, bytes, 0x00020000); }
DEV u4 mkdesc(const void* p, int bytes) {
  u64 a = (u64)p;
  return u4{(unsigned)a, (unsigned)(a >> 32) & 0xffffu, (unsigned)bytes, 0x00020000u};
}
DEV void dma16(u4 r, unsigned lds, unsigned voff, unsigned soff) {
  asm volatile("s_mov_b32 m0, %0\n\ts_nop 0\n\tbuffer_load_dwordx4 %1, %2, %3 offen lds"
               :: "s"(lds), "v"(voff), "s"(r), "s"(soff) : "memory");
}
DEV void dma16c(u4 r, unsigned lds, unsigned voff, unsigned soff) {
  asm volatile("s_mov_b32 m0, %0\n\ts_nop 0\n\tbuffer_load_dwordx4 %1, %2, %3 offen sc1 lds"
               :: "s"(lds), "v"(voff), "s"(r), "s"(soff) : "memory");
}
DEV void dma16m(u4 r, unsigned lds, unsigned voff, unsigned soff, u64 mask) {
  asm volatile("s_mov_b32 m0, %0\n\ts_mov_b64 exec, %4\n\tbuffer_load_dwordx4 %1, %2, %3 offen lds\n\ts_mov_b64 exec, -1"
               :: "s"(lds), "v"(voff), "s"(r), "s"(soff), "s"(mask) : "memory");
}
template <int AUX> DEV void st16(u4 v, rsrc_t r, unsigned off) { __builtin_amdgcn_raw_buffer_store_b128(v, r, off, 0, AUX); }
template <int AUX> DEV u4 ld16(rsrc_t r, unsigned off, unsigned soff) { return __builtin_amdgcn_raw_buffer_load_b128(r, off, soff, AUX); }
DEV u4 ld16c(u4 r, unsigned voff, unsigned soff) {
  u4 v;
  asm volatile("buffer_load_dwordx4 %0, %1, %2, %3 offen sc1" : "=v"(v) : "v"(voff), "s"(r), "s"(soff) : "memory");
  return v;
}
DEV void touch(u4 r, unsigned voff, unsigned soff) {
  asm volatile("s_mov_b32 m0, %0\n\ts_nop 0\n\tbuffer_load_dword %1, %2, %3 offen lds"
               :: "s"(L_PF), "v"(voff), "s"(r), "s"(soff) : "memory");
}
DEV s4 trread(lptr p) { return __builtin_amdgcn_ds_read_tr16_b64_v4i16((LAS s4*)p); }
DEV bf8 cat8(s4 a, s4 b) { return __builtin_bit_cast(bf8, __builtin_shufflevector(a, b, 0, 1, 2, 3, 4, 5, 6, 7)); }
DEV bf8 asb(u4 v) { return __builtin_bit_cast(bf8, v); }
DEV f4 mfma(bf8 a, bf8 b, f4 c) { return __builtin_amdgcn_mfma_f32_16x16x32_bf16(a, b, c, 0, 0, 0); }
DEV unsigned pk(float lo, float hi) { f2 v = {lo, hi}; return __builtin_bit_cast(unsigned, __builtin_convertvector(v, bf2)); }
DEV float asf(unsigned u) { return __builtin_bit_cast(float, u); }
DEV unsigned asu(float f) { return __builtin_bit_cast(unsigned, f); }
DEV float lo16(unsigned x) { return asf(x << 16); }
DEV float hi16(unsigned x) { return asf(x & 0xffff0000u); }
DEV float dot2(unsigned a, unsigned b, float c) {
  return __builtin_amdgcn_fdot2_f32_bf16(__builtin_bit_cast(bf2, a), __builtin_bit_cast(bf2, b), c, false);
}
DEV unsigned alignb(unsigned hi, unsigned lo) { return __builtin_amdgcn_alignbit(hi, lo, 16); }
DEV unsigned umax(unsigned a, unsigned b) { return a > b ? a : b; }
DEV unsigned max4(u4 v, unsigned m) { return umax(umax(umax(v[0], v[1]), m), umax(v[2], v[3])); }
DEV float fmax2(float a, float b) { return __builtin_elementwise_maximum(a, b); }
#define WAITV(n) asm volatile("s_waitcnt vmcnt(" #n ")" ::: "memory")
#define WAITL0() asm volatile("s_waitcnt lgkmcnt(0)" ::: "memory")
#define SB() __builtin_amdgcn_sched_barrier(0)
#define SGB(m, n) __builtin_amdgcn_sched_group_barrier(m, n, 0)
DEV void bar() { WAITL0(); __builtin_amdgcn_s_barrier(); asm volatile("" ::: "memory"); }
template <class T> DEV void pin(T& x) { asm volatile("" : "+v"(x)); }

DEV u64 get_tag(char* ws) {
  u64 e;
  asm volatile("s_load_dwordx2 %0, %1, 0x0\n\ts_waitcnt lgkmcnt(0)" : "=s"(e) : "s"(ws) : "memory");
  return e * 6364136223846793005ull + 1442695040888963407ull;
}
DEV void post_flag(char* ws, int idx, u64 tag) { __hip_atomic_store((GAS u64*)(ws + WS_FLAG + idx * 128), tag, RLX_AGENT); }
DEV void wait_flags(char* ws, int idx, u64 tag) {
  GAS u64* p = (GAS u64*)(ws + WS_FLAG + idx * 128);
#pragma unroll 1
  for (int spin = 0; spin < SPIN_MAX; ++spin) {
    if (__all(__hip_atomic_load(p, RLX_AGENT) == tag)) break;
    __builtin_amdgcn_s_sleep(PSLEEP);
  }
  __builtin_amdgcn_fence(__ATOMIC_ACQUIRE, "wavefront");
}

DEV void arrive(unsigned word) {
  unsigned one = 1;
  asm volatile("s_waitcnt lgkmcnt(0)\n\ts_mov_b64 exec, 1\n\tds_add_u32 %0, %1\n\ts_mov_b64 exec, -1"
               :: "v"(word), "v"(one) : "memory");
}
DEV void reach(unsigned word, unsigned n) {
#pragma unroll 1
  for (int spin = 0; spin < (1 << 22); ++spin) {
    unsigned v;
    asm volatile("ds_read_b32 %0, %1\n\ts_waitcnt lgkmcnt(0)" : "=v"(v) : "v"(word) : "memory");
    if ((unsigned)__builtin_amdgcn_readfirstlane(v) >= n) break;
    __builtin_amdgcn_s_sleep(1);
  }
}

DEV u4 ld2(lptr p) {
  const u2 a = *(const LAS u2*)p, b = *(const LAS u2*)(p + HS);
  return u4{a[0], a[1], b[0], b[1]};
}
DEV void att_consts(lptr sm, int lane, u4 (&tc)[9], int first) {
  const LAS u4* tb = (const LAS u4*)(sm + L_OT) + lane;
#pragma unroll
  for (int i = 0; i < 9; ++i) tc[i] = tb[(i < 4 ? i : i == 4 ? 4 + first : i + 1) * 64];
}

template <int HI>
DEV void g1_compute(lptr sm, int n0, int lane, u4 (&tc)[9], int first) {
  const int r = lane & 15, g = lane >> 4;
  lptr xa0 = sm + r * 128 + ((g ^ ((r >> 1) & 7)) << 4);
  lptr xa1 = (lptr)((size_t)xa0 ^ 64);
  lptr wa = sm + XB + (((g >> 1) << 4) + ((g & 1) << 3) + ((r >> 2) << 1)) * 432 + (lane & 3) * 8 + n0 * 32;
#define USED(m, n) (HI ? ((n) > 0 || (m) >= 5) : ((n) < 3 || (m) < 5))
  f4 acc[9][4];
#pragma unroll
  for (int m = 0; m < 9; ++m)
#pragma unroll
    for (int n = 0; n < 4; ++n) acc[m][n] = f4{0.f, 0.f, 0.f, 0.f};
  bf8 xf[9], wf[2][4];
  auto mm = [&](bf8 (&wv)[4], int m) {
#pragma unroll
    for (int n = 0; n < 4; ++n)
      if (USED(m, n)) acc[m][n] = mfma(wv[n], xf[m], acc[m][n]);
  };
  auto rdx = [&](lptr xp, int m) { xf[m] = *(const LAS bf8*)(xp + m * 2048); };
  auto rdw = [&](lptr wp, bf8 (&wv)[4], int n) { wv[n] = cat8(trread(wp + n * 32), trread(wp + 432 + n * 32)); };
  auto mix = [&]() {
#pragma unroll
    for (int i = 0; i < 4; ++i) { SGB(0x8, 1); SGB(0x100, 1); }
    SB();
  };
  auto step = [&](bf8 (&wc)[4], bf8 (&wn)[4], lptr xp, lptr wp, int m0) {
#pragma unroll
    for (int m = m0; m < 9; ++m) {
      mm(wc, m);
      if (m - m0 < 4) rdw(wp, wn, m - m0);
      if (m0 == 0) { if (m > 0) rdx(xp, m - 1); }
      else if (m - m0 < 4) rdx(xp, m - m0);
      else { rdx(xp, 2 * (m - m0) - 4); if (2 * (m - m0) - 3 < m) rdx(xp, 2 * (m - m0) - 3); }
      mix();
    }
#pragma unroll
    for (int m = (m0 == 0 ? 8 : 2 * (9 - m0) - 4); m < 9; ++m) rdx(xp, m);
    SB();
  };
  constexpr int MS = 3;
  bar();
#pragma unroll
  for (int n = 0; n < 4; ++n) rdw(wa, wf[0], n);
#pragma unroll
  for (int m = 0; m < 9; ++m) rdx(xa0, m);
  SB();
  int so = 0;
#pragma unroll 1
  for (int k = 0; k < 8; ++k) {
    step(wf[0], wf[1], xa1 + so, wa + so + 32 * 432, 0);
    so = so == 2 * SLOT ? 0 : so + SLOT;
#pragma unroll
    for (int m = 0; m < MS; ++m) mm(wf[1], m);
    SB();
    bar();
    step(wf[1], wf[0], xa0 + so, wa + so, MS);
  }
  xa1 += 2 * SLOT; wa += 2 * SLOT + 32 * 432;
  pin(xa1); pin(wa);
  STAMP(1);
  lptr d = sm + (2 * n0 + (g >> 1)) * PS + r * 8 + (g & 1) * HS;
  bf8 xg[9];
#pragma unroll
  for (int m = 0; m < 11; ++m) {
    if (m < 9) mm(wf[0], m);
    if (m < 2) { rdw(wa, wf[1], 2 * m); rdw(wa, wf[1], 2 * m + 1); }
    if (m < 9) xg[m] = *(const LAS bf8*)(xa1 + m * 2048);
    if (m >= 2) {
#pragma unroll
      for (int n = 0; n < 4; ++n)
        if (USED(m - 2, n)) acc[m - 2][n] = mfma(wf[1][n], xg[m - 2], acc[m - 2][n]);
    }
    if (m == 7) att_consts(sm, lane, tc, first);
    if (m == 8) arrive(L_CNT + 8);
#pragma unroll
    for (int o = m - 3; o < (m == 10 ? 9 : m - 2); ++o) {
      if (o >= 0) {
#pragma unroll
        for (int n = 0; n < 4; ++n)
          if (USED(o, n))
            *(LAS u2*)(d + n * 2 * PS + o * 128) = u2{pk(acc[o][n][0], acc[o][n][1]), pk(acc[o][n][2], acc[o][n][3])};
      }
    }
    if (m < 3) {
#pragma unroll
      for (int i = 0; i < 4; ++i) { SGB(0x8, 1); SGB(0x100, 2); }
    } else {
      SGB(0x8, 1); SGB(0x100, 1);
#pragma unroll
      for (int i = 0; i < 4; ++i) { SGB(0x8, 1); SGB(0x2, 2); SGB(0x8, 1); SGB(0x200, 1); }
    }
    SB();
  }
}

DEV void role_a(lptr sm, char* ws, const u16* x, const u16* wqkv, int bid, int w, int lane) {
  const int xcd = bid & 7, j = bid >> 3;
  const int rt = (xcd >> 1) * 4 + (j >> 2), hq = (xcd & 1) * 4 + (j & 3);
  const u4 rp = mkdesc(ws + WS_WO + (j & 7) * 82944, 82944);
  u64 tag;
  u4 tc[9], wr[12];
  const int aw = w ^ 4;
  const int first = rt + (aw >> 1) == 0;
  if (__builtin_expect(w >= 4, 1)) {
    const int lw = w - 4;
    const u4 rx = mkdesc(x, NB_ACT), rw = mkdesc(wqkv, NB_W);
    const int t27 = lane >= 27, cc = lane - 27 * t27, part = (cc >= 9) + (cc >= 18);
    const unsigned vw = t27 * (4 * 3456) + part * (1152 - 144) + cc * 16 + hq * 144;
    auto wpiece = [&](unsigned d, int kt, int i) {
      dma16m(rw, d + XB + (lw * 8 + i) * 864, vw, (kt * 64 + lw * 16 + 8 * (i >> 2) + (i & 3)) * 3456, (1ull << 54) - 1);
    };
    auto tailp = [&](unsigned d, int kt) {
#pragma unroll
      for (int i = 2; i < 8; ++i) wpiece(d, kt, i);
    };
    STAMPL(16);
    tailp(0, 0);
    SB();
    unsigned vx[6];
#pragma unroll
    for (int i = 0; i < 6; ++i) {
      const int xr = lw * 36 + i * 6 + (lane >> 3);
      vx[i] = __umul24(rt * 128 + xr, 1152) + (((lane & 7) ^ ((xr >> 1) & 7)) << 4) - 16 * 1152;
    }
    auto headp = [&](unsigned d, int kt) {
#pragma unroll
      for (int i = 0; i < 6; ++i) dma16m(rx, d + (lw * 6 + i) * 768, vx[i], kt * 128, (1ull << 48) - 1);
#pragma unroll
      for (int i = 0; i < 2; ++i) wpiece(d, kt, i);
    };
    headp(0, 0);
    unsigned d = SLOT;
#pragma unroll 1
    for (int k = 1; k < 9; ++k) {
      headp(d, k);
      WAITV(8);
      bar();
      STAMPL(16 + k);
      tailp(d, k);
      d = d == 2 * SLOT ? 0 : d + SLOT;
    }
    STAMPL(26);
    tag = *(LAS u64*)(sm + L_TAG);
    att_consts(sm, lane, tc, first);
    if (lw == 3) wait_flags(ws, lane < 8 ? F_Q + rt * 8 + lane : F_W + (j & 7) * 32 + (lane < 26 ? lane - 8 : 0), tag);
    WAITV(0);
    bar();
    STAMPL(27);
#pragma unroll
    for (int i = 0; i < 12; ++i) wr[i] = ld16c(rp, lane * 16, (24 + 4 * i + lw) * 1024);
#pragma unroll 1
    for (int i = lw - 12; i < 24; i += 4) {
      const int ii = i < 0 ? i + 84 : i;
      if (ii < 81) dma16c(rp, L_WO + ii * 1024, lane * 16, ii * 1024);
    }
    STAMPL(28);
  } else {
    tag = get_tag(ws);
    if (threadIdx.x == 0) { *(LAS u64*)(sm + L_TAG) = tag; *(LAS u4*)(sm + L_CNT) = u4{0, 0, 0, 0}; *(LAS u4*)(sm + L_CNT + 16) = u4{0, 0, 0, 0}; *(LAS u4*)(sm + L_CNT + 32) = u4{0, 0, 0, 0}; }
    {
      const int r = lane & 15, g = lane >> 4, q4 = r >> 2, p4 = lane & 3;
      LAS u4* tb = (LAS u4*)(sm + L_OT) + lane;
      u4 t0, t1;
      if (w < 2) {
#pragma unroll
        for (int dd = 0; dd < 4; ++dd) {
          t0[dd] = __builtin_amdgcn_sbfe((int)(0x1ffu << (2 * w)), 4 * g + dd, 1);
          t1[dd] = __builtin_amdgcn_sbfe((int)(0x3feu << (2 * w)), 4 * g + dd, 1);
        }
      } else if (w == 2) {
#pragma unroll
        for (int dd = 0; dd < 4; ++dd) {
          const int kk = 4 * g + dd - r;
          t0[dd] = (kk >= 1 && kk <= 8) ? 0u : 0xff800000u;
          t1[dd] = (kk >= 1 && kk <= 8 && g >= 2) ? 0u : 0xff800000u;
        }
      } else {
#pragma unroll
        for (int dd = 0; dd < 4; ++dd) t0[dd] = 4 * g + dd - r <= -8 ? 0u : 0xff800000u;
        t1[0] = (t0[0] ? 0u : 0xffffu) | (t0[1] ? 0u : 0xffff0000u);
        t1[1] = (t0[2] ? 0u : 0xffffu) | (t0[3] ? 0u : 0xffff0000u);
        t1[2] = g * PS + r * 16;
        t1[3] = g * PS + (r & 7) * 16;
        const int kr0 = (4 * g + q4) * 16, kr1 = kr0 + (g >= 2 ? 128 : 256);
        const int ve = (p4 >> 1) * PS + (p4 & 1) * HS, vo = ((p4 + 1) >> 1) * PS + ((p4 + 1) & 1) * HS;
        tb[8 * 64] = u4{(unsigned)(kr0 + ve), (unsigned)(kr1 + ve), (unsigned)(kr0 + vo), (unsigned)(kr1 + vo)};
        const unsigned os = (rt * 144 + hq * 9 + g) * 1024 + r * 16;
        tb[9 * 64] = u4{(unsigned)kr0, (unsigned)kr1, os, g ? OOB : os + 8192};
      }
      tb[2 * w * 64] = t0;
      tb[(2 * w + 1) * 64] = t1;
    }
#pragma unroll
    for (int i = 0; i < 12; ++i) wr[i] = __builtin_nondeterministic_value(wr[i]);
    if (w & 1) g1_compute<1>(sm, 3 + (w >> 1) * 7, lane, tc, first);
    else g1_compute<0>(sm, (w >> 1) * 7, lane, tc, first);
  }
  {
    const unsigned rb = (32 * (aw >> 1) + (aw & 1)) * 8;
    const u4 qm[4] = {tc[0], tc[1], tc[2], tc[3]}, u0 = tc[4], u1 = tc[5], c0 = tc[6], c1 = tc[7], c2 = tc[8];
    const f4 b0 = __builtin_bit_cast(f4, u0), b1 = __builtin_bit_cast(f4, u1);
    const unsigned m01 = c0[0], m23 = c0[1];
    lptr qa = sm + rb + c0[2], k1 = sm + rb + c0[3];
    lptr ve0 = sm + rb + c1[0], ve1 = sm + rb + c1[1], vo0 = sm + rb + c1[2], vo1 = sm + rb + c1[3];
    lptr vk0 = sm + rb + c2[0], vk1 = sm + rb + c2[1];
    const unsigned ow = (aw >> 2) * 73728 + (aw & 3) * 256;
    unsigned os0 = c2[2] + ow, os1 = os0 + 4096, os2 = c2[3] + ow;
    const bool g0 = lane < 16;
    rsrc_t ro = mkrs(ws + WS_O, NB_ACT);
    u4 ones = {0x3f803f80u, 0x3f803f80u, 0x3f803f80u, 0x3f803f80u};
    pin(qa); pin(k1); pin(ve0); pin(ve1); pin(vo0); pin(vo1); pin(vk0); pin(vk1); pin(os0); pin(os1); pin(os2); pin(ones);
    STAMP(14);
    if (w < 4) arrive(L_CNT);
    reach(w < 4 ? L_CNT : L_CNT + 8, 4);
    STAMP(2);
    STAMPL(29);
    const float cs = 0.23570226039551584f * 1.4426950408889634f;
    const f4 z = {0.f, 0.f, 0.f, 0.f};
    u4 qf[4];
    bf8 ka[4], kb[4], vt0[4], vt1[4];
#pragma unroll
    for (int h = 0; h < 4; ++h) {
      qf[h] = ld2(qa + 2 * h * PS + 128);
      ka[h] = asb(ld2(qa + (9 + 2 * h) * PS));
      kb[h] = asb(ld2(k1 + (9 + 2 * h) * PS + 256));
      SB();
    }
#pragma unroll
    for (int h = 0; h < 4; ++h) {
      vt0[h] = cat8(trread((h < 2 ? ve0 : vo0) + (18 + 2 * h) * PS), trread((h < 2 ? ve1 : vo1) + (18 + 2 * h) * PS));
      vt1[h] = cat8(trread(vk0 + (20 + 2 * h) * PS + (h < 2 ? 0 : HS)), trread(vk1 + (20 + 2 * h) * PS + (h < 2 ? 0 : HS)));
    }
    SB();
    f4 s0[4], s1[4], s[4];
    float mx[4];
#pragma unroll
    for (int h = 0; h < 4; ++h) {
      bf8 qq = asb(qf[h] & qm[h]);
      s0[h] = mfma(ka[h], qq, b0);
      s1[h] = mfma(kb[h], qq, b1);
      SB();
    }
#pragma unroll
    for (int h = 0; h < 4; ++h) {
#pragma unroll
      for (int dd = 0; dd < 4; ++dd) s[h][dd] = fmax2(s0[h][dd], s1[h][dd]);
      mx[h] = fmax2(fmax2(fmax2(s[h][0], s[h][1]), s[h][2]), s[h][3]);
      SB();
    }
    {
      auto a = __builtin_amdgcn_permlane16_swap(asu(mx[0]), asu(mx[1]), false, false);
      auto b = __builtin_amdgcn_permlane16_swap(asu(mx[2]), asu(mx[3]), false, false);
      auto c = __builtin_amdgcn_permlane32_swap(asu(fmax2(asf(a[0]), asf(a[1]))), asu(fmax2(asf(b[0]), asf(b[1]))), false, false);
      unsigned m = asu(-cs * fmax2(asf(c[0]), asf(c[1])));
      auto e = __builtin_amdgcn_permlane32_swap(m, m, false, false);
      unsigned e0 = e[0], e1 = e[1];
      auto f0 = __builtin_amdgcn_permlane16_swap(e0, e0, false, false);
      auto f1 = __builtin_amdgcn_permlane16_swap(e1, e1, false, false);
      mx[0] = asf(f0[0]); mx[1] = asf(f0[1]); mx[2] = asf(f1[0]); mx[3] = asf(f1[1]);
    }
    SB();
    f4 sum[4], o0[4], o1[4];
#pragma unroll
    for (int h = 0; h < 4; ++h) {
      float p[4];
#pragma unroll
      for (int dd = 0; dd < 4; ++dd) p[dd] = __builtin_amdgcn_exp2f(__builtin_fmaf(s[h][dd], cs, mx[h]));
      unsigned p01 = pk(p[0], p[1]), p23 = pk(p[2], p[3]);
      bf8 pf = asb(u4{p01 & ~m01, p23 & ~m23, p01 & m01, p23 & m23});
      sum[h] = mfma(asb(ones), pf, z);
      o0[h] = mfma(vt0[h], pf, z);
      o1[h] = mfma(vt1[h], pf, z);
      SB();
    }
    asm volatile("s_waitcnt vmcnt(0)" : "+v"(wr[0]), "+v"(wr[1]), "+v"(wr[2]), "+v"(wr[3]), "+v"(wr[4]), "+v"(wr[5]),
                 "+v"(wr[6]), "+v"(wr[7]), "+v"(wr[8]), "+v"(wr[9]), "+v"(wr[10]), "+v"(wr[11]) :: "memory");
    u2 gr[4];
    u4 left;
#pragma unroll
    for (int h = 0; h < 4; ++h) {
      const float inv = __builtin_amdgcn_rcpf(sum[h][0]);
      const unsigned a0 = pk(o0[h][0] * inv, o0[h][1] * inv), a1 = pk(o0[h][2] * inv, o0[h][3] * inv);
      const unsigned e0 = pk(o1[h][0] * inv, o1[h][1] * inv);
      if (h & 1) {
        const unsigned e1 = pk(o1[h][2] * inv, o1[h][3] * inv);
        left[h] = a1;
        gr[h] = u2{g0 ? e0 : a0, g0 ? e1 : a1};
        st16<SC1>(u4{gr[h - 1][0], gr[h - 1][1], gr[h][0], gr[h][1]}, ro, h == 1 ? os0 : os1);
        SB();
      } else {
        left[h] = e0;
        gr[h] = u2{a0, a1};
      }
    }
    st16<SC1>(left, ro, os2);
  }
  if (threadIdx.x == 192) post_flag(ws, F_A + rt * 8 + hq, tag);
  STAMP(3);
  STAMPL(4);
  if (w >= 4) {
#pragma unroll
    for (int i = 0; i < 12; ++i) *(LAS u4*)(sm + L_WO + (20 + 4 * i + w) * 1024 + lane * 16) = wr[i];
    arrive(L_CNT + 20);
    STAMPL(30);
  }
}

DEV void role_b(lptr sm, char* ws, const u16* x, const u16* wqkv, const u16* wo, const u16* q, const u16* k, const u16* v,
                const u16* shk, const u16* hk, const u16* dv, int bid, int w, int lane) {
  const int tid = threadIdx.x, xcd = bid & 7, jb = (bid >> 3) - 16, me = xcd * 16 + jb;
  const int G = lane >> 4, i16 = lane & 15, w4 = w & 3;
  rsrc_t ru = mkrs(ws + WS_U, NB_ACT);
  const int xc = (xcd + XSH) & 7, cm = xc * 16 + jb;
  const int c5 = 512 + xcd * 8 + (jb & 7);
  const unsigned tb = L_WO + w * 9216;
  const u4 ny = {NOTYET, NOTYET, NOTYET, NOTYET};
  rsrc_t ry = mkrs(ws + WS_Y, NB_ACT);
  u64 tag;
  if (w < 4 || (w == 4 && jb < 8)) {
    const int grp = w < 4 ? xcd : 8, un = w < 4 ? jb * 4 + w : xcd * 8 + jb;
    const u4 rk = mkdesc(k, NB_ACT), rv = mkdesc(v, NB_ACT);
    const unsigned o0 = (un * 32 + (lane >> 3)) * 1152 + grp * 128 + (((lane & 7) ^ ((lane >> 4) & 1)) << 4);
    if (TDELAY) __builtin_amdgcn_s_sleep(TDELAY);
    dma16(rk, tb, o0 - 8 * 1152, 0);
#pragma unroll
    for (int i = 0; i < 4; ++i) {
      dma16(rk, tb + 1024 + i * 1024, o0 ^ (((i + 1) & 3) << 5), i * 8 * 1152);
      dma16(rv, tb + 5120 + i * 1024, o0 ^ (i << 5), i * 8 * 1152);
    }
    u2 wk[4];
#pragma unroll
    for (int s = 0; s < 4; ++s) wk[s] = *(const GAS u2*)((const char*)shk + (grp * 64 + G * 4 + (i16 & 3)) * 8 + s * 128);
    const unsigned off = ((grp * 16 + G) * 256 + 4 * un) * 64 + i16 * 16;
    tag = get_tag(ws);
    if (tid == 0) { *(LAS u64*)(sm + L_TAG) = tag; *(LAS u4*)(sm + L_CNT) = u4{0, 0, 0, 0}; *(LAS u4*)(sm + L_CNT + 16) = u4{0, 0, 0, 0}; *(LAS u4*)(sm + L_CNT + 32) = u4{0, 0, 0, 0}; }
    bar();
    const int q4 = i16 >> 2, p4 = lane & 3;
    const int f0 = (q4 >> 1) | (p4 << 1), f1 = (q4 >> 1) | (((p4 + 1) & 3) << 1);
    const unsigned a0 = tb + (8 * p4 + q4) * 128 + (((G >> 1) ^ f0) << 4) + (G & 1) * 8;
    const unsigned a1 = tb + (8 * p4 + q4) * 128 + (((G >> 1) ^ f1) << 4) + (G & 1) * 8;
    asm volatile("s_waitcnt vmcnt(0)" : "+v"(wk[0]), "+v"(wk[1]), "+v"(wk[2]), "+v"(wk[3]) :: "memory");
    STAMP(1);
    u2 K[4][3], V[4][2];
#pragma unroll
    for (int s = 0; s < 4; ++s) {
      lptr b0 = (lptr)(size_t)(a0 ^ (s << 5)), b1 = (lptr)(size_t)(a1 ^ (s << 5));
      K[s][0] = __builtin_bit_cast(u2, trread(b0 + 512));
      K[s][1] = __builtin_bit_cast(u2, trread(b1 + 1024));
      K[s][2] = __builtin_bit_cast(u2, trread(b1 + 1536));
      V[s][0] = __builtin_bit_cast(u2, trread(b0 + 5120));
      V[s][1] = __builtin_bit_cast(u2, trread(b0 + 5120 + 512));
      SB();
    }
#pragma unroll
    for (int s = 0; s < 4; ++s) {
      const unsigned wA = alignb(wk[s][1], wk[s][1]), wB = alignb(wk[s][0], wk[s][0]);
      unsigned kk[6] = {K[s][0][0], K[s][0][1], K[s][1][0], K[s][1][1], K[s][2][0], K[s][2][1]};
      unsigned vv[4] = {V[s][0][0], V[s][0][1], V[s][1][0], V[s][1][1]};
      unsigned sh[5];
#pragma unroll
      for (int d = 0; d < 5; ++d) sh[d] = alignb(kk[d + 1], kk[d]);
      u4 o;
#pragma unroll
      for (int d = 0; d < 4; ++d) {
        float ev = dot2(sh[d + 1], wB, dot2(sh[d], wA, 0.f)) * lo16(vv[d]);
        float od = dot2(kk[d + 2], wB, dot2(kk[d + 1], wA, 0.f)) * hi16(vv[d]);
        o[3 - d] = pk(od, ev);
      }
      st16<SC1>(o, ru, off + s * 65536);
    }
    STAMP(2);
    WAITV(0);
    if (w < 4) arrive(L_CNT + 36);
    else if (lane == 0) post_flag(ws, F_G + un, tag);
  } else if (w == 7) {
    const u4 rx = mkdesc(x, NB_ACT), rw = mkdesc(wqkv, NB_W);
    const int kr = (lane * 4370) >> 16, cc = lane - kr * 15, part = (cc * 52) >> 8;
    const unsigned fw = lane < 60 ? (jb * 4 + kr) * 3456 + part * (1152 - 640) + ((xcd & 1) * 4 + cc) * 128 : OOB;
    const unsigned fx = lane < 33 ? ((xcd >> 1) * 512 - 16 + jb * 33 + lane) * 1152 : OOB;
    tag = get_tag(ws);
    bar();
    __builtin_amdgcn_s_setprio(3);
#pragma unroll 1
    for (int st = 1; st < 9; ++st) {
      touch(rw, fw, st * 64 * 3456);
      touch(rx, fx, st * 128);
      if (st >= PF0) __builtin_amdgcn_s_sleep(PFS);
    }
    STAMPW(23, 7);
    const u4 rq = mkdesc(q, NB_ACT);
    touch(rq, (jb * 128 + lane) * 1152 + ((xcd + YSH) & 7) * 128, 0);
    touch(rq, (jb * 128 + lane) * 1152 + ((xcd + YSH) & 7) * 128, 64 * 1152);
  } else if (w == 6) {
    tag = get_tag(ws);
    bar();
    rsrc_t rh = mkrs(hk, NB_ACT);
    u4 ha[20];
    unsigned hp[20];
#pragma unroll
    for (int i = 0; i < 20; ++i) {
      const unsigned on = (i >= 16 && jb < 8) ? OOB : 0;
      const unsigned so = (i < 16 ? cm * 4 + (i >> 2) : c5) * 4096;
      ha[i] = ld16<0>(rh, (lane * 16 | on) + (i & 3) * 1024, so);
      hp[i] = __builtin_amdgcn_raw_buffer_load_b32(rh, ((i & 3) ? lane * 16 + (i & 3) * 1024 - 4 : lane ? lane * 16 - 4 : OOB) | on, so, 0);
    }
    SB();
    if (lane < 34) {
#pragma unroll
      for (int i = 0; i < 5; ++i)
        *(LAS u4*)(sm + i * CH_SZ + (lane < 30 ? U_OFF + lane * 16 : ((lane >> 1) & 1) * H_CP + (lane & 1) * 16)) = u4{0, 0, 0, 0};
    }
#pragma unroll
    for (int i = 0; i < 20; ++i) {
      lptr hb = sm + (i >> 2) * CH_SZ + 32 + lane * 16 + (i & 3) * 1024;
      *(LAS u4*)(hb + H_CP) = ha[i];
      *(LAS u4*)hb =
          u4{alignb(ha[i][0], hp[i]), alignb(ha[i][1], ha[i][0]), alignb(ha[i][2], ha[i][1]), alignb(ha[i][3], ha[i][2])};
    }
    arrive(L_CNT + 8);
    STAMPW(18, 6);
    reach(L_CNT + 4, 1);
    __builtin_amdgcn_s_setprio(2);
    __builtin_amdgcn_s_sleep(2);
    rsrc_t ro = mkrs(ws + WS_O, NB_ACT);
#pragma unroll 1
    for (int i = 0; i < 18; i += 3) {
#pragma unroll
      for (int e = 0; e < 3; ++e) st16<SC1>(ny, ro, (me * 18 + i) * 1024 + lane * 16 + e * 1024);
    }
    WAITV(0);
    if (lane == 0) post_flag(ws, F_Q + me, tag);
    STAMPW(22, 6);
  } else {
    const int slot = w == 5 ? jb : 2 + jb;
    tag = get_tag(ws);
    bar();
    if (w == 5 || jb >= 14) {
      const u4 rwo = mkdesc(wo, NB_WO);
      const int e4 = (lane * 57) >> 9;
      const unsigned vb = (e4 >> 1) * 1152 + xcd * 144 + (lane - e4 * 9) * 16, vt = (e4 & 1) * 1152;
      int ks = slot + ROTK * xcd;
      if (ks >= 18) ks -= 18;
#pragma unroll 1
      for (int i = 0; i < 8; ++i) {
        const int ci = 4 * ks + (i >> 1), h = (ci * 57) >> 9, c = ci - 9 * h;
        const int row = h * 72 + (c == 8 ? 16 : 36 * (c >> 2) + 4 * (c & 3)) + 2 * (i & 1);
        dma16m(rwo, tb + i * 576, vb + __umul24(vt, c == 8 ? 36 : (c & 3) ? 16 : 32), row * 1152, (1ull << 36) - 1);
      }
      WAITV(0);
      rsrc_t rpw = mkrs(ws + WS_WO, NB_WO);
      lptr p = (lptr)(size_t)tb + (G * 8 + ((i16 >> 2) << 1)) * 144 + (lane & 3) * 8;
      const unsigned po = (xcd * 18 + slot) * 4608;
      u4 f[5];
#pragma unroll
      for (int n = 0; n < 5; ++n) f[n] = __builtin_bit_cast(u4, cat8(trread(p + n * 32), trread(p + 144 + n * 32)));
#pragma unroll
      for (int n = 0; n < 4; ++n) st16<SC1>(f[n], rpw, po + n * 1024 + lane * 16);
      st16<SC1>(f[4], rpw, i16 < 8 ? po + 4096 + (G * 8 + i16) * 16 : OOB);
      WAITV(0);
      if (lane == 0) post_flag(ws, F_W + xcd * 32 + slot, tag);
      STAMPW(21, 5);
    }
  }
  const int r = lane & 15, g = lane >> 4;
  if (w >= 4) {
    if (w < 6) {
      const int ct = (bid >> 3) & 7;
      __builtin_amdgcn_s_setprio(2);
      if (w == 5) {
        reach(L_CNT + 4, 1);
        wait_flags(ws, F_W + ct * 32 + (lane < 18 ? lane : 0), tag);
      } else if (jb >= 8) {
        reach(L_CNT + 4, 1);
        wait_flags(ws, F_G + lane, tag);
        u4 u5[4];
#pragma unroll
        for (int i = 0; i < 4; ++i) u5[i] = ld16<SC1>(ru, lane * 64 + (c5 & 3) * 16, (c5 >> 2) * 16384 + i * 4096);
#pragma unroll
        for (int i = 0; i < 4; ++i) *(LAS u4*)(sm + U_OFF + 480 + 4 * CH_SZ + lane * 16 + i * 1024) = u5[i];
      }
      arrive(L_CNT + 32);
      reach(L_CNT + 32, 2);
      reach(L_CNT + 8, 5);
      STAMPL(16);
      const u4 rp = mkdesc(ws + WS_WO + ct * 82944, 82944);
#pragma unroll 1
      for (int i = (w & 1) * 1024; i < 81 * 1024; i += 2048) dma16c(rp, L_WO + i, lane * 16, i);
      WAITV(0);
      STAMPL(17);
    }
    arrive(L_CNT + 20);
    __builtin_amdgcn_s_setprio(0);
    return;
  }
  typedef const __attribute__((address_space(4))) unsigned* cptr;
  const unsigned dv0 = ((cptr)dv)[cm * 2 + (w >> 1)], dv1 = ((cptr)dv)[c5 >> 1];
  if (w == 3) {
    reach(L_CNT + 36, 4);
    if (lane == 0) post_flag(ws, F_P + me, tag);
    STAMPW(20, 3);
  }
  STAMP(3);
  if (w == 0) {
    __builtin_amdgcn_s_sleep(4);
    wait_flags(ws, F_P + xc * 16 + i16, tag);
    arrive(L_CNT + 4);
  }
  reach(L_CNT + 4, 1);
  STAMP(4);
  {
    u4 uv[4];
#pragma unroll
    for (int i = 0; i < 4; ++i) uv[i] = ld16<SC1>(ru, tid * 16, cm * 16384 + i * 4096);
#pragma unroll
    for (int i = 0; i < 4; ++i) *(LAS u4*)(sm + U_OFF + 480 + (tid & 3) * CH_SZ + (tid >> 2) * 16 + i * 1024) = uv[i];
  }
  arrive(L_CNT + 8);
  const int xd = 15 - r - 8 * g;
#pragma unroll 1
  for (int rnd = 0; rnd < (jb >= 8 ? 2 : 1); ++rnd) {
    const int dd0 = rnd ? 2 * w : 0, ch = rnd ? c5 : cm * 4 + w;
    const unsigned dval = dd0 == 0 ? ((rnd ? dv1 : dv0) >> ((ch & 1) * 16)) & 0xffffu : 0;
    lptr cb = sm + (rnd ? 4 : w) * CH_SZ;
    lptr aa[4];
#pragma unroll
    for (int i = 0; i < 4; ++i) { aa[i] = cb + (r & 1) * H_CP + 4 + 4 * (4 * g + (r >> 1)) + dd0 * 64 + i * 1024; pin(aa[i]); }
    lptr ba = cb + U_OFF + 16 * (31 + 2 * r - g) - 128 - dd0 * 64;
    pin(ba);
    u4 ad;
#pragma unroll
    for (int d = 0; d < 4; ++d) ad[d] = (xd >> 1) == d ? dval << ((xd & 1) * 16) : 0u;
    pin(ad);
    reach(rnd ? L_CNT + 32 : L_CNT + 8, rnd ? 2 : 5);
    STAMP(rnd ? 19 : 5);
    f4 acc[8];
    bf8 A[2][8], B[2][8];
    auto rd = [&](int dd, bf8 (&a)[8], bf8 (&b)[8]) {
#pragma unroll
      for (int i = 0; i < 8; ++i) {
        const LAS unsigned* p = (const LAS unsigned*)(aa[i >> 1] + dd * 64 + (i & 1) * 512);
        a[i] = asb(u4{p[0], p[1], p[2], p[3]});
        b[i] = *(const LAS bf8*)(ba + 128 - dd * 64 + i * 512);
      }
    };
    auto mm = [&](bf8 (&a)[8], bf8 (&b)[8]) {
#pragma unroll
      for (int i = 0; i < 8; ++i)
#pragma unroll
        for (int jj = 0; jj + i < 8; ++jj) acc[i + jj] = mfma(a[i], b[jj], acc[i + jj]);
#pragma unroll
      for (int i = 0; i < 24; ++i) { SGB(0x8, 1); SGB(0x100, 1); }
      SGB(0x8, 12);
      SB();
    };
    rd(0, A[0], B[0]);
#pragma unroll
    for (int n = 0; n < 8; ++n) acc[n] = mfma(asb(ad), B[0][n], f4{0.f, 0.f, 0.f, 0.f});
    SB();
#pragma unroll 1
    for (int it = rnd * 3; it < 4; ++it) {
      rd(1, A[1], B[1]);
      mm(A[0], B[0]);
      rd(2, A[0], B[0]);
      mm(A[1], B[1]);
#pragma unroll
      for (int i = 0; i < 4; ++i) aa[i] += 128;
      ba -= 128;
    }
    STAMP(6 + rnd);
    const unsigned yo = (((ch >> 6) * 16 + 2 * (g & 1) + (r >> 3)) * 4 + ((r >> 2) & 1) + 2 * (g >> 1)) * 4096 + (ch & 63) * 64 + (r & 3) * 16;
    auto put = [&](f4 x, f4 y, int a) {
      auto s0 = __builtin_amdgcn_permlane16_swap(pk(x[0], x[1]), pk(y[0], y[1]), false, false);
      auto s1 = __builtin_amdgcn_permlane16_swap(pk(x[2], x[3]), pk(y[2], y[3]), false, false);
      st16<SC1>(u4{s0[0], s1[0], s0[1], s1[1]}, ry, yo + a * 65536);
    };
    if (rnd == 0) {
#pragma unroll
      for (int a = 0; a < 4; ++a) put(acc[2 * a], acc[2 * a + 1], a);
      if (jb >= 8) continue;
    } else {
      LAS f4* mine = (LAS f4*)(sm + w * CH_SZ + lane * 16);
#pragma unroll
      for (int n = 0; n < 8; ++n) mine[n * 64] = acc[n];
    }
    WAITV(0);
    arrive(L_CNT + 16);
    if (rnd || w == 1) reach(L_CNT + 16, 4);
    if (tid == 64) post_flag(ws, F_Y + cm, tag);
    if (rnd) {
      constexpr int CS = CH_SZ / 16;
      LAS f4* p = (LAS f4*)(sm + lane * 16) + w * 128;
      put((p[0] + p[CS]) + (p[2 * CS] + p[3 * CS]), (p[64] + p[CS + 64]) + (p[2 * CS + 64] + p[3 * CS + 64]), w);
      WAITV(0);
      arrive(L_CNT + 16);
      if (w == 1) {
        reach(L_CNT + 16, 8);
        if (lane == 0) post_flag(ws, F_Y + 128 + (c5 & 63), tag);
      }
    }
  }
  STAMP(8);
}

DEV void finish(lptr sm, lptr yt, char* ws, const u16* q, u16* out, int grp, int idx, int w4, int lane) {
  rsrc_t ry = mkrs(ws + WS_Y, NB_ACT), rq = mkrs(q, NB_ACT), rout = mkrs(out, 2 * NB_ACT);
  const int orow = idx * 128 + 64 * (w4 & 1) + 8 * (w4 >> 1) + (lane >> 3);
  u4 qv[4], yv[4];
#pragma unroll
  for (int i = 0; i < 4; ++i) qv[i] = ld16<0>(rq, orow * 1152 + grp * 128 + (lane & 7) * 16, i * 16 * 1152);
  if (grp == 8 || w4 == 0) wait_flags(ws, F_Y + (grp == 8 ? 128 + lane : grp * 16 + (lane & 15)), *(LAS u64*)(sm + L_TAG));
  if (grp < 8) {
    if (w4 == 0) arrive(L_CNT + 24);
    reach(L_CNT + 24, 1);
  }
#pragma unroll
  for (int i = 0; i < 4; ++i) yv[i] = ld16<SC1>(ry, (grp * 16 + idx) * 16384 + w4 * 4096 + lane * 16, i * 1024);
  STAMP(12);
#pragma unroll
  for (int i = 0; i < 4; ++i) *(LAS u4*)(yt + (lane >> 5) * 528 + (lane & 31) * 16 + i * 1056) = yv[i];
  WAITL0();
  const LAS u16* p = (const LAS u16*)(yt + (lane & 7) * 528 + (lane >> 3) * 2);
  unsigned yy[4][8];
#pragma unroll
  for (int i = 0; i < 4; ++i)
#pragma unroll
    for (int d = 0; d < 8; ++d) yy[i][d] = p[i * 8 + d * 32];
  SB();
#pragma unroll
  for (int i = 0; i < 4; ++i) {
    u4 o;
#pragma unroll
    for (int d = 0; d < 4; ++d) o[d] = pk(lo16(yy[i][2 * d]) * lo16(qv[i][d]), lo16(yy[i][2 * d + 1]) * hi16(qv[i][d]));
    st16<0>(o, rout, orow * 2304 + 1152 + grp * 128 + (lane & 7) * 16 + i * 16 * 2304);
  }
}

DEV void out_proj(lptr sm, char* ws, u16* out, int bid, int lw, int lane) {
  const int rt = ((bid + OSH) & 7) * 4 + (bid >> 6), ct = (bid >> 3) & 7;
  const int r = lane & 15, g = lane >> 4;
  lptr wa[2] = {sm + L_WO + lane * 16, sm + L_WO + lane * 16 + 9 * 4608};
  lptr wb[2] = {sm + L_WO + 4096 + (g * 8 + (r & 7)) * 16, sm + L_WO + 4096 + (g * 8 + (r & 7)) * 16 + 9 * 4608};
  pin(wa[0]); pin(wa[1]); pin(wb[0]); pin(wb[1]);
  rsrc_t ro = mkrs(ws + WS_O, NB_ACT);
  const unsigned ob = rt * 72 * 1024 + g * 1024 + lw * 256 + r * 16;
  const u64 tag = *(LAS u64*)(sm + L_TAG);
  unsigned oo[3];
#pragma unroll
  for (int i = 0; i < 3; ++i) {
    const int it = lane + 64 * i, row = (it * 57) >> 9, c = it - row * 9;
    oo[i] = (i == 2 && lane >= 16) ? OOB : (rt * 64 + 32 * (lw >> 1) + (lw & 1) + 2 * row) * 2304 + ct * 144 + c * 16;
    pin(oo[i]);
  }
  lptr ot = sm + L_OT + lw * 2304 + lane * 16, ow = sm + L_OT + lw * 2304 + r * 144 + g * 8;
  pin(ot); pin(ow);
  reach(L_CNT + 20, 4);
  f4 acc[5];
#pragma unroll 1
  for (int att = 0; att < 4096; ++att) {
    constexpr int PRE = 7, AHEAD = 9;
    bf8 wf[PRE][5];
    auto rdw = [&](int i) {
#pragma unroll
      for (int n = 0; n < 5; ++n)
        wf[i % PRE][n] = *(const LAS bf8*)((n < 4 ? wa[i / 9] + n * 1024 : wb[i / 9]) + (i % 9) * 4608);
    };
#pragma unroll
    for (int i = 0; i < PRE - 1; ++i) rdw(i);
    SB();
    if (lw == 0 && att == 0) {
      wait_flags(ws, F_A + (rt >> 1) * 8 + (lane & 7), tag);
      arrive(L_CNT + 28);
    }
    reach(L_CNT + 28, 1);
    STAMPL(9);
    u4 of[18];
    auto ldo = [&](int i) { of[i] = ld16<SC1>(ro, ob, (i + ROTK * ct < 18 ? i + ROTK * ct : i + ROTK * ct - 18) * 4096); };
#pragma unroll
    for (int i = 0; i < AHEAD; ++i) { ldo(i); SB(); }
#pragma unroll
    for (int n = 0; n < 5; ++n) acc[n] = f4{0.f, 0.f, 0.f, 0.f};
    unsigned mx = 0;
#pragma unroll
    for (int i = 0; i < 18; ++i) {
      if (i + PRE - 1 < 18) rdw(i + PRE - 1);
#pragma unroll
      for (int n = 0; n < 5; ++n) acc[n] = mfma(wf[i % PRE][n], asb(of[i]), acc[n]);
      mx = max4(of[i], mx);
      if (i + AHEAD < 18) ldo(i + AHEAD);
      SGB(0x8, 1); SGB(0x20, 1); SGB(0x100, 1);
#pragma unroll
      for (int n = 1; n < 5; ++n) { SGB(0x8, 1); SGB(0x100, 1); SGB(0x2, 1); }
      SB();
    }
    if (__all(mx != NOTYET)) break;
    asm volatile("" ::: "memory");
  }
  STAMPL(10);
#pragma unroll
  for (int n = 0; n < 5; ++n)
    if (n < 4 || g < 2) *(LAS u2*)(ow + n * 32) = u2{pk(acc[n][0], acc[n][1]), pk(acc[n][2], acc[n][3])};
  WAITL0();
  rsrc_t rout = mkrs(out, 2 * NB_ACT);
#pragma unroll
  for (int i = 0; i < 3; ++i) st16<0>(*(const LAS u4*)(ot + i * 1024), rout, oo[i]);
  STAMPL(15);
}

__global__ __launch_bounds__(512) __attribute__((amdgpu_waves_per_eu(2, 2))) void fused(
    const u16* x, const u16* wqkv, const u16* wo, const u16* q, const u16* k, const u16* v, const u16* shk,
    const u16* hk, const u16* dv, u16* out, char* ws) {
  extern __shared__ __attribute__((aligned(16))) char smem[];
  lptr sm = (lptr)smem;
  const int tid = threadIdx.x, lane = tid & 63, w = __builtin_amdgcn_readfirstlane(tid >> 6);
  const int bid = blockIdx.x;
  u64 rt0, ct0;
  asm volatile("s_memrealtime %0\n\ts_memtime %1" : "=s"(rt0), "=s"(ct0));
  if (tid >= 256 && tid < 288) *(LAS u64*)(size_t)(L_PF - 256 + (tid - 256) * 8) = 0;
  if (bid < 128 && tid < 15) {
    u64 pc;
    asm volatile("s_getpc_b64 %0" : "=s"(pc));
    asm volatile("s_mov_b32 m0, %0\n\ts_nop 0\n\tglobal_load_lds_dword %1, %2"
                 :: "s"(L_PF), "v"(((bid >> 3) + 16 * tid) * 128), "s"(pc & ~127ull) : "memory");
  }
  if (__builtin_expect(bid < 128, 1)) role_a(sm, ws, x, wqkv, bid, w, lane);
  else role_b(sm, ws, x, wqkv, wo, q, k, v, shk, hk, dv, bid, w, lane);
  if (w >= 4) {
    out_proj(sm, ws, out, bid, w & 3, lane);
  } else if (bid < 128 || (bid < 192 && w == 0)) {
    const bool g8 = bid >= 128;
    if (!g8) { arrive(L_CNT + 16); reach(L_CNT + 16, 4); reach(L_CNT + 20, 4); }
    finish(sm, sm + (g8 ? U_OFF : w * 4224), ws, q, out, g8 ? 8 : (bid + YSH) & 7, g8 ? (bid - 128) >> 2 : bid >> 3, g8 ? bid & 3 : w, lane);
    STAMP(13);
  } else if (bid == 128 && w == 2) {
    const u64 tag = *(LAS u64*)(sm + L_TAG);
#pragma unroll 1
    for (int i = 0; i < 4; ++i) wait_flags(ws, i * 64 + lane, tag);
    if (lane == 0) __hip_atomic_store((GAS u64*)ws, tag, RLX_AGENT);
  }
  bar();
  STAMP(11);
  WAITL0();
  if (tid > 0 && tid < 32) ((GAS u64*)(ws + WS_DBG))[blockIdx.x * 32 + tid] = *(LAS u64*)(size_t)(L_PF - 256 + tid * 8);
  if (threadIdx.x == 0) {
    ((GAS u64*)(ws + WS_DBG + 65536))[blockIdx.x * 2] = rt0; ((GAS u64*)(ws + WS_DBG))[blockIdx.x * 32] = ct0;
    ((GAS u64*)(ws + WS_DBG + 65536))[blockIdx.x * 2 + 1] = __builtin_amdgcn_s_memrealtime();
  }
}

__global__ void k_spin(const char* ws){
  const u64* st=(const u64*)(ws+WS_DBG); const u64* rt=(const u64*)(ws+WS_DBG+65536);
  u64 mn=~0ull,mx=0; for(int b=0;b<256;b++){ u64 a=rt[b*2]; mn=a<mn?a:mn; mx=a>mx?a:mx; } u64 d=(mx-mn)*100;
  if(d>1000000) d=1000000;
  u64 s=__builtin_amdgcn_s_memrealtime();
  while(__builtin_amdgcn_s_memrealtime()-s<d) __builtin_amdgcn_s_sleep(8);
}
extern "C" void kernel_launch(void* const* d_in, const int* in_sizes, int n_in, void* d_out, int out_size, void* d_ws,
                              size_t ws_size, hipStream_t stream) {
  (void)hipFuncSetAttribute((const void*)fused, hipFuncAttributeMaxDynamicSharedMemorySize, LDS_BYTES);
  fused<<<256, 512, LDS_BYTES, stream>>>((const u16*)d_in[0], (const u16*)d_in[1], (const u16*)d_in[2], (const u16*)d_in[3],
                                         (const u16*)d_in[4], (const u16*)d_in[5], (const u16*)d_in[6], (const u16*)d_in[7],
                                         (const u16*)d_in[8], (u16*)d_out, (char*)d_ws);
  k_spin<<<1,1,0,stream>>>((const char*)d_ws);
}
```

```cpp
#include <hip/hip_runtime.h>

typedef unsigned short u16;
typedef unsigned long long u64;
typedef short s4 __attribute__((ext_vector_type(4)));
typedef __bf16 bf2 __attribute__((ext_vector_type(2)));
typedef __bf16 bf8 __attribute__((ext_vector_type(8)));
typedef float f2 __attribute__((ext_vector_type(2)));
typedef float f4 __attribute__((ext_vector_type(4)));
typedef unsigned u4 __attribute__((ext_vector_type(4)));
typedef unsigned u2 __attribute__((ext_vector_type(2)));
typedef __amdgpu_buffer_rsrc_t rsrc_t;
#define LAS __attribute__((address_space(3)))
#define GAS __attribute__((address_space(1)))
typedef LAS char* lptr;
#define RLX_AGENT __ATOMIC_RELAXED, __HIP_MEMORY_SCOPE_AGENT
#define DEV __device__ __forceinline__

constexpr int NB_ACT = 2048 * 576 * 2;
constexpr int NB_W = 576 * 1728 * 2;
constexpr int NB_WO = 576 * 576 * 2;
constexpr unsigned OOB = 0x80000000u;
constexpr unsigned NOTYET = 0xffffffffu;
constexpr int SC1 = 16;
#define ROTK 0
#define PFS 12
#define TDELAY 0
#define YSH 0
#define PF0 2
#define HDELAY 6
#define PFA 4
#define PFB 7
#define PDELAY 3
#define EARLYHINT 1
#define HSLOT 0
#define PSLOT 0
#define PF1 9
#define LPRIO 0
#define HSWAP 1
#define XSH 1
#define OSH 2
#define PSLEEP 2
constexpr int SPIN_MAX = 1 << 15;

constexpr int WS_FLAG = 4096;
constexpr int F_A = 0, F_P = 128, F_Y = 256, F_Q = 512, F_W = 640, F_G = 896, F_L = 960;
constexpr int WS_U = 1 << 20;
constexpr int WS_Y = WS_U + (3 << 20);
constexpr int WS_O = WS_Y + (3 << 20);
constexpr int WS_WO = WS_O + (3 << 20);
constexpr int WS_DBG = 16 << 20;

constexpr int LDS_BYTES = 163840;
constexpr int XB = 144 * 128;
constexpr int SLOT = XB + 64 * 432;
constexpr int PS = 1160, HS = 32640;
constexpr int L_WO = 65152;
constexpr int L_OT = L_WO + 83200;
constexpr int H_CP = 4160, U_OFF = 8448, CH_SZ = 13024;
constexpr int L_TAG = L_OT + 10240;
constexpr int L_CNT = L_TAG + 16;
constexpr int L_YA = L_TAG + 128;
constexpr int L_PF = LDS_BYTES - 256;

#define STAMPW(i, wv) do {} while (0)
#define STAMP(i) STAMPW(i, 0)
#define STAMPL(i) STAMPW(i, 4)

DEV rsrc_t mkrs(const void* p, int bytes) { return __builtin_amdgcn_make_buffer_rsrc((void*)p, 0, bytes, 0x00020000); }
DEV u4 mkdesc(const void* p, int bytes) {
  u64 a = (u64)p;
  return u4{(unsigned)a, (unsigned)(a >> 32) & 0xffffu, (unsigned)bytes, 0x00020000u};
}
DEV void dma16(u4 r, unsigned lds, unsigned voff, unsigned soff) {
  asm volatile("s_mov_b32 m0, %0\n\ts_nop 0\n\tbuffer_load_dwordx4 %1, %2, %3 offen lds"
               :: "s"(lds), "v"(voff), "s"(r), "s"(soff) : "memory");
}
DEV void dma16c(u4 r, unsigned lds, unsigned voff, unsigned soff) {
  asm volatile("s_mov_b32 m0, %0\n\ts_nop 0\n\tbuffer_load_dwordx4 %1, %2, %3 offen sc1 lds"
               :: "s"(lds), "v"(voff), "s"(r), "s"(soff) : "memory");
}
DEV void dma16m(u4 r, unsigned lds, unsigned voff, unsigned soff, u64 mask) {
  asm volatile("s_mov_b32 m0, %0\n\ts_mov_b64 exec, %4\n\tbuffer_load_dwordx4 %1, %2, %3 offen lds\n\ts_mov_b64 exec, -1"
               :: "s"(lds), "v"(voff), "s"(r), "s"(soff), "s"(mask) : "memory");
}
template <int AUX> DEV void st16(u4 v, rsrc_t r, unsigned off) { __builtin_amdgcn_raw_buffer_store_b128(v, r, off, 0, AUX); }
template <int AUX> DEV u4 ld16(rsrc_t r, unsigned off, unsigned soff) { return __builtin_amdgcn_raw_buffer_load_b128(r, off, soff, AUX); }
DEV u4 ld16c(u4 r, unsigned voff, unsigned soff) {
  u4 v;
  asm volatile("buffer_load_dwordx4 %0, %1, %2, %3 offen sc1" : "=v"(v) : "v"(voff), "s"(r), "s"(soff) : "memory");
  return v;
}
DEV void touch(u4 r, unsigned voff, unsigned soff) {
  asm volatile("s_mov_b32 m0, %0\n\ts_nop 0\n\tbuffer_load_dword %1, %2, %3 offen lds"
               :: "s"(L_PF), "v"(voff), "s"(r), "s"(soff) : "memory");
}
DEV s4 trread(lptr p) { return __builtin_amdgcn_ds_read_tr16_b64_v4i16((LAS s4*)p); }
DEV bf8 cat8(s4 a, s4 b) { return __builtin_bit_cast(bf8, __builtin_shufflevector(a, b, 0, 1, 2, 3, 4, 5, 6, 7)); }
DEV bf8 asb(u4 v) { return __builtin_bit_cast(bf8, v); }
DEV f4 mfma(bf8 a, bf8 b, f4 c) { return __builtin_amdgcn_mfma_f32_16x16x32_bf16(a, b, c, 0, 0, 0); }
DEV unsigned pk(float lo, float hi) { f2 v = {lo, hi}; return __builtin_bit_cast(unsigned, __builtin_convertvector(v, bf2)); }
DEV float asf(unsigned u) { return __builtin_bit_cast(float, u); }
DEV unsigned asu(float f) { return __builtin_bit_cast(unsigned, f); }
DEV float lo16(unsigned x) { return asf(x << 16); }
DEV float hi16(unsigned x) { return asf(x & 0xffff0000u); }
DEV float dot2(unsigned a, unsigned b, float c) {
  return __builtin_amdgcn_fdot2_f32_bf16(__builtin_bit_cast(bf2, a), __builtin_bit_cast(bf2, b), c, false);
}
DEV float dot2z(unsigned a, unsigned b) {
  float d;
  asm("v_dot2_f32_bf16 %0, %1, %2, 0" : "=v"(d) : "v"(a), "v"(b));
  return d;
}
DEV float dot2a(unsigned a, unsigned b, float c) {
  float d;
  asm("v_dot2_f32_bf16 %0, %1, %2, %3" : "=v"(d) : "v"(a), "v"(b), "v"(c));
  return d;
}
DEV unsigned alignb(unsigned hi, unsigned lo) { return __builtin_amdgcn_alignbit(hi, lo, 16); }
DEV unsigned umax(unsigned a, unsigned b) { return a > b ? a : b; }
DEV unsigned max4(u4 v, unsigned m) { return umax(umax(umax(v[0], v[1]), m), umax(v[2], v[3])); }
DEV unsigned max3u(unsigned a, unsigned b, unsigned c) { return umax(umax(a, b), c); }
DEV unsigned max12(u4 a, u4 b, u4 c) {
  return umax(max3u(max3u(max3u(a[0], a[1], a[2]), a[3], b[0]), b[1], b[2]), max3u(max3u(b[3], c[0], c[1]), c[2], c[3]));
}
DEV float fmax2(float a, float b) { return __builtin_elementwise_maximum(a, b); }
#define WAITV(n) asm volatile("s_waitcnt vmcnt(" #n ")" ::: "memory")
#define WAITL0() asm volatile("s_waitcnt lgkmcnt(0)" ::: "memory")
#define SB() __builtin_amdgcn_sched_barrier(0)
#define SGB(m, n) __builtin_amdgcn_sched_group_barrier(m, n, 0)
DEV void bar() { WAITL0(); __builtin_amdgcn_s_barrier(); asm volatile("" ::: "memory"); }
template <class T> DEV void pin(T& x) { asm volatile("" : "+v"(x)); }

DEV u64 get_tag(char* ws) {
  u64 e;
  asm volatile("s_load_dwordx2 %0, %1, 0x0\n\ts_waitcnt lgkmcnt(0)" : "=s"(e) : "s"(ws) : "memory");
  return e * 6364136223846793005ull + 1442695040888963407ull;
}
DEV void post_flag(char* ws, int idx, u64 tag) { __hip_atomic_store((GAS u64*)(ws + WS_FLAG + idx * 128), tag, RLX_AGENT); }
DEV void wait_flags(char* ws, int idx, u64 tag) {
  GAS u64* p = (GAS u64*)(ws + WS_FLAG + idx * 128);
#pragma unroll 1
  for (int spin = 0; spin < SPIN_MAX; ++spin) {
    if (__all(__hip_atomic_load(p, RLX_AGENT) == tag)) break;
    __builtin_amdgcn_s_sleep(PSLEEP);
  }
  __builtin_amdgcn_fence(__ATOMIC_ACQUIRE, "wavefront");
}

DEV void arrive(unsigned word) {
  unsigned one = 1;
  asm volatile("s_waitcnt lgkmcnt(0)\n\ts_mov_b64 exec, 1\n\tds_add_u32 %0, %1\n\ts_mov_b64 exec, -1"
               :: "v"(word), "v"(one) : "memory");
}
DEV void reach(unsigned word, unsigned n) {
#pragma unroll 1
  for (int spin = 0; spin < (1 << 22); ++spin) {
    unsigned v;
    asm volatile("ds_read_b32 %0, %1\n\ts_waitcnt lgkmcnt(0)" : "=v"(v) : "v"(word) : "memory");
    if ((unsigned)__builtin_amdgcn_readfirstlane(v) >= n) break;
    __builtin_amdgcn_s_sleep(1);
  }
}

DEV u4 ld2(lptr p) {
  const u2 a = *(const LAS u2*)p, b = *(const LAS u2*)(p + HS);
  return u4{a[0], a[1], b[0], b[1]};
}
DEV u64 lds_tag(lptr sm) {
  reach(L_CNT, 1);
  return *(LAS u64*)(sm + L_TAG);
}
DEV void att_consts(lptr sm, int lane, u4 (&tc)[9], int first) {
  const LAS u4* tb = (const LAS u4*)(sm + L_OT) + lane;
#pragma unroll
  for (int i = 0; i < 9; ++i) tc[i] = tb[(i < 4 ? i : i == 4 ? 4 + first : i + 1) * 64];
}

struct Proj { unsigned wa, wb, ob, oo0, oo1, oo2, ot, ow, fl; };
DEV void proj_consts(Proj& p, int bid, int lw, int lane) {
  const int rt = ((bid + OSH) & 7) * 4 + ((bid >> 6) & 1) * 2 + (bid >> 7), ct = (bid >> 3) & 7;
  const int r = lane & 15, g = lane >> 4;
  p.wa = L_WO + lane * 16;
  p.wb = L_WO + 4096 + (g * 8 + (r & 7)) * 16;
  p.ob = rt * 72 * 1024 + g * 1024 + lw * 256 + r * 16;
  unsigned oo[3];
#pragma unroll
  for (int i = 0; i < 3; ++i) {
    const int it = lane + 64 * i, row = (it * 57) >> 9, c = it - row * 9;
    oo[i] = (i == 2 && lane >= 16) ? OOB : (rt * 64 + 32 * (lw >> 1) + (lw & 1)) * 2304 + __umul24(row, 4608) + ct * 144 + c * 16;
  }
  p.oo0 = oo[0]; p.oo1 = oo[1]; p.oo2 = oo[2];
  p.ot = L_OT + lw * 2304 + lane * 16;
  p.ow = L_OT + lw * 2304 + r * 144 + g * 8;
  p.fl = ((rt & 1) ? F_A : F_L) + (rt >> 1) * 8 + (lane & 7);
  pin(p.wa); pin(p.wb); pin(p.ob); pin(p.oo0); pin(p.oo1); pin(p.oo2); pin(p.ot); pin(p.ow); pin(p.fl);
}

template <int HI>
DEV void g1_compute(lptr sm, int n0, int lane, u4 (&tc)[9], int first) {
  const int r = lane & 15, g = lane >> 4;
  lptr xa0 = sm + r * 128 + ((g ^ ((r >> 1) & 7)) << 4);
  lptr xa1 = (lptr)((size_t)xa0 ^ 64);
  lptr wa = sm + XB + (((g >> 1) << 4) + ((g & 1) << 3) + ((r >> 2) << 1)) * 432 + (lane & 3) * 8 + n0 * 32;
#define USED(m, n) (HI ? ((n) > 0 || (m) >= 5) : ((n) < 3 || (m) < 5))
  f4 acc[9][4];
#pragma unroll
  for (int m = 0; m < 9; ++m)
#pragma unroll
    for (int n = 0; n < 4; ++n) acc[m][n] = f4{0.f, 0.f, 0.f, 0.f};
  bf8 xf[9], wf[2][4];
  auto mm = [&](bf8 (&wv)[4], int m) {
#pragma unroll
    for (int n = 0; n < 4; ++n)
      if (USED(m, n)) acc[m][n] = mfma(wv[n], xf[m], acc[m][n]);
  };
  auto rdx = [&](lptr xp, int m) { xf[m] = *(const LAS bf8*)(xp + m * 2048); };
  auto rdw = [&](lptr wp, bf8 (&wv)[4], int n) { wv[n] = cat8(trread(wp + n * 32), trread(wp + 432 + n * 32)); };
  auto mix = [&]() {
#pragma unroll
    for (int i = 0; i < 4; ++i) { SGB(0x8, 1); SGB(0x100, 1); }
    SB();
  };
  auto step = [&](bf8 (&wc)[4], bf8 (&wn)[4], lptr xp, lptr wp, int m0) {
#pragma unroll
    for (int m = m0; m < 9; ++m) {
      mm(wc, m);
      if (m - m0 < 4) rdw(wp, wn, m - m0);
      if (m0 == 0) { if (m > 0) rdx(xp, m - 1); }
      else if (m - m0 < 4) rdx(xp, m - m0);
      else { rdx(xp, 2 * (m - m0) - 4); if (2 * (m - m0) - 3 < m) rdx(xp, 2 * (m - m0) - 3); }
      mix();
    }
#pragma unroll
    for (int m = (m0 == 0 ? 8 : 2 * (9 - m0) - 4); m < 9; ++m) rdx(xp, m);
    SB();
  };
  constexpr int MS = 3;
  bar();
#pragma unroll
  for (int n = 0; n < 4; ++n) rdw(wa, wf[0], n);
#pragma unroll
  for (int m = 0; m < 9; ++m) rdx(xa0, m);
  SB();
  int so = 0;
#pragma unroll 1
  for (int k = 0; k < 8; ++k) {
    step(wf[0], wf[1], xa1 + so, wa + so + 32 * 432, 0);
    so = so == 2 * SLOT ? 0 : so + SLOT;
#pragma unroll
    for (int m = 0; m < MS; ++m) mm(wf[1], m);
    SB();
    bar();
    step(wf[1], wf[0], xa0 + so, wa + so, MS);
  }
  xa1 += 2 * SLOT; wa += 2 * SLOT + 32 * 432;
  pin(xa1); pin(wa);
  STAMP(1);
  lptr d = sm + (2 * n0 + (g >> 1)) * PS + r * 8 + (g & 1) * HS;
  bf8 xg[9];
#pragma unroll
  for (int m = 0; m < 11; ++m) {
    if (m < 9) mm(wf[0], m);
    if (m < 2) { rdw(wa, wf[1], 2 * m); rdw(wa, wf[1], 2 * m + 1); }
    if (m < 9) xg[m] = *(const LAS bf8*)(xa1 + m * 2048);
    if (m >= 2) {
#pragma unroll
      for (int n = 0; n < 4; ++n)
        if (USED(m - 2, n)) acc[m - 2][n] = mfma(wf[1][n], xg[m - 2], acc[m - 2][n]);
    }
    if (m == 7) att_consts(sm, lane, tc, first);
    if (m == 8) arrive(L_CNT + 8);
#pragma unroll
    for (int o = m - 3; o < (m == 10 ? 9 : m - 2); ++o) {
      if (o >= 0) {
#pragma unroll
        for (int n = 0; n < 4; ++n)
          if (USED(o, n))
            *(LAS u2*)(d + n * 2 * PS + o * 128) = u2{pk(acc[o][n][0], acc[o][n][1]), pk(acc[o][n][2], acc[o][n][3])};
      }
    }
    if (m < 3) {
#pragma unroll
      for (int i = 0; i < 4; ++i) { SGB(0x8, 1); SGB(0x100, 2); }
    } else {
      SGB(0x8, 1); SGB(0x100, 1);
#pragma unroll
      for (int i = 0; i < 4; ++i) { SGB(0x8, 1); SGB(0x2, 2); SGB(0x8, 1); SGB(0x200, 1); }
    }
    SB();
  }
}

DEV void role_a(lptr sm, char* ws, const u16* x, const u16* wqkv, int bid, int w, int lane, Proj& pc) {
  const int xcd = bid & 7, j = bid >> 3;
  const int rt = (xcd >> 1) * 4 + (j >> 2), hq = (xcd & 1) * 4 + (j & 3);
  const u4 rp = mkdesc(ws + WS_WO + (j & 7) * 82944, 82944);
  u64 tag;
  u4 tc[9], wr[21];
  const int aw = w ^ 4;
  const int first = rt + (aw >> 1) == 0;
  if (__builtin_expect(w >= 4, 1)) {
    const int lw = w - 4;
    const u4 rx = mkdesc(x, NB_ACT), rw = mkdesc(wqkv, NB_W);
    const int t27 = lane >= 27, cc = lane - 27 * t27, part = (cc >= 9) + (cc >= 18);
    const unsigned vw = t27 * (4 * 3456) + part * (1152 - 144) + cc * 16 + hq * 144;
    auto wpiece = [&](unsigned d, int kt, int i) {
      dma16m(rw, d + XB + (lw * 8 + i) * 864, vw, (kt * 64 + lw * 16 + 8 * (i >> 2) + (i & 3)) * 3456, (1ull << 54) - 1);
    };
    auto tailp = [&](unsigned d, int kt) {
#pragma unroll
      for (int i = 2; i < 8; ++i) wpiece(d, kt, i);
    };
    STAMPL(16);
    tailp(0, 0);
    SB();
    unsigned vx[6];
#pragma unroll
    for (int i = 0; i < 6; ++i) {
      const int xr = lw * 36 + i * 6 + (lane >> 3);
      vx[i] = __umul24(rt * 128 + xr, 1152) + (((lane & 7) ^ ((xr >> 1) & 7)) << 4) - 16 * 1152;
    }
    auto headp = [&](unsigned d, int kt) {
#pragma unroll
      for (int i = 0; i < 6; ++i) dma16m(rx, d + (lw * 6 + i) * 768, vx[i], kt * 128, (1ull << 48) - 1);
#pragma unroll
      for (int i = 0; i < 2; ++i) wpiece(d, kt, i);
    };
    headp(0, 0);
    unsigned d = SLOT;
#pragma unroll 1
    for (int k = 1; k < 9; ++k) {
      headp(d, k);
      WAITV(8);
      bar();
      STAMPL(16 + k);
      tailp(d, k);
      d = d == 2 * SLOT ? 0 : d + SLOT;
    }
    STAMPL(26);
    tag = *(LAS u64*)(sm + L_TAG);
    att_consts(sm, lane, tc, first);
    proj_consts(pc, bid, lw, lane);
    if (lw == 3) wait_flags(ws, F_W + (j & 7) * 32 + (lane < 18 ? lane : 0), tag);
    WAITV(0);
    bar();
    STAMPL(27);
    GAS u64* fq = (GAS u64*)(ws + WS_FLAG + (F_Q + rt * 8 + (lane & 7)) * 128);
    u64 fv = tag;
    if (lw == 0) asm volatile("global_load_dwordx2 %0, %1, off sc1" : "=v"(fv) : "v"(fq) : "memory");
#pragma unroll
    for (int i = 0; i < 21; ++i) wr[i] = ld16c(rp, lane * 16, (4 * i + lw) * 1024);
    if (lw == 0) {
      asm volatile("s_waitcnt vmcnt(21)" : "+v"(fv) :: "memory");
      if (!__all(fv == tag)) wait_flags(ws, F_Q + rt * 8 + (lane & 7), tag);
      arrive(L_CNT + 4);
    }
    __builtin_amdgcn_s_setprio(LPRIO);
    STAMPL(28);
  } else {
    tag = get_tag(ws);
    if (threadIdx.x == 0) { *(LAS u64*)(sm + L_TAG) = tag; *(LAS u4*)(sm + L_CNT) = u4{0, 0, 0, 0}; *(LAS u4*)(sm + L_CNT + 16) = u4{0, 0, 0, 0}; *(LAS u4*)(sm + L_CNT + 32) = u4{0, 0, 0, 0}; }
    {
      const int r = lane & 15, g = lane >> 4, q4 = r >> 2, p4 = lane & 3;
      LAS u4* tb = (LAS u4*)(sm + L_OT) + lane;
      u4 t0, t1;
      if (w < 2) {
#pragma unroll
        for (int dd = 0; dd < 4; ++dd) {
          t0[dd] = __builtin_amdgcn_sbfe((int)(0x1ffu << (2 * w)), 4 * g + dd, 1);
          t1[dd] = __builtin_amdgcn_sbfe((int)(0x3feu << (2 * w)), 4 * g + dd, 1);
        }
      } else if (w == 2) {
#pragma unroll
        for (int dd = 0; dd < 4; ++dd) {
          const int kk = 4 * g + dd - r;
          t0[dd] = (kk >= 1 && kk <= 8) ? 0u : 0xff800000u;
          t1[dd] = (kk >= 1 && kk <= 8 && g >= 2) ? 0u : 0xff800000u;
        }
      } else {
#pragma unroll
        for (int dd = 0; dd < 4; ++dd) t0[dd] = 4 * g + dd - r <= -8 ? 0u : 0xff800000u;
        t1[0] = (t0[0] ? 0u : 0xffffu) | (t0[1] ? 0u : 0xffff0000u);
        t1[1] = (t0[2] ? 0u : 0xffffu) | (t0[3] ? 0u : 0xffff0000u);
        t1[2] = g * PS + r * 16;
        t1[3] = g * PS + (r & 7) * 16;
        const int kr0 = (4 * g + q4) * 16, kr1 = kr0 + (g >= 2 ? 128 : 256);
        const int ve = (p4 >> 1) * PS + (p4 & 1) * HS, vo = ((p4 + 1) >> 1) * PS + ((p4 + 1) & 1) * HS;
        tb[8 * 64] = u4{(unsigned)(kr0 + ve), (unsigned)(kr1 + ve), (unsigned)(kr0 + vo), (unsigned)(kr1 + vo)};
        const unsigned os = (rt * 144 + hq * 9 + g) * 1024 + r * 16;
        tb[9 * 64] = u4{(unsigned)kr0, (unsigned)kr1, os, g ? OOB : os + 8192};
      }
      tb[2 * w * 64] = t0;
      tb[(2 * w + 1) * 64] = t1;
    }
#pragma unroll
    for (int i = 0; i < 21; ++i) wr[i] = __builtin_nondeterministic_value(wr[i]);
    if (w & 1) g1_compute<1>(sm, 3 + (w >> 1) * 7, lane, tc, first);
    else g1_compute<0>(sm, (w >> 1) * 7, lane, tc, first);
  }
  {
    const unsigned rb = (32 * (aw >> 1) + (aw & 1)) * 8;
    const u4 qm[4] = {tc[0], tc[1], tc[2], tc[3]}, u0 = tc[4], u1 = tc[5], c0 = tc[6], c1 = tc[7], c2 = tc[8];
    const f4 b0 = __builtin_bit_cast(f4, u0), b1 = __builtin_bit_cast(f4, u1);
    const unsigned m01 = c0[0], m23 = c0[1];
    lptr qa = sm + rb + c0[2], k1 = sm + rb + c0[3];
    lptr ve0 = sm + rb + c1[0], ve1 = sm + rb + c1[1], vo0 = sm + rb + c1[2], vo1 = sm + rb + c1[3];
    lptr vk0 = sm + rb + c2[0], vk1 = sm + rb + c2[1];
    const unsigned ow = (aw >> 2) * 73728 + (aw & 3) * 256;
    unsigned os0 = c2[2] + ow, os1 = os0 + 4096, os2 = c2[3] + ow;
    const bool g0 = lane < 16;
    rsrc_t ro = mkrs(ws + WS_O, NB_ACT);
    u4 ones = {0x3f803f80u, 0x3f803f80u, 0x3f803f80u, 0x3f803f80u};
    pin(qa); pin(k1); pin(ve0); pin(ve1); pin(vo0); pin(vo1); pin(vk0); pin(vk1); pin(os0); pin(os1); pin(os2); pin(ones);
    STAMP(14);
    if (w < 4) arrive(L_CNT);
    reach(w < 4 ? L_CNT : L_CNT + 8, 4);
    STAMP(2);
    STAMPL(29);
    unsigned cl;
    asm volatile("ds_read_b32 %0, %1" : "=v"(cl) : "v"(L_CNT + 4) : "memory");
    const float cs = 0.23570226039551584f * 1.4426950408889634f;
    const f4 z = {0.f, 0.f, 0.f, 0.f};
    u4 qf[4];
    bf8 ka[4], kb[4], vt0[4], vt1[4];
#pragma unroll
    for (int h = 0; h < 4; ++h) {
      qf[h] = ld2(qa + 2 * h * PS + 128);
      ka[h] = asb(ld2(qa + (9 + 2 * h) * PS));
      kb[h] = asb(ld2(k1 + (9 + 2 * h) * PS + 256));
      SB();
    }
#pragma unroll
    for (int h = 0; h < 4; ++h) {
      vt0[h] = cat8(trread((h < 2 ? ve0 : vo0) + (18 + 2 * h) * PS), trread((h < 2 ? ve1 : vo1) + (18 + 2 * h) * PS));
      vt1[h] = cat8(trread(vk0 + (20 + 2 * h) * PS + (h < 2 ? 0 : HS)), trread(vk1 + (20 + 2 * h) * PS + (h < 2 ? 0 : HS)));
    }
    SB();
    f4 s0[4], s1[4], s[4];
    float mx[4];
#pragma unroll
    for (int h = 0; h < 4; ++h) {
      bf8 qq = asb(qf[h] & qm[h]);
      s0[h] = mfma(ka[h], qq, b0);
      s1[h] = mfma(kb[h], qq, b1);
      SB();
    }
#pragma unroll
    for (int h = 0; h < 4; ++h) {
#pragma unroll
      for (int dd = 0; dd < 4; ++dd) s[h][dd] = fmax2(s0[h][dd], s1[h][dd]);
      mx[h] = fmax2(fmax2(fmax2(s[h][0], s[h][1]), s[h][2]), s[h][3]);
      SB();
    }
    {
      auto a = __builtin_amdgcn_permlane16_swap(asu(mx[0]), asu(mx[1]), false, false);
      auto b = __builtin_amdgcn_permlane16_swap(asu(mx[2]), asu(mx[3]), false, false);
      auto c = __builtin_amdgcn_permlane32_swap(asu(fmax2(asf(a[0]), asf(a[1]))), asu(fmax2(asf(b[0]), asf(b[1]))), false, false);
      unsigned m = asu(-cs * fmax2(asf(c[0]), asf(c[1])));
      auto e = __builtin_amdgcn_permlane32_swap(m, m, false, false);
      unsigned e0 = e[0], e1 = e[1];
      auto f0 = __builtin_amdgcn_permlane16_swap(e0, e0, false, false);
      auto f1 = __builtin_amdgcn_permlane16_swap(e1, e1, false, false);
      mx[0] = asf(f0[0]); mx[1] = asf(f0[1]); mx[2] = asf(f1[0]); mx[3] = asf(f1[1]);
    }
    SB();
    asm volatile("s_waitcnt vmcnt(0)" : "+v"(wr[0]), "+v"(wr[1]), "+v"(wr[2]), "+v"(wr[3]), "+v"(wr[4]), "+v"(wr[5]),
                 "+v"(wr[6]), "+v"(wr[7]), "+v"(wr[8]), "+v"(wr[9]), "+v"(wr[10]), "+v"(wr[11]), "+v"(wr[12]),
                 "+v"(wr[13]), "+v"(wr[14]), "+v"(wr[15]), "+v"(wr[16]), "+v"(wr[17]), "+v"(wr[18]), "+v"(wr[19]),
                 "+v"(wr[20]) :: "memory");
    asm volatile("s_waitcnt lgkmcnt(0)" : "+v"(cl) :: "memory");
    if (__builtin_amdgcn_readfirstlane(cl) == 0) reach(L_CNT + 4, 1);
    if ((threadIdx.x & 255) == 192) post_flag(ws, (w < 4 ? F_A : F_L) + rt * 8 + hq, tag);
    f4 sum[4], o0[4], o1[4];
#pragma unroll
    for (int h = 0; h < 4; ++h) {
      float p[4];
#pragma unroll
      for (int dd = 0; dd < 4; ++dd) p[dd] = __builtin_amdgcn_exp2f(__builtin_fmaf(s[h][dd], cs, mx[h]));
      unsigned p01 = pk(p[0], p[1]), p23 = pk(p[2], p[3]);
      bf8 pf = asb(u4{p01 & ~m01, p23 & ~m23, p01 & m01, p23 & m23});
      sum[h] = mfma(asb(ones), pf, z);
      o0[h] = mfma(vt0[h], pf, z);
      o1[h] = mfma(vt1[h], pf, z);
      SB();
    }
    u2 gr[4];
    u4 left;
#pragma unroll
    for (int h = 0; h < 4; ++h) {
      const float inv = __builtin_amdgcn_rcpf(sum[h][0]);
      const unsigned a0 = pk(o0[h][0] * inv, o0[h][1] * inv), a1 = pk(o0[h][2] * inv, o0[h][3] * inv);
      const unsigned e0 = pk(o1[h][0] * inv, o1[h][1] * inv);
      if (h & 1) {
        const unsigned e1 = pk(o1[h][2] * inv, o1[h][3] * inv);
        left[h] = a1;
        gr[h] = u2{g0 ? e0 : a0, g0 ? e1 : a1};
        st16<SC1>(u4{gr[h - 1][0], gr[h - 1][1], gr[h][0], gr[h][1]}, ro, h == 1 ? os0 : os1);
        SB();
      } else {
        left[h] = e0;
        gr[h] = u2{a0, a1};
      }
    }
    st16<SC1>(left, ro, os2);
  }
  STAMP(3);
  STAMPL(4);
  if (w >= 4) {
    __builtin_amdgcn_s_setprio(0);
    u64 fv = 0;
    GAS u64* fp = (GAS u64*)(ws + WS_FLAG + pc.fl * 128);
#pragma unroll
    for (int i = 0; i < 21; ++i) {
      if (i < 20 || w == 4) *(LAS u4*)(sm + L_WO + (4 * i + w - 4) * 1024 + lane * 16) = wr[i];
      if (i == 10 && w == 5) asm volatile("global_load_dwordx2 %0, %1, off sc1" : "+v"(fv) : "v"(fp) : "memory");
    }
    arrive(L_CNT + 20);
    if (w == 5) {
      asm volatile("s_waitcnt vmcnt(0)" : "+v"(fv) :: "memory");
      if (!__all(fv == tag)) wait_flags(ws, pc.fl, tag);
      arrive(L_CNT + 28);
    }
    STAMPL(30);
  }
}

DEV void role_b(lptr sm, char* ws, const u16* x, const u16* wqkv, const u16* wo, const u16* q, const u16* k, const u16* v,
                const u16* shk, const u16* hk, const u16* dv, int bid, int w, int lane) {
  const int tid = threadIdx.x, xcd = bid & 7, jb = (bid >> 3) - 16, me = xcd * 16 + jb;
  const int G = lane >> 4, i16 = lane & 15, w4 = w & 3;
  rsrc_t ru = mkrs(ws + WS_U, NB_ACT);
  const int xc = (xcd + XSH) & 7, cm = xc * 16 + jb;
  const int c5 = 512 + xcd * 8 + (jb & 7);
  const unsigned tb = L_WO + w * 9216;
  const u4 ny = {NOTYET, NOTYET, NOTYET, NOTYET};
  rsrc_t ry = mkrs(ws + WS_Y, NB_ACT);
  u64 tag;
  if (w < 4 || (w == 4 && jb < 8)) {
    const int grp = w < 4 ? xcd : 8, un = w < 4 ? jb * 4 + w : xcd * 8 + jb;
    const u4 rk = mkdesc(k, NB_ACT), rv = mkdesc(v, NB_ACT);
    const unsigned o0 = (un * 32 + (lane >> 3)) * 1152 + grp * 128 + (((lane & 7) ^ ((lane >> 4) & 1)) << 4);
    dma16(rk, tb, o0 - 8 * 1152, 0);
#pragma unroll
    for (int i = 0; i < 4; ++i) {
      dma16(rk, tb + 1024 + i * 1024, o0 ^ (((i + 1) & 3) << 5), i * 8 * 1152);
      dma16(rv, tb + 5120 + i * 1024, o0 ^ (i << 5), i * 8 * 1152);
      if (i == 0) dma16m(mkdesc(shk, 4608), L_OT + w * 512, lane * 16, grp * 512, 0xffffffffull);
    }
    const int q4 = i16 >> 2, p4 = lane & 3;
    const int f0 = (q4 >> 1) | (p4 << 1), f1 = (q4 >> 1) | (((p4 + 1) & 3) << 1);
    const unsigned a0 = tb + (8 * p4 + q4) * 128 + (((G >> 1) ^ f0) << 4) + (G & 1) * 8;
    const unsigned a1 = tb + (8 * p4 + q4) * 128 + (((G >> 1) ^ f1) << 4) + (G & 1) * 8;
    lptr wp = sm + L_OT + w * 512 + (G * 4 + (i16 & 3)) * 8;
    unsigned off = ((grp * 16 + G) * 256 + 4 * un) * 64 + i16 * 16;
    u2 K[3], V[2], wk;
    auto rd = [&](int s) {
      lptr b0 = (lptr)(size_t)(a0 ^ (s << 5)), b1 = (lptr)(size_t)(a1 ^ (s << 5));
      wk = *(LAS u2*)(wp + s * 128);
      K[0] = __builtin_bit_cast(u2, trread(b0 + 512));
      K[1] = __builtin_bit_cast(u2, trread(b1 + 1024));
      K[2] = __builtin_bit_cast(u2, trread(b1 + 1536));
      V[0] = __builtin_bit_cast(u2, trread(b0 + 5120));
      V[1] = __builtin_bit_cast(u2, trread(b0 + 5120 + 512));
    };
    WAITV(0);
    if (w < 4) arrive(L_CNT + 44);
    STAMP(1);
    rd(0);
#pragma unroll 1
    for (int s = 0; s < 4; ++s) {
      const unsigned wA = alignb(wk[1], wk[1]), wB = alignb(wk[0], wk[0]);
      const unsigned kk[6] = {K[0][0], K[0][1], K[1][0], K[1][1], K[2][0], K[2][1]};
      const unsigned vv[4] = {V[0][0], V[0][1], V[1][0], V[1][1]};
      rd((s + 1) & 3);
      unsigned sh[5];
      float ev[4], od[4], ve[4], vo[4];
#pragma unroll
      for (int d = 0; d < 5; ++d) sh[d] = alignb(kk[d + 1], kk[d]);
      SB();
#pragma unroll
      for (int d = 0; d < 4; ++d) { ev[d] = dot2z(sh[d], wA); od[d] = dot2z(kk[d + 1], wA); }
      SB();
#pragma unroll
      for (int d = 0; d < 4; ++d) { ev[d] = dot2a(sh[d + 1], wB, ev[d]); od[d] = dot2a(kk[d + 2], wB, od[d]); }
      SB();
#pragma unroll
      for (int d = 0; d < 4; ++d) { ve[d] = lo16(vv[d]); vo[d] = hi16(vv[d]); pin(ve[d]); pin(vo[d]); }
      SB();
      u4 o;
#pragma unroll
      for (int d = 0; d < 4; ++d) o[3 - d] = pk(od[d] * vo[d], ev[d] * ve[d]);
      st16<SC1>(o, ru, off);
      off += 65536;
      SB();
    }
    STAMP(2);
    if (w < 4) arrive(L_CNT + 40);
    tag = lds_tag(sm);
    WAITV(0);
    if (w < 4) arrive(L_CNT + 36);
    else if (lane == 0) post_flag(ws, F_G + un, tag);
  } else if (w == 7) {
    const u4 rx = mkdesc(x, NB_ACT), rw = mkdesc(wqkv, NB_W);
    const int kr = (lane * 4370) >> 16, cc = lane - kr * 15, part = (cc * 52) >> 8;
    const unsigned fw = lane < 60 ? (jb * 4 + kr) * 3456 + part * (1152 - 640) + ((xcd & 1) * 4 + cc) * 128 : OOB;
    const unsigned fx = lane < 33 ? ((xcd >> 1) * 512 - 16 + jb * 33 + lane) * 1152 : OOB;
    __builtin_amdgcn_s_setprio(3);
    __builtin_amdgcn_s_sleep(PDELAY);
#pragma unroll 1
    for (int st = 1; st < 9; ++st) {
      if (st == PF1) reach(L_CNT + 44, 4);
      if (st == PFA) reach(L_CNT + 40, 4);
      if (st == PFB) reach(L_CNT + 4, 1);
      touch(rw, fw, st * 64 * 3456);
      touch(rx, fx, st * 128);
    }
    STAMPW(23, 7);
    reach(L_CNT + 4, 1);
    const u4 rq = mkdesc(q, NB_ACT);
    touch(rq, (jb * 128 + lane) * 1152 + ((xcd + YSH) & 7) * 128, 0);
    touch(rq, (jb * 128 + lane) * 1152 + ((xcd + YSH) & 7) * 128, 64 * 1152);
    if (jb >= 8) {
      const int un = bid - 192;
      touch(rq, lane < 32 ? ((un >> 2) * 128 + 64 * (un & 1) + 8 * ((un >> 1) & 1) + (lane >> 3) * 16 + (lane & 7)) * 1152 + 1024 : OOB, 0);
    }
    const unsigned y4 = (xc * 64 + G) * 4096 + jb * 256 + i16 * 16;
#pragma unroll 1
    for (int i = 0; i < 16; i += 2) { st16<SC1>(ny, ry, y4 + i * 16384); st16<SC1>(ny, ry, y4 + i * 16384 + 16384); }
    if (jb >= 8) {
#pragma unroll
      for (int i = 0; i < 4; ++i) st16<SC1>(ny, ry, (512 + i * 16 + (lane >> 2)) * 4096 + (c5 & 63) * 64 + (lane & 3) * 16);
    }
    tag = lds_tag(sm);
    WAITV(0);
    arrive(L_CNT + 16);
    if (lane == 0) post_flag(ws, F_Y + cm, tag);
    STAMPW(25, 7);
    __builtin_amdgcn_s_sleep(12);
    if (jb >= 8) __builtin_amdgcn_s_sleep(16);
    wait_flags(ws, F_Y + (jb < 8 ? xcd * 16 + i16 : (lane >> 3) * 16 + 8 + (lane & 7)), tag);
    arrive(L_CNT + 24);
  } else if (w == 6) {
    __builtin_amdgcn_s_sleep(HDELAY);
    const u4 rh = mkdesc(hk, NB_ACT);
    const int nh = jb < 8 ? 4 : 5;
#pragma unroll 1
    for (int c = 0; c < nh; ++c) {
#pragma unroll
      for (int i = 0; i < 4; ++i) dma16(rh, c * CH_SZ + H_CP + 32 + i * 1024, lane * 16, (c < 4 ? cm * 4 + c : c5) * 4096 + i * 1024);
    }
    if (lane < 34) {
#pragma unroll
      for (int i = 0; i < 5; ++i)
        *(LAS u4*)(sm + i * CH_SZ + (lane < 30 ? U_OFF + lane * 16 : ((lane >> 1) & 1) * H_CP + (lane & 1) * 16)) = u4{0, 0, 0, 0};
    }
    WAITV(0);
#pragma unroll 1
    for (int c = 0; c < nh; ++c) {
      lptr hb = sm + c * CH_SZ + 32 + lane * 16;
      u4 ha[4];
      unsigned hp[4];
#pragma unroll
      for (int i = 0; i < 4; ++i) { ha[i] = *(LAS u4*)(hb + H_CP + i * 1024); hp[i] = *(LAS unsigned*)(hb + H_CP - 4 + i * 1024); }
#pragma unroll
      for (int i = 0; i < 4; ++i)
        *(LAS u4*)(hb + i * 1024) =
            u4{alignb(ha[i][0], hp[i]), alignb(ha[i][1], ha[i][0]), alignb(ha[i][2], ha[i][1]), alignb(ha[i][3], ha[i][2])};
    }
    arrive(L_CNT + 8);
    STAMPW(18, 6);
    reach(L_CNT + 4, 1);
    __builtin_amdgcn_s_setprio(2);
    __builtin_amdgcn_s_sleep(2);
    rsrc_t ro = mkrs(ws + WS_O, NB_ACT);
#pragma unroll 1
    for (int i = 0; i < 18; i += 3) {
#pragma unroll
      for (int e = 0; e < 3; ++e) st16<SC1>(ny, ro, (me * 18 + i) * 1024 + lane * 16 + e * 1024);
    }
    tag = lds_tag(sm);
    WAITV(0);
    if (lane == 0) post_flag(ws, F_Q + me, tag);
    STAMPW(22, 6);
  } else {
    const int slot = w == 5 ? jb : 2 + jb;
    if (w == 5) {
      tag = get_tag(ws);
      if (lane == 0) *(LAS u64*)(sm + L_TAG) = tag;
      arrive(L_CNT);
    } else {
      tag = lds_tag(sm);
    }
    if (w == 5 || jb >= 14) {
      const u4 rwo = mkdesc(wo, NB_WO);
      const int e4 = (lane * 57) >> 9;
      const unsigned vb = (e4 >> 1) * 1152 + xcd * 144 + (lane - e4 * 9) * 16, vt = (e4 & 1) * 1152;
      int ks = slot + ROTK * xcd;
      if (ks >= 18) ks -= 18;
#pragma unroll 1
      for (int i = 0; i < 8; ++i) {
        const int ci = 4 * ks + (i >> 1), h = (ci * 57) >> 9, c = ci - 9 * h;
        const int row = h * 72 + (c == 8 ? 16 : 36 * (c >> 2) + 4 * (c & 3)) + 2 * (i & 1);
        dma16m(rwo, tb + i * 576, vb + __umul24(vt, c == 8 ? 36 : (c & 3) ? 16 : 32), row * 1152, (1ull << 36) - 1);
      }
      WAITV(0);
      rsrc_t rpw = mkrs(ws + WS_WO, NB_WO);
      lptr p = (lptr)(size_t)tb + (G * 8 + ((i16 >> 2) << 1)) * 144 + (lane & 3) * 8;
      const unsigned po = (xcd * 18 + slot) * 4608;
      u4 f[5];
#pragma unroll
      for (int n = 0; n < 5; ++n) f[n] = __builtin_bit_cast(u4, cat8(trread(p + n * 32), trread(p + 144 + n * 32)));
#pragma unroll
      for (int n = 0; n < 4; ++n) st16<SC1>(f[n], rpw, po + n * 1024 + lane * 16);
      st16<SC1>(f[4], rpw, i16 < 8 ? po + 4096 + (G * 8 + i16) * 16 : OOB);
      WAITV(0);
      if (lane == 0) post_flag(ws, F_W + xcd * 32 + slot, tag);
      STAMPW(21, 5);
    }
  }
  const int r = lane & 15, g = lane >> 4;
  if (w >= 4) {
    if (w < 6) {
      const int ct = (bid >> 3) & 7;
      __builtin_amdgcn_s_setprio(2);
      if (w == 5) {
        reach(L_CNT + 4, 1);
        wait_flags(ws, F_W + ct * 32 + (lane < 18 ? lane : 0), tag);
      } else if (jb >= 8) {
        reach(L_CNT + 4, 1);
        wait_flags(ws, F_G + lane, tag);
        u4 u5[4];
#pragma unroll
        for (int i = 0; i < 4; ++i) u5[i] = ld16<SC1>(ru, lane * 64 + (c5 & 3) * 16, (c5 >> 2) * 16384 + i * 4096);
#pragma unroll
        for (int i = 0; i < 4; ++i) *(LAS u4*)(sm + U_OFF + 480 + 4 * CH_SZ + lane * 16 + i * 1024) = u5[i];
      }
      if (w == 4) arrive(L_CNT + 32);
      arrive(L_CNT + 44);
      reach(L_CNT + 44, 6);
      reach(L_CNT + 8, 5);
      STAMPL(16);
      const u4 rp = mkdesc(ws + WS_WO + ct * 82944, 82944);
#pragma unroll 1
      for (int i = (w & 1) * 1024; i < 81 * 1024; i += 2048) dma16c(rp, L_WO + i, lane * 16, i);
      WAITV(0);
      STAMPL(17);
    }
    arrive(L_CNT + 20);
    __builtin_amdgcn_s_setprio(0);
    return;
  }
  typedef const __attribute__((address_space(4))) unsigned* cptr;
  const unsigned dv0 = ((cptr)dv)[cm * 2 + (w >> 1)], dv1 = ((cptr)dv)[c5 >> 1];
  if (w == 3) {
    reach(L_CNT + 36, 4);
    if (lane == 0) post_flag(ws, F_P + me, tag);
    STAMPW(20, 3);
  }
  STAMP(3);
  if (w == 0) {
    __builtin_amdgcn_s_sleep(4);
    wait_flags(ws, F_P + xc * 16 + i16, tag);
    arrive(L_CNT + 12);
  }
  reach(L_CNT + 12, 1);
  STAMP(4);
  {
    u4 uv[4];
#pragma unroll
    for (int i = 0; i < 4; ++i) uv[i] = ld16<SC1>(ru, tid * 16, cm * 16384 + i * 4096);
    SB();
    if (w == 3) arrive(L_CNT + 4);
#pragma unroll
    for (int i = 0; i < 4; ++i) *(LAS u4*)(sm + U_OFF + 480 + (tid & 3) * CH_SZ + (tid >> 2) * 16 + i * 1024) = uv[i];
  }
  arrive(L_CNT + 8);
  const int xd = 15 - r - 8 * g;
#pragma unroll 1
  for (int rnd = 0; rnd < (jb >= 8 ? 2 : 1); ++rnd) {
    const int dd0 = rnd ? 2 * w : 0, ch = rnd ? c5 : cm * 4 + w;
    const unsigned dval = dd0 == 0 ? ((rnd ? dv1 : dv0) >> ((ch & 1) * 16)) & 0xffffu : 0;
    lptr cb = sm + (rnd ? 4 : w) * CH_SZ;
    lptr aa[4];
#pragma unroll
    for (int i = 0; i < 4; ++i) { aa[i] = cb + (r & 1) * H_CP + 4 + 4 * (4 * g + (r >> 1)) + dd0 * 64 + i * 1024; pin(aa[i]); }
    lptr ba = cb + U_OFF + 16 * (31 + 2 * r - g) - 128 - dd0 * 64;
    pin(ba);
    u4 ad;
#pragma unroll
    for (int d = 0; d < 4; ++d) ad[d] = (xd >> 1) == d ? dval << ((xd & 1) * 16) : 0u;
    pin(ad);
    reach(rnd ? L_CNT + 32 : L_CNT + 8, rnd ? 1 : 5);
    STAMP(rnd ? 19 : 5);
    f4 acc[8];
    bf8 A[2][8], B[2][8];
    auto rd = [&](int dd, bf8 (&a)[8], bf8 (&b)[8]) {
#pragma unroll
      for (int i = 0; i < 8; ++i) {
        const LAS unsigned* p = (const LAS unsigned*)(aa[i >> 1] + dd * 64 + (i & 1) * 512);
        a[i] = asb(u4{p[0], p[1], p[2], p[3]});
        b[i] = *(const LAS bf8*)(ba + 128 - dd * 64 + i * 512);
      }
    };
    auto mm = [&](bf8 (&a)[8], bf8 (&b)[8]) {
#pragma unroll
      for (int i = 0; i < 8; ++i)
#pragma unroll
        for (int jj = 0; jj + i < 8; ++jj) acc[i + jj] = mfma(a[i], b[jj], acc[i + jj]);
#pragma unroll
      for (int i = 0; i < 24; ++i) { SGB(0x8, 1); SGB(0x100, 1); }
      SGB(0x8, 12);
      SB();
    };
    rd(0, A[0], B[0]);
#pragma unroll
    for (int n = 0; n < 8; ++n) acc[n] = mfma(asb(ad), B[0][n], f4{0.f, 0.f, 0.f, 0.f});
    SB();
#pragma unroll 1
    for (int it = rnd * 3; it < 4; ++it) {
      rd(1, A[1], B[1]);
      mm(A[0], B[0]);
      rd(2, A[0], B[0]);
      mm(A[1], B[1]);
#pragma unroll
      for (int i = 0; i < 4; ++i) aa[i] += 128;
      ba -= 128;
    }
    STAMP(6 + rnd);
    const unsigned yo = (((ch >> 6) * 16 + 2 * (g & 1) + (r >> 3)) * 4 + ((r >> 2) & 1) + 2 * (g >> 1)) * 4096 + (ch & 63) * 64 + (r & 3) * 16;
    auto put = [&](f4 x, f4 y, int a) {
      auto s0 = __builtin_amdgcn_permlane16_swap(pk(x[0], x[1]), pk(y[0], y[1]), false, false);
      auto s1 = __builtin_amdgcn_permlane16_swap(pk(x[2], x[3]), pk(y[2], y[3]), false, false);
      st16<SC1>(u4{s0[0], s1[0], s0[1], s1[1]}, ry, yo + a * 65536);
    };
    if (rnd == 0) {
      reach(L_CNT + 16, 1);
#pragma unroll
      for (int a = 0; a < 4; ++a) put(acc[2 * a], acc[2 * a + 1], a);
    } else {
      LAS f4* mine = (LAS f4*)(sm + w * CH_SZ + lane * 16);
#pragma unroll
      for (int n = 0; n < 8; ++n) mine[n * 64] = acc[n];
      arrive(L_CNT + 16);
      reach(L_CNT + 16, 5);
      constexpr int CS = CH_SZ / 16;
      LAS f4* p = (LAS f4*)(sm + lane * 16) + w * 128;
      put((p[0] + p[CS]) + (p[2 * CS] + p[3 * CS]), (p[64] + p[CS + 64]) + (p[2 * CS + 64] + p[3 * CS + 64]), w);
    }
  }
  STAMP(8);
}

DEV void finish(lptr sm, lptr yt, char* ws, const u16* q, u16* out, int grp, int idx, int w4, int lane) {
  rsrc_t ry = mkrs(ws + WS_Y, NB_ACT), rq = mkrs(q, NB_ACT), rout = mkrs(out, 2 * NB_ACT);
  const int orow = idx * 128 + 64 * (w4 & 1) + 8 * (w4 >> 1) + (lane >> 3);
  const unsigned two = grp == 8 ? OOB : 0;
  const unsigned qo = orow * 1152 + grp * 128 + (lane & 7) * 16, yo = (grp * 16 + idx) * 16384 + w4 * 4096 + lane * 16;
  u4 qv[8], yv[8];
#pragma unroll
  for (int i = 0; i < 8; ++i) qv[i] = ld16<0>(rq, i < 4 ? qo : qo | two, ((i & 3) * 16 + (i >> 2) * 1024) * 1152);
  reach(L_CNT + 24, 1);
  asm volatile("s_waitcnt vmcnt(0)" : "+v"(qv[0]), "+v"(qv[1]), "+v"(qv[2]), "+v"(qv[3]), "+v"(qv[4]), "+v"(qv[5]),
               "+v"(qv[6]), "+v"(qv[7]) :: "memory");
#pragma unroll 1
  for (int spin = 0; spin < SPIN_MAX; ++spin) {
    unsigned mx = 0;
#pragma unroll
    for (int i = 0; i < 8; ++i) yv[i] = ld16<SC1>(ry, i < 4 ? yo : yo | two, (i & 3) * 1024 + (i >> 2) * 131072);
#pragma unroll
    for (int i = 0; i < 8; ++i) mx = max3u(max3u(mx, yv[i][0], yv[i][1]), yv[i][2], yv[i][3]);
    if (__all(mx != NOTYET)) break;
    asm volatile("s_sleep 2" ::: "memory");
  }
  STAMP(12);
  unsigned oo = orow * 2304 + 1152 + grp * 128 + (lane & 7) * 16;
#pragma unroll
  for (int i = 0; i < 8; ++i) *(LAS u4*)(yt + (lane >> 5) * 528 + (lane & 31) * 16 + i * 1056) = yv[i];
  WAITL0();
  const LAS u16* p = (const LAS u16*)(yt + (lane & 7) * 528 + (lane >> 3) * 2);
#pragma unroll
  for (int t = 0; t < 2; ++t) {
    if (t == 0 || grp < 8) {
      unsigned yy[4][8];
#pragma unroll
      for (int i = 0; i < 4; ++i)
#pragma unroll
        for (int d = 0; d < 8; ++d) yy[i][d] = p[t * 2112 + i * 8 + d * 32];
      SB();
#pragma unroll
      for (int i = 0; i < 4; ++i) {
        u4 o;
#pragma unroll
        for (int d = 0; d < 4; ++d)
          o[d] = pk(lo16(yy[i][2 * d]) * lo16(qv[t * 4 + i][d]), lo16(yy[i][2 * d + 1]) * hi16(qv[t * 4 + i][d]));
        st16<0>(o, rout, oo + (t * 1024 + i * 16) * 2304);
      }
    }
  }
}

DEV void out_proj(lptr sm, char* ws, u16* out, int bid, int lw, int lane, const Proj& pc) {
  lptr wa[2] = {(lptr)(size_t)pc.wa, (lptr)(size_t)(pc.wa + 9 * 4608)};
  lptr wb[2] = {(lptr)(size_t)pc.wb, (lptr)(size_t)(pc.wb + 9 * 4608)};
  pin(wa[1]); pin(wb[1]);
  rsrc_t ro = mkrs(ws + WS_O, NB_ACT);
  const unsigned ob = pc.ob, oo[3] = {pc.oo0, pc.oo1, pc.oo2};
  lptr ot = (lptr)(size_t)pc.ot, ow = (lptr)(size_t)pc.ow;
  reach(L_CNT + 20, 4);
  f4 acc[5];
  {
    constexpr int PRE = 7, AHEAD = 9;
    bf8 wf[PRE][5];
    auto rdw = [&](int i) {
#pragma unroll
      for (int n = 0; n < 5; ++n)
        wf[i % PRE][n] = *(const LAS bf8*)((n < 4 ? wa[i / 9] + n * 1024 : wb[i / 9]) + (i % 9) * 4608);
    };
#pragma unroll
    for (int i = 0; i < PRE - 1; ++i) rdw(i);
    SB();
    if (lw == 0 && bid >= 128) {
      wait_flags(ws, pc.fl, *(LAS u64*)(sm + L_TAG));
      arrive(L_CNT + 28);
    }
    reach(L_CNT + 28, 1);
    STAMPL(9);
    u4 of[18];
    const u4 rod = mkdesc(ws + WS_O, NB_ACT);
    auto ldo = [&](int i) { of[i] = ld16<SC1>(ro, ob, i * 4096); };
#pragma unroll
    for (int i = 0; i < AHEAD; ++i) { ldo(i); SB(); }
#pragma unroll
    for (int n = 0; n < 5; ++n) acc[n] = f4{0.f, 0.f, 0.f, 0.f};
#pragma unroll
    for (int i = 0; i < 18; i += 3) {
      if (__builtin_expect(!__all(max12(of[i], of[i + 1], of[i + 2]) != NOTYET), 0)) {
        STAMPL(31);
#pragma unroll 1
        for (int spin = 0; spin < SPIN_MAX; ++spin) {
          asm volatile("buffer_load_dwordx4 %0, %3, %4, %5 offen sc1\n\tbuffer_load_dwordx4 %1, %3, %4, %6 offen sc1\n\t"
                       "buffer_load_dwordx4 %2, %3, %4, %7 offen sc1\n\ts_waitcnt vmcnt(0)"
                       : "=&v"(of[i]), "=&v"(of[i + 1]), "=&v"(of[i + 2])
                       : "v"(ob), "s"(rod), "s"(i * 4096), "s"(i * 4096 + 4096), "s"(i * 4096 + 8192) : "memory");
          if (__all(max12(of[i], of[i + 1], of[i + 2]) != NOTYET)) break;
        }
      }
#pragma unroll
      for (int e = i; e < i + 3; ++e) {
        if (e + PRE - 1 < 18) rdw(e + PRE - 1);
#pragma unroll
        for (int n = 0; n < 5; ++n) acc[n] = mfma(wf[e % PRE][n], asb(of[e]), acc[n]);
        if (e + AHEAD < 18) ldo(e + AHEAD);
        SGB(0x8, 1); SGB(0x20, 1); SGB(0x100, 1);
#pragma unroll
        for (int n = 1; n < 5; ++n) { SGB(0x8, 1); SGB(0x100, 1); }
        SB();
      }
    }
  }
  STAMPL(10);
#pragma unroll
  for (int n = 0; n < 5; ++n)
    if (n < 4 || lane < 32) *(LAS u2*)(ow + n * 32) = u2{pk(acc[n][0], acc[n][1]), pk(acc[n][2], acc[n][3])};
  WAITL0();
  rsrc_t rout = mkrs(out, 2 * NB_ACT);
#pragma unroll
  for (int i = 0; i < 3; ++i) st16<0>(*(const LAS u4*)(ot + i * 1024), rout, oo[i]);
  STAMPL(15);
}

__global__ __launch_bounds__(512) __attribute__((amdgpu_waves_per_eu(2, 2))) void fused(
    const u16* x, const u16* wqkv, const u16* wo, const u16* q, const u16* k, const u16* v, const u16* shk,
    const u16* hk, const u16* dv, u16* out, char* ws) {
  extern __shared__ __attribute__((aligned(16))) char smem[];
  lptr sm = (lptr)smem;
  const int tid = threadIdx.x, lane = tid & 63, w = __builtin_amdgcn_readfirstlane(tid >> 6);
  const int bid = blockIdx.x;
  if (bid < 128 && tid < 16) {
    u64 pc;
    asm volatile("s_getpc_b64 %0" : "=s"(pc));
    asm volatile("s_mov_b32 m0, %0\n\ts_nop 0\n\tglobal_load_lds_dword %1, %2"
                 :: "s"(L_PF), "v"(((bid >> 3) + 16 * tid) * 128), "s"(pc & ~127ull) : "memory");
  }
  if (bid >= 128) {
    if (tid < 12) *(LAS unsigned*)(sm + L_CNT + tid * 4) = 0;
    bar();
  }
  Proj pc = {};
  if (__builtin_expect(bid < 128, 1)) role_a(sm, ws, x, wqkv, bid, w, lane, pc);
  else role_b(sm, ws, x, wqkv, wo, q, k, v, shk, hk, dv, bid, w, lane);
  if (w >= 4) {
    if (bid >= 128) proj_consts(pc, bid, w & 3, lane);
    out_proj(sm, ws, out, bid, w & 3, lane, pc);
  } else if (bid >= 128) {
    const bool g8 = bid >= 192;
    if (!g8 || w == 0)
      finish(sm, sm + (g8 ? 4 : w) * CH_SZ, ws, q, out, g8 ? 8 : bid & 7, g8 ? (bid - 192) >> 2 : (bid >> 3) - 16,
             g8 ? bid & 3 : w, lane);
    STAMP(13);
  } else {
    if (bid == 64 && w == 1) {
      const u64 tag = *(LAS u64*)(sm + L_TAG);
      GAS u64* p = (GAS u64*)(ws + WS_FLAG + lane * 128);
#pragma unroll 1
      for (int spin = 0; spin < SPIN_MAX; ++spin) {
        const u64 a = __hip_atomic_load(p, RLX_AGENT), b = __hip_atomic_load(p + 1024, RLX_AGENT);
        const u64 c = __hip_atomic_load(p + 2048, RLX_AGENT), d = __hip_atomic_load(p + 3072, RLX_AGENT);
        if (__all(a == tag && b == tag && c == tag && d == tag)) break;
        __builtin_amdgcn_s_sleep(PSLEEP);
      }
      if (lane == 0) __hip_atomic_store((GAS u64*)ws, tag, RLX_AGENT);
    }
  }
}

extern "C" void kernel_launch(void* const* d_in, const int* in_sizes, int n_in, void* d_out, int out_size, void* d_ws,
                              size_t ws_size, hipStream_t stream) {
  (void)hipFuncSetAttribute((const void*)fused, hipFuncAttributeMaxDynamicSharedMemorySize, LDS_BYTES);
  fused<<<256, 512, LDS_BYTES, stream>>>((const u16*)d_in[0], (const u16*)d_in[1], (const u16*)d_in[2], (const u16*)d_in[3],
                                         (const u16*)d_in[4], (const u16*)d_in[5], (const u16*)d_in[6], (const u16*)d_in[7],
                                         (const u16*)d_in[8], (u16*)d_out, (char*)d_ws);
}
```

```cpp
#include <hip/hip_runtime.h>

typedef unsigned short u16;
typedef unsigned long long u64;
typedef short s4 __attribute__((ext_vector_type(4)));
typedef __bf16 bf2 __attribute__((ext_vector_type(2)));
typedef __bf16 bf8 __attribute__((ext_vector_type(8)));
typedef float f2 __attribute__((ext_vector_type(2)));
typedef float f4 __attribute__((ext_vector_type(4)));
typedef unsigned u4 __attribute__((ext_vector_type(4)));
typedef unsigned u2 __attribute__((ext_vector_type(2)));
typedef __amdgpu_buffer_rsrc_t rsrc_t;
#define LAS __attribute__((address_space(3)))
#define GAS __attribute__((address_space(1)))
typedef LAS char* lptr;
#define RLX_AGENT __ATOMIC_RELAXED, __HIP_MEMORY_SCOPE_AGENT
#define DEV __device__ __forceinline__

constexpr int NB_ACT = 2048 * 576 * 2;
constexpr int NB_W = 576 * 1728 * 2;
constexpr int NB_WO = 576 * 576 * 2;
constexpr unsigned OOB = 0x80000000u;
constexpr unsigned NOTYET = 0xffffffffu;
constexpr int SC1 = 16;
constexpr int CODE_LINES = 248;
#define ROTK 0
#define PFS 12
#define TDELAY 0
#define YSH 0
#define PF0 2
#define HDELAY 6
#define PFA 6
#define PFB 9
#define PDELAY 3
#define EARLYHINT 0
#define HSLOT 0
#define PSLOT 0
#define PF1 4
#define LPRIO 0
#define HSWAP 1
#define UDELAY 1
#define YDELAY 0
#define XSH 1
#define OSH 2
#define PSLEEP 2
constexpr int SPIN_MAX = 1 << 15;

constexpr int WS_FLAG = 4096;
constexpr int F_A = 0, F_P = 128, F_Y = 256, F_Q = 512, F_W = 640, F_G = 896, F_L = 960;
constexpr int WS_U = 1 << 20;
constexpr int WS_Y = WS_U + (3 << 20);
constexpr int WS_O = WS_Y + (3 << 20);
constexpr int WS_WO = WS_O + (3 << 20);
constexpr int WS_DBG = 16 << 20;

constexpr int LDS_BYTES = 163840;
constexpr int XB = 144 * 128;
constexpr int SLOT = XB + 64 * 432;
constexpr int PS = 1160, HS = 32640;
constexpr int L_WO = 65152;
constexpr int L_OT = L_WO + 83200;
constexpr int H_CP = 4160, U_OFF = 8448, CH_SZ = 13024;
constexpr int L_TAG = L_OT + 10240;
constexpr int L_CNT = L_TAG + 16;
constexpr int L_YA = L_TAG + 128;
constexpr int L_PF = LDS_BYTES - 256;

#define STAMPW(i, wv) do {} while (0)
#define STAMP(i) STAMPW(i, 0)
#define STAMPL(i) STAMPW(i, 4)

DEV rsrc_t mkrs(const void* p, int bytes) { return __builtin_amdgcn_make_buffer_rsrc((void*)p, 0, bytes, 0x00020000); }
DEV u4 mkdesc(const void* p, int bytes) {
  u64 a = (u64)p;
  return u4{(unsigned)a, (unsigned)(a >> 32) & 0xffffu, (unsigned)bytes, 0x00020000u};
}
DEV void dma16(u4 r, unsigned lds, unsigned voff, unsigned soff) {
  asm volatile("s_mov_b32 m0, %0\n\ts_nop 0\n\tbuffer_load_dwordx4 %1, %2, %3 offen lds"
               :: "s"(lds), "v"(voff), "s"(r), "s"(soff) : "memory");
}
DEV void dma16c(u4 r, unsigned lds, unsigned voff, unsigned soff) {
  asm volatile("s_mov_b32 m0, %0\n\ts_nop 0\n\tbuffer_load_dwordx4 %1, %2, %3 offen sc1 lds"
               :: "s"(lds), "v"(voff), "s"(r), "s"(soff) : "memory");
}
DEV void dma16m(u4 r, unsigned lds, unsigned voff, unsigned soff, u64 mask) {
  asm volatile("s_mov_b32 m0, %0\n\ts_mov_b64 exec, %4\n\tbuffer_load_dwordx4 %1, %2, %3 offen lds\n\ts_mov_b64 exec, -1"
               :: "s"(lds), "v"(voff), "s"(r), "s"(soff), "s"(mask) : "memory");
}
template <int AUX> DEV void st16(u4 v, rsrc_t r, unsigned off) { __builtin_amdgcn_raw_buffer_store_b128(v, r, off, 0, AUX); }
template <int AUX> DEV u4 ld16(rsrc_t r, unsigned off, unsigned soff) { return __builtin_amdgcn_raw_buffer_load_b128(r, off, soff, AUX); }
DEV u4 ld16c(u4 r, unsigned voff, unsigned soff) {
  u4 v;
  asm volatile("buffer_load_dwordx4 %0, %1, %2, %3 offen sc1" : "=v"(v) : "v"(voff), "s"(r), "s"(soff) : "memory");
  return v;
}
DEV void touch(u4 r, unsigned voff, unsigned soff) {
  asm volatile("s_mov_b32 m0, %0\n\ts_nop 0\n\tbuffer_load_dword %1, %2, %3 offen lds"
               :: "s"(L_PF), "v"(voff), "s"(r), "s"(soff) : "memory");
}
DEV s4 trread(lptr p) { return __builtin_amdgcn_ds_read_tr16_b64_v4i16((LAS s4*)p); }
DEV bf8 cat8(s4 a, s4 b) { return __builtin_bit_cast(bf8, __builtin_shufflevector(a, b, 0, 1, 2, 3, 4, 5, 6, 7)); }
DEV bf8 asb(u4 v) { return __builtin_bit_cast(bf8, v); }
DEV f4 mfma(bf8 a, bf8 b, f4 c) { return __builtin_amdgcn_mfma_f32_16x16x32_bf16(a, b, c, 0, 0, 0); }
DEV unsigned pk(float lo, float hi) { f2 v = {lo, hi}; return __builtin_bit_cast(unsigned, __builtin_convertvector(v, bf2)); }
DEV float asf(unsigned u) { return __builtin_bit_cast(float, u); }
DEV unsigned asu(float f) { return __builtin_bit_cast(unsigned, f); }
DEV float lo16(unsigned x) { return asf(x << 16); }
DEV float hi16(unsigned x) { return asf(x & 0xffff0000u); }
DEV float dot2(unsigned a, unsigned b, float c) {
  return __builtin_amdgcn_fdot2_f32_bf16(__builtin_bit_cast(bf2, a), __builtin_bit_cast(bf2, b), c, false);
}
DEV float dot2z(unsigned a, unsigned b) {
  float d;
  asm("v_dot2_f32_bf16 %0, %1, %2, 0" : "=v"(d) : "v"(a), "v"(b));
  return d;
}
DEV float dot2a(unsigned a, unsigned b, float c) {
  float d;
  asm("v_dot2_f32_bf16 %0, %1, %2, %3" : "=v"(d) : "v"(a), "v"(b), "v"(c));
  return d;
}
DEV unsigned alignb(unsigned hi, unsigned lo) { return __builtin_amdgcn_alignbit(hi, lo, 16); }
DEV unsigned umax(unsigned a, unsigned b) { return a > b ? a : b; }
DEV unsigned max4(u4 v, unsigned m) { return umax(umax(umax(v[0], v[1]), m), umax(v[2], v[3])); }
DEV unsigned max3u(unsigned a, unsigned b, unsigned c) { return umax(umax(a, b), c); }
DEV unsigned max12(u4 a, u4 b, u4 c) {
  return umax(max3u(max3u(max3u(a[0], a[1], a[2]), a[3], b[0]), b[1], b[2]), max3u(max3u(b[3], c[0], c[1]), c[2], c[3]));
}
DEV unsigned chk(u4 v, unsigned key) { return (v[0] + v[1] + v[2]) + (v[3] + key); }
DEV unsigned rowsum(unsigned x) {
  auto a = __builtin_amdgcn_permlane16_swap(x, x, false, false);
  const unsigned y = a[0] + a[1];
  auto b = __builtin_amdgcn_permlane32_swap(y, y, false, false);
  return b[0] + b[1];
}
DEV float fmax2(float a, float b) { return __builtin_elementwise_maximum(a, b); }
#define WAITV(n) asm volatile("s_waitcnt vmcnt(" #n ")" ::: "memory")
#define WAITL0() asm volatile("s_waitcnt lgkmcnt(0)" ::: "memory")
#define SB() __builtin_amdgcn_sched_barrier(0)
#define SGB(m, n) __builtin_amdgcn_sched_group_barrier(m, n, 0)
DEV void bar() { WAITL0(); __builtin_amdgcn_s_barrier(); asm volatile("" ::: "memory"); }
template <class T> DEV void pin(T& x) { asm volatile("" : "+v"(x)); }

DEV u64 get_tag(char* ws) {
  u64 e;
  asm volatile("s_load_dwordx2 %0, %1, 0x0\n\ts_waitcnt lgkmcnt(0)" : "=s"(e) : "s"(ws) : "memory");
  return e * 6364136223846793005ull + 1442695040888963407ull;
}
DEV void post_flag(char* ws, int idx, u64 tag) { __hip_atomic_store((GAS u64*)(ws + WS_FLAG + idx * 128), tag, RLX_AGENT); }
DEV void wait_flags(char* ws, int idx, u64 tag) {
  GAS u64* p = (GAS u64*)(ws + WS_FLAG + idx * 128);
#pragma unroll 1
  for (int spin = 0; spin < SPIN_MAX; ++spin) {
    if (__all(__hip_atomic_load(p, RLX_AGENT) == tag)) break;
    __builtin_amdgcn_s_sleep(PSLEEP);
  }
  __builtin_amdgcn_fence(__ATOMIC_ACQUIRE, "wavefront");
}

DEV void arrive(unsigned word) {
  unsigned one = 1;
  asm volatile("s_waitcnt lgkmcnt(0)\n\ts_mov_b64 exec, 1\n\tds_add_u32 %0, %1\n\ts_mov_b64 exec, -1"
               :: "v"(word), "v"(one) : "memory");
}
DEV void reach(unsigned word, unsigned n) {
#pragma unroll 1
  for (int spin = 0; spin < (1 << 22); ++spin) {
    unsigned v;
    asm volatile("ds_read_b32 %0, %1\n\ts_waitcnt lgkmcnt(0)" : "=v"(v) : "v"(word) : "memory");
    if ((unsigned)__builtin_amdgcn_readfirstlane(v) >= n) break;
    __builtin_amdgcn_s_sleep(1);
  }
}

DEV u4 ld2(lptr p) {
  const u2 a = *(const LAS u2*)p, b = *(const LAS u2*)(p + HS);
  return u4{a[0], a[1], b[0], b[1]};
}
DEV u64 lds_tag(lptr sm) {
  reach(L_CNT, 1);
  return *(LAS u64*)(sm + L_TAG);
}
DEV void att_consts(lptr sm, int lane, u4 (&tc)[9], int first) {
  const LAS u4* tb = (const LAS u4*)(sm + L_OT) + lane;
#pragma unroll
  for (int i = 0; i < 9; ++i) tc[i] = tb[(i < 4 ? i : i == 4 ? 4 + first : i + 1) * 64];
}

struct Proj { unsigned wa, wb, ob, oh, oo0, oo1, oo2, ot, ow, fl; };
DEV void proj_consts(Proj& p, int bid, int lw, int lane) {
  const int rt = ((bid + OSH) & 7) * 4 + ((bid >> 6) & 1) * 2 + (bid >> 7), ct = (bid >> 3) & 7;
  const int r = lane & 15, g = lane >> 4;
  p.wa = L_WO + lane * 16;
  p.wb = L_WO + 4096 + (g * 8 + (r & 7)) * 16;
  p.ob = rt * 72 * 1024 + g * 1024 + lw * 256 + r * 16;
  p.oh = NB_ACT + (rt * 4 + lw) * 512 + lane * 4;
  pin(p.oh);
  unsigned oo[3];
#pragma unroll
  for (int i = 0; i < 3; ++i) {
    const int it = lane + 64 * i, row = (it * 57) >> 9, c = it - row * 9;
    oo[i] = (i == 2 && lane >= 16) ? OOB : (rt * 64 + 32 * (lw >> 1) + (lw & 1)) * 2304 + __umul24(row, 4608) + ct * 144 + c * 16;
  }
  p.oo0 = oo[0]; p.oo1 = oo[1]; p.oo2 = oo[2];
  p.ot = L_OT + lw * 2304 + lane * 16;
  p.ow = L_OT + lw * 2304 + r * 144 + g * 8;
  p.fl = ((rt & 1) ? F_A : F_L) + (rt >> 1) * 8 + (lane & 7);
  pin(p.wa); pin(p.wb); pin(p.ob); pin(p.oo0); pin(p.oo1); pin(p.oo2); pin(p.ot); pin(p.ow); pin(p.fl);
}

template <int HI>
DEV void g1_compute(lptr sm, int n0, int lane, u4 (&tc)[9], int first) {
  const int r = lane & 15, g = lane >> 4;
  lptr xa0 = sm + r * 128 + ((g ^ ((r >> 1) & 7)) << 4);
  lptr xa1 = (lptr)((size_t)xa0 ^ 64);
  lptr wa = sm + XB + (((g >> 1) << 4) + ((g & 1) << 3) + ((r >> 2) << 1)) * 432 + (lane & 3) * 8 + n0 * 32;
#define USED(m, n) (HI ? ((n) > 0 || (m) >= 5) : ((n) < 3 || (m) < 5))
  f4 acc[9][4];
#pragma unroll
  for (int m = 0; m < 9; ++m)
#pragma unroll
    for (int n = 0; n < 4; ++n) {
      acc[m][n] = f4{0.f, 0.f, 0.f, 0.f};
      if (USED(m, n)) pin(acc[m][n]);
    }
  bf8 xf[9], wf[2][4];
  auto mm = [&](bf8 (&wv)[4], int m) {
#pragma unroll
    for (int n = 0; n < 4; ++n)
      if (USED(m, n)) acc[m][n] = mfma(wv[n], xf[m], acc[m][n]);
  };
  auto rdx = [&](lptr xp, int m) { xf[m] = *(const LAS bf8*)(xp + m * 2048); };
  auto rdw = [&](lptr wp, bf8 (&wv)[4], int n) { wv[n] = cat8(trread(wp + n * 32), trread(wp + 432 + n * 32)); };
  auto mix = [&]() {
#pragma unroll
    for (int i = 0; i < 4; ++i) { SGB(0x8, 1); SGB(0x100, 1); }
    SB();
  };
  auto step = [&](bf8 (&wc)[4], bf8 (&wn)[4], lptr xp, lptr wp, int m0) {
#pragma unroll
    for (int m = m0; m < 9; ++m) {
      mm(wc, m);
      if (m - m0 < 4) rdw(wp, wn, m - m0);
      if (m0 == 0) { if (m > 0) rdx(xp, m - 1); }
      else if (m - m0 < 4) rdx(xp, m - m0);
      else { rdx(xp, 2 * (m - m0) - 4); if (2 * (m - m0) - 3 < m) rdx(xp, 2 * (m - m0) - 3); }
      mix();
    }
#pragma unroll
    for (int m = (m0 == 0 ? 8 : 2 * (9 - m0) - 4); m < 9; ++m) rdx(xp, m);
    SB();
  };
  constexpr int MS = 3;
  bar();
#pragma unroll
  for (int n = 0; n < 4; ++n) rdw(wa, wf[0], n);
#pragma unroll
  for (int m = 0; m < 9; ++m) rdx(xa0, m);
  SB();
  int so = 0;
#pragma unroll 1
  for (int k = 0; k < 8; ++k) {
    step(wf[0], wf[1], xa1 + so, wa + so + 32 * 432, 0);
    so = so == 2 * SLOT ? 0 : so + SLOT;
#pragma unroll
    for (int m = 0; m < MS; ++m) mm(wf[1], m);
    SB();
    bar();
    step(wf[1], wf[0], xa0 + so, wa + so, MS);
  }
  xa1 += 2 * SLOT; wa += 2 * SLOT + 32 * 432;
  pin(xa1); pin(wa);
  STAMP(1);
  lptr d = sm + (2 * n0 + (g >> 1)) * PS + r * 8 + (g & 1) * HS;
  bf8 xg[9];
#pragma unroll
  for (int m = 0; m < 11; ++m) {
    if (m < 9) mm(wf[0], m);
    if (m < 2) { rdw(wa, wf[1], 2 * m); rdw(wa, wf[1], 2 * m + 1); }
    if (m < 9) xg[m] = *(const LAS bf8*)(xa1 + m * 2048);
    if (m >= 2) {
#pragma unroll
      for (int n = 0; n < 4; ++n)
        if (USED(m - 2, n)) acc[m - 2][n] = mfma(wf[1][n], xg[m - 2], acc[m - 2][n]);
    }
    if (m == 7) att_consts(sm, lane, tc, first);
    if (m == 8) arrive(L_CNT + 8);
#pragma unroll
    for (int o = m - 3; o < (m == 10 ? 9 : m - 2); ++o) {
      if (o >= 0) {
#pragma unroll
        for (int n = 0; n < 4; ++n)
          if (USED(o, n))
            *(LAS u2*)(d + n * 2 * PS + o * 128) = u2{pk(acc[o][n][0], acc[o][n][1]), pk(acc[o][n][2], acc[o][n][3])};
      }
    }
    if (m < 3) {
#pragma unroll
      for (int i = 0; i < 4; ++i) { SGB(0x8, 1); SGB(0x100, 2); }
    } else {
      SGB(0x8, 1); SGB(0x100, 1);
#pragma unroll
      for (int i = 0; i < 4; ++i) { SGB(0x8, 1); SGB(0x2, 2); SGB(0x8, 1); SGB(0x200, 1); }
    }
    SB();
  }
}

DEV void role_a(lptr sm, char* ws, const u16* x, const u16* wqkv, int bid, int w, int lane, Proj& pc) {
  const int xcd = bid & 7, j = bid >> 3;
  const int rt = (xcd >> 1) * 4 + (j >> 2), hq = (xcd & 1) * 4 + (j & 3);
  const u4 rp = mkdesc(ws + WS_WO + (j & 7) * 82944, 82944);
  u64 tag;
  u4 tc[9], wr[21];
  const int aw = w ^ 4;
  const int first = rt + (aw >> 1) == 0;
  if (__builtin_expect(w >= 4, 1)) {
    const int lw = w - 4;
    const u4 rx = mkdesc(x, NB_ACT), rw = mkdesc(wqkv, NB_W);
    const int t27 = lane >= 27, cc = lane - 27 * t27, part = (cc >= 9) + (cc >= 18);
    const unsigned vw = t27 * (4 * 3456) + part * (1152 - 144) + cc * 16 + hq * 144;
    auto wpiece = [&](unsigned d, int kt, int i) {
      dma16m(rw, d + XB + (lw * 8 + i) * 864, vw, (kt * 64 + lw * 16 + 8 * (i >> 2) + (i & 3)) * 3456, (1ull << 54) - 1);
    };
    auto tailp = [&](unsigned d, int kt) {
#pragma unroll
      for (int i = 2; i < 8; ++i) wpiece(d, kt, i);
    };
    STAMPL(16);
    tailp(0, 0);
    SB();
    unsigned vx[6];
#pragma unroll
    for (int i = 0; i < 6; ++i) {
      const int xr = lw * 36 + i * 6 + (lane >> 3);
      vx[i] = __umul24(rt * 128 + xr, 1152) + (((lane & 7) ^ ((xr >> 1) & 7)) << 4) - 16 * 1152;
    }
    auto headp = [&](unsigned d, int kt) {
#pragma unroll
      for (int i = 0; i < 6; ++i) dma16m(rx, d + (lw * 6 + i) * 768, vx[i], kt * 128, (1ull << 48) - 1);
#pragma unroll
      for (int i = 0; i < 2; ++i) wpiece(d, kt, i);
    };
    headp(0, 0);
    unsigned d = SLOT;
    const int fi = F_W + (j & 7) * 32 + (lane < 18 ? lane : 0);
    GAS u64* fp = (GAS u64*)(ws + WS_FLAG + fi * 128);
    u64 fw = 0;
#pragma unroll 1
    for (int k = 1; k < 9; ++k) {
      headp(d, k);
      WAITV(8);
      bar();
      STAMPL(16 + k);
      if (k == 8 && lw == 3) asm volatile("global_load_dwordx2 %0, %1, off sc1" : "+v"(fw) : "v"(fp) : "memory");
      tailp(d, k);
      d = d == 2 * SLOT ? 0 : d + SLOT;
    }
    STAMPL(26);
    tag = *(LAS u64*)(sm + L_TAG);
    att_consts(sm, lane, tc, first);
    proj_consts(pc, bid, lw, lane);
    asm volatile("s_waitcnt vmcnt(0)" : "+v"(fw) :: "memory");
    if (lw == 3 && !__all(fw == tag)) wait_flags(ws, fi, tag);
    bar();
    STAMPL(27);
#pragma unroll
    for (int i = 0; i < 21; ++i) wr[i] = ld16c(rp, lane * 16, (4 * i + lw) * 1024);
    __builtin_amdgcn_s_setprio(LPRIO);
    STAMPL(28);
  } else {
    tag = get_tag(ws);
    if (threadIdx.x == 0) { *(LAS u64*)(sm + L_TAG) = tag; *(LAS u4*)(sm + L_CNT) = u4{0, 0, 0, 0}; *(LAS u4*)(sm + L_CNT + 16) = u4{0, 0, 0, 0}; *(LAS u4*)(sm + L_CNT + 32) = u4{0, 0, 0, 0}; }
    {
      const int r = lane & 15, g = lane >> 4, q4 = r >> 2, p4 = lane & 3;
      LAS u4* tb = (LAS u4*)(sm + L_OT) + lane;
      u4 t0, t1;
      if (w < 2) {
#pragma unroll
        for (int dd = 0; dd < 4; ++dd) {
          t0[dd] = __builtin_amdgcn_sbfe((int)(0x1ffu << (2 * w)), 4 * g + dd, 1);
          t1[dd] = __builtin_amdgcn_sbfe((int)(0x3feu << (2 * w)), 4 * g + dd, 1);
        }
      } else if (w == 2) {
#pragma unroll
        for (int dd = 0; dd < 4; ++dd) {
          const int kk = 4 * g + dd - r;
          t0[dd] = (kk >= 1 && kk <= 8) ? 0u : 0xff800000u;
          t1[dd] = (kk >= 1 && kk <= 8 && g >= 2) ? 0u : 0xff800000u;
        }
      } else {
#pragma unroll
        for (int dd = 0; dd < 4; ++dd) t0[dd] = 4 * g + dd - r <= -8 ? 0u : 0xff800000u;
        t1[0] = (t0[0] ? 0u : 0xffffu) | (t0[1] ? 0u : 0xffff0000u);
        t1[1] = (t0[2] ? 0u : 0xffffu) | (t0[3] ? 0u : 0xffff0000u);
        t1[2] = g * PS + r * 16;
        t1[3] = g * PS + (r & 7) * 16;
        const int kr0 = (4 * g + q4) * 16, kr1 = kr0 + (g >= 2 ? 128 : 256);
        const int ve = (p4 >> 1) * PS + (p4 & 1) * HS, vo = ((p4 + 1) >> 1) * PS + ((p4 + 1) & 1) * HS;
        tb[8 * 64] = u4{(unsigned)(kr0 + ve), (unsigned)(kr1 + ve), (unsigned)(kr0 + vo), (unsigned)(kr1 + vo)};
        const unsigned os = (rt * 144 + hq * 9 + g) * 1024 + r * 16;
        tb[9 * 64] = u4{(unsigned)kr0, (unsigned)kr1, os, g ? OOB : os + 8192};
      }
      tb[2 * w * 64] = t0;
      tb[(2 * w + 1) * 64] = t1;
    }
    if (w & 1) g1_compute<1>(sm, 3 + (w >> 1) * 7, lane, tc, first);
    else g1_compute<0>(sm, (w >> 1) * 7, lane, tc, first);
#pragma unroll
    for (int i = 0; i < 21; ++i) asm volatile("" : "=v"(wr[i]));
    asm volatile("" : "=v"(pc.wa), "=v"(pc.wb), "=v"(pc.ob), "=v"(pc.oh), "=v"(pc.oo0), "=v"(pc.oo1), "=v"(pc.oo2),
                 "=v"(pc.ot), "=v"(pc.ow), "=v"(pc.fl));
  }
  {
    const unsigned rb = (32 * (aw >> 1) + (aw & 1)) * 8;
    const u4 qm[4] = {tc[0], tc[1], tc[2], tc[3]}, u0 = tc[4], u1 = tc[5], c0 = tc[6], c1 = tc[7], c2 = tc[8];
    const f4 b0 = __builtin_bit_cast(f4, u0), b1 = __builtin_bit_cast(f4, u1);
    const unsigned m01 = c0[0], m23 = c0[1];
    lptr qa = sm + rb + c0[2], k1 = sm + rb + c0[3];
    lptr ve0 = sm + rb + c1[0], ve1 = sm + rb + c1[1], vo0 = sm + rb + c1[2], vo1 = sm + rb + c1[3];
    lptr vk0 = sm + rb + c2[0], vk1 = sm + rb + c2[1];
    const unsigned ow = (aw >> 2) * 73728 + (aw & 3) * 256;
    unsigned os0 = c2[2] + ow, os1 = os0 + 4096, os2 = c2[3] + ow;
    const bool g0 = lane < 16;
    rsrc_t ro = mkrs(ws + WS_O, NB_ACT + 65536);
    unsigned oh = g0 ? NB_ACT + ((rt * 8 + aw) * 8 + hq) * 64 + lane * 4 : OOB;
    pin(oh);
    u4 ones = {0x3f803f80u, 0x3f803f80u, 0x3f803f80u, 0x3f803f80u};
    pin(qa); pin(k1); pin(ve0); pin(ve1); pin(vo0); pin(vo1); pin(vk0); pin(vk1); pin(os0); pin(os1); pin(os2); pin(ones);
    STAMP(14);
    if (w < 4) arrive(L_CNT);
    reach(w < 4 ? L_CNT : L_CNT + 8, 4);
    STAMP(2);
    STAMPL(29);
    const float cs = 0.23570226039551584f * 1.4426950408889634f;
    const f4 z = {0.f, 0.f, 0.f, 0.f};
    u4 qf[4];
    bf8 ka[4], kb[4], vt0[4], vt1[4];
#pragma unroll
    for (int h = 0; h < 4; ++h) {
      qf[h] = ld2(qa + 2 * h * PS + 128);
      ka[h] = asb(ld2(qa + (9 + 2 * h) * PS));
      kb[h] = asb(ld2(k1 + (9 + 2 * h) * PS + 256));
      SB();
    }
#pragma unroll
    for (int h = 0; h < 4; ++h) {
      vt0[h] = cat8(trread((h < 2 ? ve0 : vo0) + (18 + 2 * h) * PS), trread((h < 2 ? ve1 : vo1) + (18 + 2 * h) * PS));
      vt1[h] = cat8(trread(vk0 + (20 + 2 * h) * PS + (h < 2 ? 0 : HS)), trread(vk1 + (20 + 2 * h) * PS + (h < 2 ? 0 : HS)));
    }
    SB();
    f4 s0[4], s1[4], s[4];
    float mx[4];
#pragma unroll
    for (int h = 0; h < 4; ++h) {
      bf8 qq = asb(qf[h] & qm[h]);
      s0[h] = mfma(ka[h], qq, b0);
      s1[h] = mfma(kb[h], qq, b1);
      SB();
    }
#pragma unroll
    for (int h = 0; h < 4; ++h) {
#pragma unroll
      for (int dd = 0; dd < 4; ++dd) s[h][dd] = fmax2(s0[h][dd], s1[h][dd]);
      mx[h] = fmax2(fmax2(fmax2(s[h][0], s[h][1]), s[h][2]), s[h][3]);
      SB();
    }
    {
      auto a = __builtin_amdgcn_permlane16_swap(asu(mx[0]), asu(mx[1]), false, false);
      auto b = __builtin_amdgcn_permlane16_swap(asu(mx[2]), asu(mx[3]), false, false);
      auto c = __builtin_amdgcn_permlane32_swap(asu(fmax2(asf(a[0]), asf(a[1]))), asu(fmax2(asf(b[0]), asf(b[1]))), false, false);
      unsigned m = asu(-cs * fmax2(asf(c[0]), asf(c[1])));
      auto e = __builtin_amdgcn_permlane32_swap(m, m, false, false);
      unsigned e0 = e[0], e1 = e[1];
      auto f0 = __builtin_amdgcn_permlane16_swap(e0, e0, false, false);
      auto f1 = __builtin_amdgcn_permlane16_swap(e1, e1, false, false);
      mx[0] = asf(f0[0]); mx[1] = asf(f0[1]); mx[2] = asf(f1[0]); mx[3] = asf(f1[1]);
    }
    SB();
    asm volatile("s_waitcnt vmcnt(0)" : "+v"(wr[0]), "+v"(wr[1]), "+v"(wr[2]), "+v"(wr[3]), "+v"(wr[4]), "+v"(wr[5]),
                 "+v"(wr[6]), "+v"(wr[7]), "+v"(wr[8]), "+v"(wr[9]), "+v"(wr[10]), "+v"(wr[11]), "+v"(wr[12]),
                 "+v"(wr[13]), "+v"(wr[14]), "+v"(wr[15]), "+v"(wr[16]), "+v"(wr[17]), "+v"(wr[18]), "+v"(wr[19]),
                 "+v"(wr[20]) :: "memory");
    f4 sum[4], o0[4], o1[4];
#pragma unroll
    for (int h = 0; h < 4; ++h) {
      float p[4];
#pragma unroll
      for (int dd = 0; dd < 4; ++dd) p[dd] = __builtin_amdgcn_exp2f(__builtin_fmaf(s[h][dd], cs, mx[h]));
      unsigned p01 = pk(p[0], p[1]), p23 = pk(p[2], p[3]);
      bf8 pf = asb(u4{p01 & ~m01, p23 & ~m23, p01 & m01, p23 & m23});
      sum[h] = mfma(asb(ones), pf, z);
      o0[h] = mfma(vt0[h], pf, z);
      o1[h] = mfma(vt1[h], pf, z);
      SB();
    }
    u2 gr[4];
    u4 left;
#pragma unroll
    for (int h = 0; h < 4; ++h) {
      const float inv = __builtin_amdgcn_rcpf(sum[h][0]);
      const unsigned a0 = pk(o0[h][0] * inv, o0[h][1] * inv), a1 = pk(o0[h][2] * inv, o0[h][3] * inv);
      const unsigned e0 = pk(o1[h][0] * inv, o1[h][1] * inv);
      if (h & 1) {
        const unsigned e1 = pk(o1[h][2] * inv, o1[h][3] * inv);
        left[h] = a1;
        gr[h] = u2{g0 ? e0 : a0, g0 ? e1 : a1};
        st16<SC1>(u4{gr[h - 1][0], gr[h - 1][1], gr[h][0], gr[h][1]}, ro, h == 1 ? os0 : os1);
        SB();
      } else {
        left[h] = e0;
        gr[h] = u2{a0, a1};
      }
    }
    st16<SC1>(left, ro, os2);
    const unsigned cs0 = chk(u4{gr[0][0], gr[0][1], gr[1][0], gr[1][1]}, chk(u4{gr[2][0], gr[2][1], gr[3][0], gr[3][1]}, 0));
    __builtin_amdgcn_raw_buffer_store_b32(chk(left, rowsum(cs0) + (unsigned)tag), ro, oh, 0, SC1);
  }
  if ((threadIdx.x & 255) == 192) post_flag(ws, (w < 4 ? F_A : F_L) + rt * 8 + hq, tag);
  STAMP(3);
  STAMPL(4);
  if (w >= 4) {
    __builtin_amdgcn_s_setprio(0);
    u64 fv = 0;
    GAS u64* fp = (GAS u64*)(ws + WS_FLAG + pc.fl * 128);
#pragma unroll
    for (int i = 0; i < 21; ++i) {
      if (i < 20 || w == 4) *(LAS u4*)(sm + L_WO + (4 * i + w - 4) * 1024 + lane * 16) = wr[i];
      if (i == 10 && w == 5) asm volatile("global_load_dwordx2 %0, %1, off sc1" : "+v"(fv) : "v"(fp) : "memory");
    }
    arrive(L_CNT + 20);
    if (w == 5) {
      asm volatile("s_waitcnt vmcnt(0)" : "+v"(fv) :: "memory");
      if (!__all(fv == tag)) wait_flags(ws, pc.fl, tag);
      arrive(L_CNT + 28);
    }
    STAMPL(30);
  }
}

DEV void role_b(lptr sm, char* ws, const u16* x, const u16* wqkv, const u16* wo, const u16* q, const u16* k, const u16* v,
                const u16* shk, const u16* hk, const u16* dv, int bid, int w, int lane) {
  const int tid = threadIdx.x, xcd = bid & 7, jb = (bid >> 3) - 16, me = xcd * 16 + jb;
  const int G = lane >> 4, i16 = lane & 15, w4 = w & 3;
  rsrc_t ru = mkrs(ws + WS_U, NB_ACT + NB_ACT / 4);
  const int xc = (xcd + XSH) & 7, cm = xc * 16 + jb;
  const int c5 = 512 + xcd * 8 + (jb & 7);
  const unsigned tb = L_WO + w * 9216;
  const u4 ny = {NOTYET, NOTYET, NOTYET, NOTYET};
  rsrc_t ry = mkrs(ws + WS_Y, NB_ACT);
  u64 tag;
  if (w < 4 || (w == 4 && jb < 8)) {
    const int grp = w < 4 ? xcd : 8, un = w < 4 ? jb * 4 + w : xcd * 8 + jb;
    const u4 rk = mkdesc(k, NB_ACT), rv = mkdesc(v, NB_ACT);
    const unsigned o0 = (un * 32 + (lane >> 3)) * 1152 + grp * 128 + (((lane & 7) ^ ((lane >> 4) & 1)) << 4);
    dma16(rk, tb, o0 - 8 * 1152, 0);
#pragma unroll
    for (int i = 0; i < 4; ++i) {
      dma16(rk, tb + 1024 + i * 1024, o0 ^ (((i + 1) & 3) << 5), i * 8 * 1152);
      dma16(rv, tb + 5120 + i * 1024, o0 ^ (i << 5), i * 8 * 1152);
      if (i == 0) dma16m(mkdesc(shk, 4608), L_OT + w * 512, lane * 16, grp * 512, 0xffffffffull);
    }
    const int q4 = i16 >> 2, p4 = lane & 3;
    const int f0 = (q4 >> 1) | (p4 << 1), f1 = (q4 >> 1) | (((p4 + 1) & 3) << 1);
    const unsigned a0 = tb + (8 * p4 + q4) * 128 + (((G >> 1) ^ f0) << 4) + (G & 1) * 8;
    const unsigned a1 = tb + (8 * p4 + q4) * 128 + (((G >> 1) ^ f1) << 4) + (G & 1) * 8;
    lptr wp = sm + L_OT + w * 512 + (G * 4 + (i16 & 3)) * 8;
    unsigned off = ((grp * 16 + G) * 256 + 4 * un) * 64 + i16 * 16;
    u2 K[3], V[2], wk;
    auto rd = [&](int s) {
      lptr b0 = (lptr)(size_t)(a0 ^ (s << 5)), b1 = (lptr)(size_t)(a1 ^ (s << 5));
      wk = *(LAS u2*)(wp + s * 128);
      K[0] = __builtin_bit_cast(u2, trread(b0 + 512));
      K[1] = __builtin_bit_cast(u2, trread(b1 + 1024));
      K[2] = __builtin_bit_cast(u2, trread(b1 + 1536));
      V[0] = __builtin_bit_cast(u2, trread(b0 + 5120));
      V[1] = __builtin_bit_cast(u2, trread(b0 + 5120 + 512));
    };
    tag = lds_tag(sm);
    const unsigned key = (unsigned)tag;
    WAITV(0);
    if (w < 4) arrive(L_CNT + 44);
    STAMP(1);
    rd(0);
#pragma unroll 1
    for (int s = 0; s < 4; ++s) {
      const unsigned wA = alignb(wk[1], wk[1]), wB = alignb(wk[0], wk[0]);
      const unsigned kk[6] = {K[0][0], K[0][1], K[1][0], K[1][1], K[2][0], K[2][1]};
      const unsigned vv[4] = {V[0][0], V[0][1], V[1][0], V[1][1]};
      rd((s + 1) & 3);
      unsigned sh[5];
      float ev[4], od[4], ve[4], vo[4];
#pragma unroll
      for (int d = 0; d < 5; ++d) sh[d] = alignb(kk[d + 1], kk[d]);
      SB();
#pragma unroll
      for (int d = 0; d < 4; ++d) { ev[d] = dot2z(sh[d], wA); od[d] = dot2z(kk[d + 1], wA); }
      SB();
#pragma unroll
      for (int d = 0; d < 4; ++d) { ev[d] = dot2a(sh[d + 1], wB, ev[d]); od[d] = dot2a(kk[d + 2], wB, od[d]); }
      SB();
#pragma unroll
      for (int d = 0; d < 4; ++d) { ve[d] = lo16(vv[d]); vo[d] = hi16(vv[d]); pin(ve[d]); pin(vo[d]); }
      SB();
      u4 o;
#pragma unroll
      for (int d = 0; d < 4; ++d) o[3 - d] = pk(od[d] * vo[d], ev[d] * ve[d]);
      st16<SC1>(o, ru, off);
      __builtin_amdgcn_raw_buffer_store_b32(chk(o, key), ru, NB_ACT + (off >> 2), 0, SC1);
      off += 65536;
      SB();
    }
    STAMP(2);
    if (w < 4) arrive(L_CNT + 40);
  } else if (w == 7) {
    const u4 rx = mkdesc(x, NB_ACT), rw = mkdesc(wqkv, NB_W);
    const int kr = (lane * 4370) >> 16, cc = lane - kr * 15, part = (cc * 52) >> 8;
    const unsigned fw = lane < 60 ? (jb * 4 + kr) * 3456 + part * (1152 - 640) + ((xcd & 1) * 4 + cc) * 128 : OOB;
    const unsigned fx = lane < 33 ? ((xcd >> 1) * 512 - 16 + jb * 33 + lane) * 1152 : OOB;
    __builtin_amdgcn_s_setprio(3);
    __builtin_amdgcn_s_sleep(PDELAY);
#pragma unroll 1
    for (int st = 1; st < 9; ++st) {
      if (st == PF1) reach(L_CNT + 44, 4);
      if (st == PFA) reach(L_CNT + 40, 4);
      if (st == PFB) reach(L_CNT + 4, 1);
      touch(rw, fw, st * 64 * 3456);
      touch(rx, fx, st * 128);
    }
    STAMPW(23, 7);
    reach(L_CNT + 4, 1);
    const u4 rq = mkdesc(q, NB_ACT);
    touch(rq, (jb * 128 + lane) * 1152 + ((xcd + YSH) & 7) * 128, 0);
    touch(rq, (jb * 128 + lane) * 1152 + ((xcd + YSH) & 7) * 128, 64 * 1152);
    if (jb >= 8) {
      const int un = bid - 192;
      touch(rq, lane < 32 ? ((un >> 2) * 128 + 64 * (un & 1) + 8 * ((un >> 1) & 1) + (lane >> 3) * 16 + (lane & 7)) * 1152 + 1024 : OOB, 0);
    }
    const unsigned y4 = (xc * 32 + (lane >> 5)) * 8192 + jb * 512 + (lane & 31) * 16;
#pragma unroll 1
    for (int i = 0; i < 16; i += 2) { st16<SC1>(ny, ry, y4 + i * 16384); st16<SC1>(ny, ry, y4 + i * 16384 + 16384); }
    if (jb >= 8) {
#pragma unroll
      for (int i = 0; i < 4; ++i) st16<SC1>(ny, ry, (256 + i * 8 + (lane >> 3)) * 8192 + (c5 & 63) * 128 + (lane & 7) * 16);
    }
    tag = lds_tag(sm);
    WAITV(0);
    arrive(L_CNT + 16);
    if (lane == 0) post_flag(ws, F_Y + cm, tag);
    STAMPW(25, 7);
    __builtin_amdgcn_s_sleep(12);
    if (jb >= 8) __builtin_amdgcn_s_sleep(16);
    wait_flags(ws, F_Y + (jb < 8 ? xcd * 16 + i16 : (lane >> 3) * 16 + 8 + (lane & 7)), tag);
    arrive(L_CNT + 24);
  } else if (w == 6) {
    __builtin_amdgcn_s_sleep(HDELAY);
    const u4 rh = mkdesc(hk, NB_ACT);
    const int nh = jb < 8 ? 4 : 5;
#pragma unroll 1
    for (int c = 0; c < nh; ++c) {
#pragma unroll
      for (int i = 0; i < 4; ++i) dma16(rh, c * CH_SZ + H_CP + 32 + i * 1024, lane * 16, (c < 4 ? cm * 4 + c : c5) * 4096 + i * 1024);
    }
    if (lane < 34) {
#pragma unroll
      for (int i = 0; i < 5; ++i)
        *(LAS u4*)(sm + i * CH_SZ + (lane < 30 ? U_OFF + lane * 16 : ((lane >> 1) & 1) * H_CP + (lane & 1) * 16)) = u4{0, 0, 0, 0};
    }
    WAITV(0);
#pragma unroll 1
    for (int c = 0; c < nh; ++c) {
      lptr hb = sm + c * CH_SZ + 32 + lane * 16;
      u4 ha[4];
      unsigned hp[4];
#pragma unroll
      for (int i = 0; i < 4; ++i) { ha[i] = *(LAS u4*)(hb + H_CP + i * 1024); hp[i] = *(LAS unsigned*)(hb + H_CP - 4 + i * 1024); }
#pragma unroll
      for (int i = 0; i < 4; ++i)
        *(LAS u4*)(hb + i * 1024) =
            u4{alignb(ha[i][0], hp[i]), alignb(ha[i][1], ha[i][0]), alignb(ha[i][2], ha[i][1]), alignb(ha[i][3], ha[i][2])};
    }
    arrive(L_CNT + 8);
    STAMPW(18, 6);
  } else {
    const int slot = w == 5 ? jb : 2 + jb;
    if (w == 5) {
      tag = get_tag(ws);
      if (lane == 0) *(LAS u64*)(sm + L_TAG) = tag;
      arrive(L_CNT);
      if (lane == 0) post_flag(ws, F_P + me, tag);
    } else {
      tag = lds_tag(sm);
    }
    if (w == 5 || jb >= 14) {
      const u4 rwo = mkdesc(wo, NB_WO);
      const int e4 = (lane * 57) >> 9;
      const unsigned vb = (e4 >> 1) * 1152 + xcd * 144 + (lane - e4 * 9) * 16, vt = (e4 & 1) * 1152;
      int ks = slot + ROTK * xcd;
      if (ks >= 18) ks -= 18;
#pragma unroll 1
      for (int i = 0; i < 8; ++i) {
        const int ci = 4 * ks + (i >> 1), h = (ci * 57) >> 9, c = ci - 9 * h;
        const int row = h * 72 + (c == 8 ? 16 : 36 * (c >> 2) + 4 * (c & 3)) + 2 * (i & 1);
        dma16m(rwo, tb + i * 576, vb + __umul24(vt, c == 8 ? 36 : (c & 3) ? 16 : 32), row * 1152, (1ull << 36) - 1);
      }
      WAITV(0);
      rsrc_t rpw = mkrs(ws + WS_WO, NB_WO);
      lptr p = (lptr)(size_t)tb + (G * 8 + ((i16 >> 2) << 1)) * 144 + (lane & 3) * 8;
      const unsigned po = (xcd * 18 + slot) * 4608;
      u4 f[5];
#pragma unroll
      for (int n = 0; n < 5; ++n) f[n] = __builtin_bit_cast(u4, cat8(trread(p + n * 32), trread(p + 144 + n * 32)));
#pragma unroll
      for (int n = 0; n < 4; ++n) st16<SC1>(f[n], rpw, po + n * 1024 + lane * 16);
      st16<SC1>(f[4], rpw, i16 < 8 ? po + 4096 + (G * 8 + i16) * 16 : OOB);
      WAITV(0);
      if (lane == 0) post_flag(ws, F_W + xcd * 32 + slot, tag);
      STAMPW(21, 5);
    }
  }
  const int r = lane & 15, g = lane >> 4;
  if (w >= 4) {
    if (w < 6) {
      const int ct = (bid >> 3) & 7;
      __builtin_amdgcn_s_setprio(2);
      if (w == 5) {
        reach(L_CNT + 4, 1);
        wait_flags(ws, F_W + ct * 32 + (lane < 18 ? lane : 0), tag);
      } else if (jb >= 8) {
        reach(L_CNT + 4, 1);
        u4 u5[4];
#pragma unroll 1
        for (int spin = 0; spin < SPIN_MAX; ++spin) {
          unsigned hx[4], bad = 0;
#pragma unroll
          for (int i = 0; i < 4; ++i) {
            u5[i] = ld16<SC1>(ru, lane * 64 + (c5 & 3) * 16, (c5 >> 2) * 16384 + i * 4096);
            hx[i] = __builtin_amdgcn_raw_buffer_load_b32(ru, lane * 16 + (c5 & 3) * 4, NB_ACT + (c5 >> 2) * 4096 + i * 1024, SC1);
          }
#pragma unroll
          for (int i = 0; i < 4; ++i) bad |= chk(u5[i], (unsigned)tag) ^ hx[i];
          if (__all(bad == 0)) break;
          asm volatile("s_sleep 4" ::: "memory");
        }
#pragma unroll
        for (int i = 0; i < 4; ++i) *(LAS u4*)(sm + U_OFF + 480 + 4 * CH_SZ + lane * 16 + i * 1024) = u5[i];
      }
      if (w == 4) arrive(L_CNT + 32);
      arrive(L_CNT + 44);
      reach(L_CNT + 44, 6);
      reach(L_CNT + 8, 5);
      STAMPL(16);
      const u4 rp = mkdesc(ws + WS_WO + ct * 82944, 82944);
#pragma unroll 1
      for (int i = (w & 1) * 1024; i < 81 * 1024; i += 2048) dma16c(rp, L_WO + i, lane * 16, i);
      WAITV(0);
      STAMPL(17);
    }
    arrive(L_CNT + 20);
    __builtin_amdgcn_s_setprio(0);
    return;
  }
  typedef const __attribute__((address_space(4))) unsigned* cptr;
  const unsigned dv0 = ((cptr)dv)[cm * 2 + (w >> 1)], dv1 = ((cptr)dv)[c5 >> 1];
  WAITV(0);
  __builtin_amdgcn_s_sleep(UDELAY);
  STAMP(3);
  {
    u4 uv[4];
#pragma unroll 1
    for (int spin = 0; spin < SPIN_MAX; ++spin) {
      unsigned hx[4], bad = 0;
#pragma unroll
      for (int i = 0; i < 4; ++i) {
        uv[i] = ld16<SC1>(ru, tid * 16, cm * 16384 + i * 4096);
        hx[i] = __builtin_amdgcn_raw_buffer_load_b32(ru, tid * 4, NB_ACT + cm * 4096 + i * 1024, SC1);
      }
#pragma unroll
      for (int i = 0; i < 4; ++i) bad |= chk(uv[i], (unsigned)tag) ^ hx[i];
      if (__all(bad == 0)) break;
      STAMP(20);
      asm volatile("s_sleep 1" ::: "memory");
    }
    STAMP(4);
    if (w == 3) arrive(L_CNT + 4);
#pragma unroll
    for (int i = 0; i < 4; ++i) *(LAS u4*)(sm + U_OFF + 480 + (tid & 3) * CH_SZ + (tid >> 2) * 16 + i * 1024) = uv[i];
  }
  arrive(L_CNT + 8);
  const int xd = 15 - r - 8 * g;
#pragma unroll 1
  for (int rnd = 0; rnd < (jb >= 8 ? 2 : 1); ++rnd) {
    const int dd0 = rnd ? 2 * w : 0, ch = rnd ? c5 : cm * 4 + w;
    const unsigned dval = dd0 == 0 ? ((rnd ? dv1 : dv0) >> ((ch & 1) * 16)) & 0xffffu : 0;
    lptr cb = sm + (rnd ? 4 : w) * CH_SZ;
    lptr aa[4];
#pragma unroll
    for (int i = 0; i < 4; ++i) { aa[i] = cb + (r & 1) * H_CP + 4 + 4 * (4 * g + (r >> 1)) + dd0 * 64 + i * 1024; pin(aa[i]); }
    lptr ba = cb + U_OFF + 16 * (31 + 2 * r - g) - 128 - dd0 * 64;
    pin(ba);
    u4 ad;
#pragma unroll
    for (int d = 0; d < 4; ++d) ad[d] = (xd >> 1) == d ? dval << ((xd & 1) * 16) : 0u;
    pin(ad);
    reach(rnd ? L_CNT + 32 : L_CNT + 8, rnd ? 1 : 5);
    STAMP(rnd ? 19 : 5);
    f4 acc[8];
    bf8 A[2][8], B[2][8];
    auto rd = [&](int dd, bf8 (&a)[8], bf8 (&b)[8]) {
#pragma unroll
      for (int i = 0; i < 8; ++i) {
        const LAS unsigned* p = (const LAS unsigned*)(aa[i >> 1] + dd * 64 + (i & 1) * 512);
        a[i] = asb(u4{p[0], p[1], p[2], p[3]});
        b[i] = *(const LAS bf8*)(ba + 128 - dd * 64 + i * 512);
      }
    };
    auto mm = [&](bf8 (&a)[8], bf8 (&b)[8]) {
#pragma unroll
      for (int i = 0; i < 8; ++i)
#pragma unroll
        for (int jj = 0; jj + i < 8; ++jj) acc[i + jj] = mfma(a[i], b[jj], acc[i + jj]);
#pragma unroll
      for (int i = 0; i < 24; ++i) { SGB(0x8, 1); SGB(0x100, 1); }
      SGB(0x8, 12);
      SB();
    };
    rd(0, A[0], B[0]);
#pragma unroll
    for (int n = 0; n < 8; ++n) acc[n] = mfma(asb(ad), B[0][n], f4{0.f, 0.f, 0.f, 0.f});
    SB();
#pragma unroll 1
    for (int it = rnd * 3; it < 4; ++it) {
      rd(1, A[1], B[1]);
      mm(A[0], B[0]);
      rd(2, A[0], B[0]);
      mm(A[1], B[1]);
#pragma unroll
      for (int i = 0; i < 4; ++i) aa[i] += 128;
      ba -= 128;
    }
    STAMP(6 + rnd);
    const unsigned yo = (((ch >> 6) * 16 + 2 * (g & 1) + (r >> 3)) * 2 + (g >> 1)) * 8192 + (ch & 63) * 128 + (r & 7) * 16;
    auto put = [&](f4 x, f4 y, int a) {
      auto s0 = __builtin_amdgcn_permlane16_swap(pk(x[0], x[1]), pk(y[0], y[1]), false, false);
      auto s1 = __builtin_amdgcn_permlane16_swap(pk(x[2], x[3]), pk(y[2], y[3]), false, false);
      st16<SC1>(u4{s0[0], s1[0], s0[1], s1[1]}, ry, yo + a * 65536);
    };
    if (rnd == 0) {
      reach(L_CNT + 16, 1);
#pragma unroll
      for (int a = 0; a < 4; ++a) put(acc[2 * a], acc[2 * a + 1], a);
    } else {
      LAS f4* mine = (LAS f4*)(sm + w * CH_SZ + lane * 16);
#pragma unroll
      for (int n = 0; n < 8; ++n) mine[n * 64] = acc[n];
      arrive(L_CNT + 16);
      reach(L_CNT + 16, 5);
      constexpr int CS = CH_SZ / 16;
      LAS f4* p = (LAS f4*)(sm + lane * 16) + w * 128;
      put((p[0] + p[CS]) + (p[2 * CS] + p[3 * CS]), (p[64] + p[CS + 64]) + (p[2 * CS + 64] + p[3 * CS + 64]), w);
    }
  }
  STAMP(8);
}

DEV void finish(lptr sm, lptr yt, char* ws, const u16* q, u16* out, int grp, int idx, int w4, int lane) {
  rsrc_t ry = mkrs(ws + WS_Y, NB_ACT), rq = mkrs(q, NB_ACT), rout = mkrs(out, 2 * NB_ACT);
  const int tile = idx + 8 * (w4 >> 1), orow = tile * 128 + 8 * (w4 & 1) + (lane >> 3);
  const unsigned qo = orow * 1152 + grp * 128 + (lane & 7) * 16, yo = ((grp * 16 + tile) * 2 + (w4 & 1)) * 8192 + lane * 16;
  u4 qv[8], yv[8];
#pragma unroll
  for (int i = 0; i < 8; ++i) qv[i] = ld16<0>(rq, qo, i * 16 * 1152);
  reach(L_CNT + 24, 1);
  f2 qf[8][4], yy[4][4];
#pragma unroll
  for (int i = 0; i < 16; ++i) { yy[i >> 2][i & 3] = f2{0.f, 0.f}; pin(yy[i >> 2][i & 3]); }
  asm volatile("s_waitcnt vmcnt(0)" : "+v"(qv[0]), "+v"(qv[1]), "+v"(qv[2]), "+v"(qv[3]), "+v"(qv[4]), "+v"(qv[5]),
               "+v"(qv[6]), "+v"(qv[7]) :: "memory");
  STAMP(26);
  __builtin_amdgcn_s_sleep(YDELAY);
#pragma unroll 1
  for (int spin = 0; spin < SPIN_MAX; ++spin) {
    unsigned mx = 0;
#pragma unroll
    for (int i = 0; i < 8; ++i) yv[i] = ld16<SC1>(ry, yo, i * 1024);
    SB();
#pragma unroll
    for (int i = 0; i < 32; ++i) {
      qf[i >> 2][i & 3] = f2{lo16(qv[i >> 2][i & 3]), hi16(qv[i >> 2][i & 3])};
      pin(qf[i >> 2][i & 3]);
    }
#pragma unroll
    for (int i = 0; i < 8; ++i) mx = max3u(max3u(mx, yv[i][0], yv[i][1]), yv[i][2], yv[i][3]);
    if (__all(mx != NOTYET)) break;
    STAMP(27 + (spin < 3 ? spin : 3));
    asm volatile("s_sleep 2" ::: "memory");
  }
  STAMP(12);
  unsigned oo = orow * 2304 + 1152 + grp * 128 + (lane & 7) * 16;
#pragma unroll
  for (int i = 0; i < 8; ++i) *(LAS u4*)(yt + lane * 16 + i * 1040) = yv[i];
  const unsigned p = (unsigned)(size_t)yt + (lane & 7) * 1040 + (lane >> 3) * 2;
#pragma unroll
  for (int t = 0; t < 2; ++t) {
#pragma unroll
    for (int i = 0; i < 16; ++i) {
      float a = yy[i >> 2][i & 3][0], b = yy[i >> 2][i & 3][1];
      asm volatile("ds_read_u16_d16_hi %0, %2 offset:%3\n\tds_read_u16_d16_hi %1, %2 offset:%4"
                   : "+v"(a), "+v"(b) : "v"(p), "i"(t * 64 + (i >> 2) * 16 + (i & 3) * 256), "i"(t * 64 + (i >> 2) * 16 + (i & 3) * 256 + 128)
                   : "memory");
      yy[i >> 2][i & 3] = f2{a, b};
    }
#pragma unroll
    for (int i = 0; i < 4; ++i) {
      if (i == 0) asm volatile("s_waitcnt lgkmcnt(14)" : "+v"(yy[0][0]), "+v"(yy[0][1]), "+v"(yy[0][2]), "+v"(yy[0][3]), "+v"(yy[1][0]), "+v"(yy[1][1]), "+v"(yy[1][2]), "+v"(yy[1][3]));
      if (i == 2) asm volatile("s_waitcnt lgkmcnt(0)" : "+v"(yy[2][0]), "+v"(yy[2][1]), "+v"(yy[2][2]), "+v"(yy[2][3]), "+v"(yy[3][0]), "+v"(yy[3][1]), "+v"(yy[3][2]), "+v"(yy[3][3]));
      u4 o;
#pragma unroll
      for (int d = 0; d < 4; ++d) { const f2 m = yy[i][d] * qf[t * 4 + i][d]; o[d] = pk(m[0], m[1]); }
      st16<0>(o, rout, oo + (t * 64 + i * 16) * 2304);
      SB();
    }
  }
}

DEV void finish8(lptr yt, char* ws, const u16* q, u16* out, int un, int w, int lane) {
  rsrc_t ry = mkrs(ws + WS_Y, NB_ACT), rq = mkrs(q, NB_ACT), rout = mkrs(out, 2 * NB_ACT);
  const int orow = (un >> 2) * 128 + 64 * (un & 1) + 8 * ((un >> 1) & 1) + 16 * w + (lane >> 3);
  u4 qv = ld16<0>(rq, orow * 1152 + 1024 + (lane & 7) * 16, 0), yv;
  reach(L_CNT + 24, 1);
  asm volatile("s_waitcnt vmcnt(0)" : "+v"(qv) :: "memory");
#pragma unroll 1
  for (int spin = 0; spin < SPIN_MAX; ++spin) {
    yv = ld16<SC1>(ry, lane * 128 + ((un & 1) * 4 + w) * 16, (256 + (un >> 1)) * 8192);
    if (__all(max4(yv, 0) != NOTYET)) break;
    asm volatile("s_sleep 1" ::: "memory");
  }
  STAMP(12);
  *(LAS u4*)(yt + (lane >> 3) * 144 + (lane & 7) * 16) = yv;
  WAITL0();
  const LAS u16* p = (const LAS u16*)(yt + (lane & 7) * 144 + (lane >> 3) * 2);
  u4 o;
#pragma unroll
  for (int d = 0; d < 4; ++d) o[d] = pk(lo16(p[d * 16]) * lo16(qv[d]), lo16(p[d * 16 + 8]) * hi16(qv[d]));
  st16<0>(o, rout, orow * 2304 + 1152 + 1024 + (lane & 7) * 16);
}

DEV void out_proj(lptr sm, char* ws, u16* out, int bid, int lw, int lane, const Proj& pc) {
  lptr wa[2] = {(lptr)(size_t)pc.wa, (lptr)(size_t)(pc.wa + 9 * 4608)};
  lptr wb[2] = {(lptr)(size_t)pc.wb, (lptr)(size_t)(pc.wb + 9 * 4608)};
  pin(wa[1]); pin(wb[1]);
  rsrc_t ro = mkrs(ws + WS_O, NB_ACT + 65536);
  const unsigned ob = pc.ob, oo[3] = {pc.oo0, pc.oo1, pc.oo2};
  lptr ot = (lptr)(size_t)pc.ot, ow = (lptr)(size_t)pc.ow;
  reach(L_CNT + 20, 4);
  f4 acc[5];
  const u64 tag = *(LAS u64*)(sm + L_TAG);
#pragma unroll 1
  for (int att = 0; att < 4096; ++att) {
    constexpr int PRE = 7, AHEAD = 9;
    bf8 wf[PRE][5];
    auto rdw = [&](int i) {
#pragma unroll
      for (int n = 0; n < 5; ++n)
        wf[i % PRE][n] = *(const LAS bf8*)((n < 4 ? wa[i / 9] + n * 1024 : wb[i / 9]) + (i % 9) * 4608);
    };
#pragma unroll
    for (int i = 0; i < PRE - 1; ++i) rdw(i);
    SB();
    if (lw == 0 && att == 0 && bid >= 128) {
      wait_flags(ws, pc.fl, tag);
      arrive(L_CNT + 28);
    }
    reach(L_CNT + 28, 1);
    STAMPL(att ? 31 : 9);
    u4 of[18];
    auto ldo = [&](int i) { of[i] = ld16<SC1>(ro, ob, i * 4096); };
#pragma unroll
    for (int i = 0; i < AHEAD; ++i) { ldo(i); SB(); }
#pragma unroll
    for (int n = 0; n < 5; ++n) acc[n] = f4{0.f, 0.f, 0.f, 0.f};
    unsigned sum = 2 * (unsigned)tag, h0, h1;
#pragma unroll
    for (int i = 0; i < 18; ++i) {
      if (i + PRE - 1 < 18) rdw(i + PRE - 1);
#pragma unroll
      for (int n = 0; n < 5; ++n) acc[n] = mfma(wf[i % PRE][n], asb(of[i]), acc[n]);
      sum = chk(of[i], sum);
      if (i + AHEAD < 18) ldo(i + AHEAD);
      if (i == 9) h0 = __builtin_amdgcn_raw_buffer_load_b32(ro, pc.oh, 0, SC1);
      if (i == 10) h1 = __builtin_amdgcn_raw_buffer_load_b32(ro, pc.oh + 256, 0, SC1);
      SGB(0x8, 1); SGB(0x20, 1); SGB(0x100, 1);
#pragma unroll
      for (int n = 1; n < 5; ++n) { SGB(0x8, 1); SGB(0x100, 1); SGB(0x2, 1); }
      SB();
    }
    if (__all(rowsum(sum - h0 - h1) == 0)) break;
    asm volatile("" ::: "memory");
  }
  STAMPL(10);
#pragma unroll
  for (int n = 0; n < 5; ++n)
    if (n < 4 || lane < 32) *(LAS u2*)(ow + n * 32) = u2{pk(acc[n][0], acc[n][1]), pk(acc[n][2], acc[n][3])};
  WAITL0();
  rsrc_t rout = mkrs(out, 2 * NB_ACT);
#pragma unroll
  for (int i = 0; i < 3; ++i) st16<0>(*(const LAS u4*)(ot + i * 1024), rout, oo[i]);
  STAMPL(15);
}

__global__ __launch_bounds__(512) __attribute__((amdgpu_waves_per_eu(2, 2))) void fused(
    const u16* x, const u16* wqkv, const u16* wo, const u16* q, const u16* k, const u16* v, const u16* shk,
    const u16* hk, const u16* dv, u16* out, char* ws) {
  extern __shared__ __attribute__((aligned(16))) char smem[];
  lptr sm = (lptr)smem;
  const int tid = threadIdx.x, lane = tid & 63, w = __builtin_amdgcn_readfirstlane(tid >> 6);
  const int bid = blockIdx.x;
  if (bid < 128 && (bid >> 3) + 16 * tid < CODE_LINES) {
    u64 pc;
    asm volatile("s_getpc_b64 %0" : "=s"(pc));
    asm volatile("s_mov_b32 m0, %0\n\ts_nop 0\n\tglobal_load_lds_dword %1, %2"
                 :: "s"(L_PF), "v"(((bid >> 3) + 16 * tid) * 128), "s"(pc & ~127ull) : "memory");
  }
  if (bid >= 128) {
    if (tid < 12) *(LAS unsigned*)(sm + L_CNT + tid * 4) = 0;
    bar();
  }
  Proj pc;
  if (__builtin_expect(bid < 128, 1)) role_a(sm, ws, x, wqkv, bid, w, lane, pc);
  else {
    role_b(sm, ws, x, wqkv, wo, q, k, v, shk, hk, dv, bid, w, lane);
    if (w >= 4) proj_consts(pc, bid, w & 3, lane);
    else asm volatile("" : "=v"(pc.wa), "=v"(pc.wb), "=v"(pc.ob), "=v"(pc.oh), "=v"(pc.oo0), "=v"(pc.oo1), "=v"(pc.oo2),
                      "=v"(pc.ot), "=v"(pc.ow), "=v"(pc.fl));
  }
  if (w >= 4) {
    out_proj(sm, ws, out, bid, w & 3, lane, pc);
  } else if (bid >= 128) {
    if (bid >= 192) finish8(sm + w * CH_SZ + U_OFF, ws, q, out, bid - 192, w, lane);
    else finish(sm, sm + w * CH_SZ, ws, q, out, bid & 7, (bid >> 3) - 16, w, lane);
    STAMP(13);
  } else {
    if (bid == 64 && w == 1) {
      const u64 tag = *(LAS u64*)(sm + L_TAG);
      GAS u64* p = (GAS u64*)(ws + WS_FLAG + lane * 128);
#pragma unroll 1
      for (int spin = 0; spin < SPIN_MAX; ++spin) {
        const u64 a = __hip_atomic_load(p, RLX_AGENT), b = __hip_atomic_load(p + 1024, RLX_AGENT);
        const u64 c = __hip_atomic_load(p + 2048, RLX_AGENT), d = __hip_atomic_load(p + 3072, RLX_AGENT);
        if (__all(a == tag && b == tag && c == tag && d == tag)) break;
        __builtin_amdgcn_s_sleep(PSLEEP);
      }
      if (lane == 0) __hip_atomic_store((GAS u64*)ws, tag, RLX_AGENT);
    }
  }
}

extern "C" void kernel_launch(void* const* d_in, const int* in_sizes, int n_in, void* d_out, int out_size, void* d_ws,
                              size_t ws_size, hipStream_t stream) {
  (void)hipFuncSetAttribute((const void*)fused, hipFuncAttributeMaxDynamicSharedMemorySize, LDS_BYTES);
  fused<<<256, 512, LDS_BYTES, stream>>>((const u16*)d_in[0], (const u16*)d_in[1], (const u16*)d_in[2], (const u16*)d_in[3],
                                         (const u16*)d_in[4], (const u16*)d_in[5], (const u16*)d_in[6], (const u16*)d_in[7],
                                         (const u16*)d_in[8], (u16*)d_out, (char*)d_ws);
}
```

```cpp
#include <hip/hip_runtime.h>

typedef unsigned short u16;
typedef unsigned long long u64;
typedef short s4 __attribute__((ext_vector_type(4)));
typedef __bf16 bf2 __attribute__((ext_vector_type(2)));
typedef __bf16 bf8 __attribute__((ext_vector_type(8)));
typedef float f2 __attribute__((ext_vector_type(2)));
typedef float f4 __attribute__((ext_vector_type(4)));
typedef unsigned u4 __attribute__((ext_vector_type(4)));
typedef unsigned u2 __attribute__((ext_vector_type(2)));
typedef __amdgpu_buffer_rsrc_t rsrc_t;
#define LAS __attribute__((address_space(3)))
#define GAS __attribute__((address_space(1)))
typedef LAS char* lptr;
#define RLX_AGENT __ATOMIC_RELAXED, __HIP_MEMORY_SCOPE_AGENT
#define DEV __device__ __forceinline__

constexpr int NB_ACT = 2048 * 576 * 2;
constexpr int NB_W = 576 * 1728 * 2;
constexpr int NB_WO = 576 * 576 * 2;
constexpr unsigned OOB = 0x80000000u;
constexpr unsigned NOTYET = 0xffffffffu;
constexpr int SC1 = 16;
constexpr int CODE_LINES = 248;
#define ROTK 0
#define PFS 12
#define TDELAY 0
#define YSH 0
#define PF0 2
#define HDELAY 6
#define PFA 6
#define PFB 9
#define PDELAY 0
#define EARLYHINT 1
#define HSLOT 0
#define PSLOT 0
#define PF1 4
#define LPRIO 0
#define HSWAP 1
#define UDELAY 1
#define YDELAY 0
#define OA1 2
#define OA2 0
#define PFS0 2
#define PFE 0
#define XSH 1
#define OSH 2
#define PSLEEP 2
constexpr int SPIN_MAX = 1 << 15;

constexpr int WS_FLAG = 4096;
constexpr int F_A = 0, F_P = 128, F_Y = 256, F_Q = 512, F_W = 640, F_G = 896, F_L = 960;
constexpr int WS_U = 1 << 20;
constexpr int WS_Y = WS_U + (3 << 20);
constexpr int WS_O = WS_Y + (3 << 20);
constexpr int WS_WO = WS_O + (3 << 20);
constexpr int WS_DBG = 16 << 20;

constexpr int LDS_BYTES = 163840;
constexpr int XB = 144 * 128;
constexpr int SLOT = XB + 64 * 432;
constexpr int PS = 1160, HS = 32640;
constexpr int L_WO = 65152;
constexpr int L_OT = L_WO + 83200;
constexpr int H_CP = 4160, U_OFF = 8448, CH_SZ = 13024;
constexpr int L_TAG = L_OT + 10240;
constexpr int L_CNT = L_TAG + 16;
constexpr int L_YA = L_TAG + 128;
constexpr int L_PF = LDS_BYTES - 256;

#define STAMPW(i, wv) do {} while (0)
#define STAMP(i) STAMPW(i, 0)
#define STAMPL(i) STAMPW(i, 4)

DEV rsrc_t mkrs(const void* p, int bytes) { return __builtin_amdgcn_make_buffer_rsrc((void*)p, 0, bytes, 0x00020000); }
DEV u4 mkdesc(const void* p, int bytes) {
  u64 a = (u64)p;
  return u4{(unsigned)a, (unsigned)(a >> 32) & 0xffffu, (unsigned)bytes, 0x00020000u};
}
DEV void dma16(u4 r, unsigned lds, unsigned voff, unsigned soff) {
  asm volatile("s_mov_b32 m0, %0\n\ts_nop 0\n\tbuffer_load_dwordx4 %1, %2, %3 offen lds"
               :: "s"(lds), "v"(voff), "s"(r), "s"(soff) : "memory");
}
DEV void dma16c(u4 r, unsigned lds, unsigned voff, unsigned soff) {
  asm volatile("s_mov_b32 m0, %0\n\ts_nop 0\n\tbuffer_load_dwordx4 %1, %2, %3 offen sc1 lds"
               :: "s"(lds), "v"(voff), "s"(r), "s"(soff) : "memory");
}
DEV void dma16m(u4 r, unsigned lds, unsigned voff, unsigned soff, u64 mask) {
  asm volatile("s_mov_b32 m0, %0\n\ts_mov_b64 exec, %4\n\tbuffer_load_dwordx4 %1, %2, %3 offen lds\n\ts_mov_b64 exec, -1"
               :: "s"(lds), "v"(voff), "s"(r), "s"(soff), "s"(mask) : "memory");
}
template <int AUX> DEV void st16(u4 v, rsrc_t r, unsigned off) { __builtin_amdgcn_raw_buffer_store_b128(v, r, off, 0, AUX); }
template <int AUX> DEV u4 ld16(rsrc_t r, unsigned off, unsigned soff) { return __builtin_amdgcn_raw_buffer_load_b128(r, off, soff, AUX); }
DEV u4 ld16c(u4 r, unsigned voff, unsigned soff) {
  u4 v;
  asm volatile("buffer_load_dwordx4 %0, %1, %2, %3 offen sc1" : "=v"(v) : "v"(voff), "s"(r), "s"(soff) : "memory");
  return v;
}
DEV void touch(u4 r, unsigned voff, unsigned soff) {
  asm volatile("s_mov_b32 m0, %0\n\ts_nop 0\n\tbuffer_load_dword %1, %2, %3 offen lds"
               :: "s"(L_PF), "v"(voff), "s"(r), "s"(soff) : "memory");
}
DEV s4 trread(lptr p) { return __builtin_amdgcn_ds_read_tr16_b64_v4i16((LAS s4*)p); }
DEV bf8 cat8(s4 a, s4 b) { return __builtin_bit_cast(bf8, __builtin_shufflevector(a, b, 0, 1, 2, 3, 4, 5, 6, 7)); }
DEV bf8 asb(u4 v) { return __builtin_bit_cast(bf8, v); }
DEV f4 mfma(bf8 a, bf8 b, f4 c) { return __builtin_amdgcn_mfma_f32_16x16x32_bf16(a, b, c, 0, 0, 0); }
DEV unsigned pk(float lo, float hi) { f2 v = {lo, hi}; return __builtin_bit_cast(unsigned, __builtin_convertvector(v, bf2)); }
DEV float asf(unsigned u) { return __builtin_bit_cast(float, u); }
DEV unsigned asu(float f) { return __builtin_bit_cast(unsigned, f); }
DEV float lo16(unsigned x) { return asf(x << 16); }
DEV float hi16(unsigned x) { return asf(x & 0xffff0000u); }
DEV float dot2(unsigned a, unsigned b, float c) {
  return __builtin_amdgcn_fdot2_f32_bf16(__builtin_bit_cast(bf2, a), __builtin_bit_cast(bf2, b), c, false);
}
DEV float dot2z(unsigned a, unsigned b) {
  float d;
  asm("v_dot2_f32_bf16 %0, %1, %2, 0" : "=v"(d) : "v"(a), "v"(b));
  return d;
}
DEV float dot2a(unsigned a, unsigned b, float c) {
  float d;
  asm("v_dot2_f32_bf16 %0, %1, %2, %3" : "=v"(d) : "v"(a), "v"(b), "v"(c));
  return d;
}
DEV unsigned alignb(unsigned hi, unsigned lo) { return __builtin_amdgcn_alignbit(hi, lo, 16); }
DEV unsigned umax(unsigned a, unsigned b) { return a > b ? a : b; }
DEV unsigned max4(u4 v, unsigned m) { return umax(umax(umax(v[0], v[1]), m), umax(v[2], v[3])); }
DEV unsigned max3u(unsigned a, unsigned b, unsigned c) { return umax(umax(a, b), c); }
DEV unsigned max12(u4 a, u4 b, u4 c) {
  return umax(max3u(max3u(max3u(a[0], a[1], a[2]), a[3], b[0]), b[1], b[2]), max3u(max3u(b[3], c[0], c[1]), c[2], c[3]));
}
DEV unsigned chk(u4 v, unsigned key) { return (v[0] + v[1] + v[2]) + (v[3] + key); }
DEV unsigned rowsum(unsigned x) {
  auto a = __builtin_amdgcn_permlane16_swap(x, x, false, false);
  const unsigned y = a[0] + a[1];
  auto b = __builtin_amdgcn_permlane32_swap(y, y, false, false);
  return b[0] + b[1];
}
DEV float fmax2(float a, float b) { return __builtin_elementwise_maximum(a, b); }
#define WAITV(n) asm volatile("s_waitcnt vmcnt(" #n ")" ::: "memory")
#define WAITL0() asm volatile("s_waitcnt lgkmcnt(0)" ::: "memory")
#define SB() __builtin_amdgcn_sched_barrier(0)
#define SGB(m, n) __builtin_amdgcn_sched_group_barrier(m, n, 0)
DEV void bar() { WAITL0(); __builtin_amdgcn_s_barrier(); asm volatile("" ::: "memory"); }
template <class T> DEV void pin(T& x) { asm volatile("" : "+v"(x)); }

DEV u64 get_tag(char* ws) {
  u64 e;
  asm volatile("s_load_dwordx2 %0, %1, 0x0\n\ts_waitcnt lgkmcnt(0)" : "=s"(e) : "s"(ws) : "memory");
  return e * 6364136223846793005ull + 1442695040888963407ull;
}
DEV void post_flag(char* ws, int idx, u64 tag) { __hip_atomic_store((GAS u64*)(ws + WS_FLAG + idx * 128), tag, RLX_AGENT); }
DEV void wait_flags(char* ws, int idx, u64 tag) {
  GAS u64* p = (GAS u64*)(ws + WS_FLAG + idx * 128);
#pragma unroll 1
  for (int spin = 0; spin < SPIN_MAX; ++spin) {
    if (__all(__hip_atomic_load(p, RLX_AGENT) == tag)) break;
    __builtin_amdgcn_s_sleep(PSLEEP);
  }
  __builtin_amdgcn_fence(__ATOMIC_ACQUIRE, "wavefront");
}

DEV void arrive(unsigned word) {
  unsigned one = 1;
  asm volatile("s_waitcnt lgkmcnt(0)\n\ts_mov_b64 exec, 1\n\tds_add_u32 %0, %1\n\ts_mov_b64 exec, -1"
               :: "v"(word), "v"(one) : "memory");
}
DEV void reach(unsigned word, unsigned n) {
#pragma unroll 1
  for (int spin = 0; spin < (1 << 22); ++spin) {
    unsigned v;
    asm volatile("ds_read_b32 %0, %1\n\ts_waitcnt lgkmcnt(0)" : "=v"(v) : "v"(word) : "memory");
    if ((unsigned)__builtin_amdgcn_readfirstlane(v) >= n) break;
    __builtin_amdgcn_s_sleep(1);
  }
}

DEV u4 ld2(lptr p) {
  const u2 a = *(const LAS u2*)p, b = *(const LAS u2*)(p + HS);
  return u4{a[0], a[1], b[0], b[1]};
}
DEV u64 lds_tag(lptr sm) {
  reach(L_CNT, 1);
  return *(LAS u64*)(sm + L_TAG);
}
DEV void h_dma(const u16* hk, int c, int ch, int lane) {
  const u4 rh = mkdesc(hk, NB_ACT);
#pragma unroll
  for (int i = 0; i < 4; ++i) dma16(rh, c * CH_SZ + H_CP + 32 + i * 1024, lane * 16, ch * 4096 + i * 1024);
}
DEV void h_shift(lptr cb, int lane) {
  if (lane < 34) *(LAS u4*)(cb + (lane < 30 ? U_OFF + lane * 16 : ((lane >> 1) & 1) * H_CP + (lane & 1) * 16)) = u4{0, 0, 0, 0};
  lptr hb = cb + 32 + lane * 16;
  u4 ha[4];
  unsigned hp[4];
#pragma unroll
  for (int i = 0; i < 4; ++i) { ha[i] = *(LAS u4*)(hb + H_CP + i * 1024); hp[i] = *(LAS unsigned*)(hb + H_CP - 4 + i * 1024); }
#pragma unroll
  for (int i = 0; i < 4; ++i)
    *(LAS u4*)(hb + i * 1024) =
        u4{alignb(ha[i][0], hp[i]), alignb(ha[i][1], ha[i][0]), alignb(ha[i][2], ha[i][1]), alignb(ha[i][3], ha[i][2])};
}
DEV void att_consts(lptr sm, int lane, u4 (&tc)[9], int first) {
  const LAS u4* tb = (const LAS u4*)(sm + L_OT) + lane;
#pragma unroll
  for (int i = 0; i < 9; ++i) tc[i] = tb[(i < 4 ? i : i == 4 ? 4 + first : i + 1) * 64];
}

struct Proj { unsigned wa, wb, ob, oh, oo0, oo1, oo2, ot, ow, fl; };
DEV void proj_consts(Proj& p, int bid, int lw, int lane) {
  const int rt = ((bid + OSH) & 7) * 4 + ((bid >> 6) & 1) * 2 + (bid >> 7), ct = (bid >> 3) & 7;
  const int r = lane & 15, g = lane >> 4;
  p.wa = L_WO + lane * 16;
  p.wb = L_WO + 4096 + (g * 8 + (r & 7)) * 16;
  p.ob = rt * 72 * 1024 + g * 1024 + lw * 256 + r * 16;
  p.oh = NB_ACT + (rt * 4 + lw) * 512 + lane * 4;
  pin(p.oh);
  unsigned oo[3];
#pragma unroll
  for (int i = 0; i < 3; ++i) {
    const int it = lane + 64 * i, row = (it * 57) >> 9, c = it - row * 9;
    oo[i] = (i == 2 && lane >= 16) ? OOB : (rt * 64 + 32 * (lw >> 1) + (lw & 1)) * 2304 + __umul24(row, 4608) + ct * 144 + c * 16;
  }
  p.oo0 = oo[0]; p.oo1 = oo[1]; p.oo2 = oo[2];
  p.ot = L_OT + lw * 2304 + lane * 16;
  p.ow = L_OT + lw * 2304 + r * 144 + g * 8;
  p.fl = ((rt & 1) ? F_A : F_L) + (rt >> 1) * 8 + (lane & 7);
  pin(p.wa); pin(p.wb); pin(p.ob); pin(p.oo0); pin(p.oo1); pin(p.oo2); pin(p.ot); pin(p.ow); pin(p.fl);
}

template <int HI>
DEV void g1_compute(lptr sm, int n0, int lane, u4 (&tc)[9], int first) {
  const int r = lane & 15, g = lane >> 4;
  lptr xa0 = sm + r * 128 + ((g ^ ((r >> 1) & 7)) << 4);
  lptr xa1 = (lptr)((size_t)xa0 ^ 64);
  lptr wa = sm + XB + (((g >> 1) << 4) + ((g & 1) << 3) + ((r >> 2) << 1)) * 432 + (lane & 3) * 8 + n0 * 32;
#define USED(m, n) (HI ? ((n) > 0 || (m) >= 5) : ((n) < 3 || (m) < 5))
  f4 acc[9][4];
#pragma unroll
  for (int m = 0; m < 9; ++m)
#pragma unroll
    for (int n = 0; n < 4; ++n) {
      acc[m][n] = f4{0.f, 0.f, 0.f, 0.f};
      if (USED(m, n)) pin(acc[m][n]);
    }
  bf8 xf[9], wf[2][4];
  auto mm = [&](bf8 (&wv)[4], int m) {
#pragma unroll
    for (int n = 0; n < 4; ++n)
      if (USED(m, n)) acc[m][n] = mfma(wv[n], xf[m], acc[m][n]);
  };
  auto rdx = [&](lptr xp, int m) { xf[m] = *(const LAS bf8*)(xp + m * 2048); };
  auto rdw = [&](lptr wp, bf8 (&wv)[4], int n) { wv[n] = cat8(trread(wp + n * 32), trread(wp + 432 + n * 32)); };
  auto mix = [&]() {
#pragma unroll
    for (int i = 0; i < 4; ++i) { SGB(0x8, 1); SGB(0x100, 1); }
    SB();
  };
  auto step = [&](bf8 (&wc)[4], bf8 (&wn)[4], lptr xp, lptr wp, int m0) {
#pragma unroll
    for (int m = m0; m < 9; ++m) {
      mm(wc, m);
      if (m - m0 < 4) rdw(wp, wn, m - m0);
      if (m0 == 0) { if (m > 0) rdx(xp, m - 1); }
      else if (m - m0 < 4) rdx(xp, m - m0);
      else { rdx(xp, 2 * (m - m0) - 4); if (2 * (m - m0) - 3 < m) rdx(xp, 2 * (m - m0) - 3); }
      mix();
    }
#pragma unroll
    for (int m = (m0 == 0 ? 8 : 2 * (9 - m0) - 4); m < 9; ++m) rdx(xp, m);
    SB();
  };
  constexpr int MS = 3;
  bar();
#pragma unroll
  for (int n = 0; n < 4; ++n) rdw(wa, wf[0], n);
#pragma unroll
  for (int m = 0; m < 9; ++m) rdx(xa0, m);
  SB();
  int so = 0;
#pragma unroll 1
  for (int k = 0; k < 8; ++k) {
    step(wf[0], wf[1], xa1 + so, wa + so + 32 * 432, 0);
    so = so == 2 * SLOT ? 0 : so + SLOT;
#pragma unroll
    for (int m = 0; m < MS; ++m) mm(wf[1], m);
    SB();
    bar();
    step(wf[1], wf[0], xa0 + so, wa + so, MS);
  }
  xa1 += 2 * SLOT; wa += 2 * SLOT + 32 * 432;
  pin(xa1); pin(wa);
  STAMP(1);
  lptr d = sm + (2 * n0 + (g >> 1)) * PS + r * 8 + (g & 1) * HS;
  bf8 xg[9];
#pragma unroll
  for (int m = 0; m < 11; ++m) {
    if (m < 9) mm(wf[0], m);
    if (m < 2) { rdw(wa, wf[1], 2 * m); rdw(wa, wf[1], 2 * m + 1); }
    if (m < 9) xg[m] = *(const LAS bf8*)(xa1 + m * 2048);
    if (m >= 2) {
#pragma unroll
      for (int n = 0; n < 4; ++n)
        if (USED(m - 2, n)) acc[m - 2][n] = mfma(wf[1][n], xg[m - 2], acc[m - 2][n]);
    }
    if (m == 7) att_consts(sm, lane, tc, first);
    if (m == 8) arrive(L_CNT + 8);
#pragma unroll
    for (int o = m - 3; o < (m == 10 ? 9 : m - 2); ++o) {
      if (o >= 0) {
#pragma unroll
        for (int n = 0; n < 4; ++n)
          if (USED(o, n))
            *(LAS u2*)(d + n * 2 * PS + o * 128) = u2{pk(acc[o][n][0], acc[o][n][1]), pk(acc[o][n][2], acc[o][n][3])};
      }
    }
    if (m < 3) {
#pragma unroll
      for (int i = 0; i < 4; ++i) { SGB(0x8, 1); SGB(0x100, 2); }
    } else {
      SGB(0x8, 1); SGB(0x100, 1);
#pragma unroll
      for (int i = 0; i < 4; ++i) { SGB(0x8, 1); SGB(0x2, 2); SGB(0x8, 1); SGB(0x200, 1); }
    }
    SB();
  }
}

DEV void role_a(lptr sm, char* ws, const u16* x, const u16* wqkv, int bid, int w, int lane, Proj& pc, unsigned vw) {
  const int xcd = bid & 7, j = bid >> 3;
  const int rt = (xcd >> 1) * 4 + (j >> 2), hq = (xcd & 1) * 4 + (j & 3);
  const u4 rp = mkdesc(ws + WS_WO + (j & 7) * 82944, 82944);
  u64 tag;
  u4 tc[9], wr[21];
  const int aw = w ^ 4;
  const int first = rt + (aw >> 1) == 0;
  if (__builtin_expect(w >= 4, 1)) {
    const int lw = w - 4;
    const u4 rx = mkdesc(x, NB_ACT), rw = mkdesc(wqkv, NB_W);
    auto wpiece = [&](unsigned d, int kt, int i) {
      dma16m(rw, d + XB + (lw * 8 + i) * 864, vw, (kt * 64 + lw * 16 + 8 * (i >> 2) + (i & 3)) * 3456, (1ull << 54) - 1);
    };
    auto tailp = [&](unsigned d, int kt) {
#pragma unroll
      for (int i = 2; i < 8; ++i) wpiece(d, kt, i);
    };
    STAMPL(16);
    tailp(0, 0);
    SB();
    unsigned vx[6];
#pragma unroll
    for (int i = 0; i < 6; ++i) {
      const int xr = lw * 36 + i * 6 + (lane >> 3);
      vx[i] = __umul24(rt * 128 + xr, 1152) + (((lane & 7) ^ ((xr >> 1) & 7)) << 4) - 16 * 1152;
    }
    auto headp = [&](unsigned d, int kt) {
#pragma unroll
      for (int i = 0; i < 6; ++i) dma16m(rx, d + (lw * 6 + i) * 768, vx[i], kt * 128, (1ull << 48) - 1);
#pragma unroll
      for (int i = 0; i < 2; ++i) wpiece(d, kt, i);
    };
    headp(0, 0);
    unsigned d = SLOT;
    const int fi = F_W + (j & 7) * 32 + (lane < 18 ? lane : 0);
    GAS u64* fp = (GAS u64*)(ws + WS_FLAG + fi * 128);
    u64 fw = 0;
#pragma unroll 1
    for (int k = 1; k < 9; ++k) {
      headp(d, k);
      WAITV(8);
      bar();
      STAMPL(16 + k);
      if (k == 8 && lw == 3) asm volatile("global_load_dwordx2 %0, %1, off sc1" : "+v"(fw) : "v"(fp) : "memory");
      tailp(d, k);
      d = d == 2 * SLOT ? 0 : d + SLOT;
    }
    STAMPL(26);
    tag = *(LAS u64*)(sm + L_TAG);
    att_consts(sm, lane, tc, first);
    proj_consts(pc, bid, lw, lane);
    asm volatile("s_waitcnt vmcnt(0)" : "+v"(fw) :: "memory");
    if (lw == 3 && !__all(fw == tag)) wait_flags(ws, fi, tag);
    bar();
    STAMPL(27);
#pragma unroll
    for (int i = 0; i < 21; ++i) wr[i] = ld16c(rp, lane * 16, (4 * i + lw) * 1024);
    __builtin_amdgcn_s_setprio(LPRIO);
    STAMPL(28);
  } else {
    tag = get_tag(ws);
    if (threadIdx.x == 0) { *(LAS u64*)(sm + L_TAG) = tag; *(LAS u4*)(sm + L_CNT) = u4{0, 0, 0, 0}; *(LAS u4*)(sm + L_CNT + 16) = u4{0, 0, 0, 0}; *(LAS u4*)(sm + L_CNT + 32) = u4{0, 0, 0, 0}; }
    {
      const int r = lane & 15, g = lane >> 4, q4 = r >> 2, p4 = lane & 3;
      LAS u4* tb = (LAS u4*)(sm + L_OT) + lane;
      u4 t0, t1;
      if (w < 2) {
#pragma unroll
        for (int dd = 0; dd < 4; ++dd) {
          t0[dd] = __builtin_amdgcn_sbfe((int)(0x1ffu << (2 * w)), 4 * g + dd, 1);
          t1[dd] = __builtin_amdgcn_sbfe((int)(0x3feu << (2 * w)), 4 * g + dd, 1);
        }
      } else if (w == 2) {
#pragma unroll
        for (int dd = 0; dd < 4; ++dd) {
          const int kk = 4 * g + dd - r;
          t0[dd] = (kk >= 1 && kk <= 8) ? 0u : 0xff800000u;
          t1[dd] = (kk >= 1 && kk <= 8 && g >= 2) ? 0u : 0xff800000u;
        }
      } else {
#pragma unroll
        for (int dd = 0; dd < 4; ++dd) t0[dd] = 4 * g + dd - r <= -8 ? 0u : 0xff800000u;
        t1[0] = (t0[0] ? 0u : 0xffffu) | (t0[1] ? 0u : 0xffff0000u);
        t1[1] = (t0[2] ? 0u : 0xffffu) | (t0[3] ? 0u : 0xffff0000u);
        t1[2] = g * PS + r * 16;
        t1[3] = g * PS + (r & 7) * 16;
        const int kr0 = (4 * g + q4) * 16, kr1 = kr0 + (g >= 2 ? 128 : 256);
        const int ve = (p4 >> 1) * PS + (p4 & 1) * HS, vo = ((p4 + 1) >> 1) * PS + ((p4 + 1) & 1) * HS;
        tb[8 * 64] = u4{(unsigned)(kr0 + ve), (unsigned)(kr1 + ve), (unsigned)(kr0 + vo), (unsigned)(kr1 + vo)};
        const unsigned os = (rt * 144 + hq * 9 + g) * 1024 + r * 16;
        tb[9 * 64] = u4{(unsigned)kr0, (unsigned)kr1, os, g ? OOB : os + 8192};
      }
      tb[2 * w * 64] = t0;
      tb[(2 * w + 1) * 64] = t1;
    }
    if (w & 1) g1_compute<1>(sm, 3 + (w >> 1) * 7, lane, tc, first);
    else g1_compute<0>(sm, (w >> 1) * 7, lane, tc, first);
#pragma unroll
    for (int i = 0; i < 21; ++i) asm volatile("" : "=v"(wr[i]));
    asm volatile("" : "=v"(pc.wa), "=v"(pc.wb), "=v"(pc.ob), "=v"(pc.oh), "=v"(pc.oo0), "=v"(pc.oo1), "=v"(pc.oo2),
                 "=v"(pc.ot), "=v"(pc.ow), "=v"(pc.fl));
  }
  {
    const unsigned rb = (32 * (aw >> 1) + (aw & 1)) * 8;
    const u4 qm[4] = {tc[0], tc[1], tc[2], tc[3]}, u0 = tc[4], u1 = tc[5], c0 = tc[6], c1 = tc[7], c2 = tc[8];
    const f4 b0 = __builtin_bit_cast(f4, u0), b1 = __builtin_bit_cast(f4, u1);
    const unsigned m01 = c0[0], m23 = c0[1];
    lptr qa = sm + rb + c0[2], k1 = sm + rb + c0[3];
    lptr ve0 = sm + rb + c1[0], ve1 = sm + rb + c1[1], vo0 = sm + rb + c1[2], vo1 = sm + rb + c1[3];
    lptr vk0 = sm + rb + c2[0], vk1 = sm + rb + c2[1];
    const unsigned ow = (aw >> 2) * 73728 + (aw & 3) * 256;
    unsigned os0 = c2[2] + ow, os1 = os0 + 4096, os2 = c2[3] + ow;
    const bool g0 = lane < 16;
    rsrc_t ro = mkrs(ws + WS_O, NB_ACT + 65536);
    unsigned oh = g0 ? NB_ACT + ((rt * 8 + aw) * 8 + hq) * 64 + lane * 4 : OOB;
    pin(oh);
    u4 ones = {0x3f803f80u, 0x3f803f80u, 0x3f803f80u, 0x3f803f80u};
    pin(qa); pin(k1); pin(ve0); pin(ve1); pin(vo0); pin(vo1); pin(vk0); pin(vk1); pin(os0); pin(os1); pin(os2); pin(ones);
    STAMP(14);
    if (w < 4) arrive(L_CNT);
    reach(w < 4 ? L_CNT : L_CNT + 8, 4);
    STAMP(2);
    STAMPL(29);
    const float cs = 0.23570226039551584f * 1.4426950408889634f;
    const f4 z = {0.f, 0.f, 0.f, 0.f};
    u4 qf[4];
    bf8 ka[4], kb[4], vt0[4], vt1[4];
#pragma unroll
    for (int h = 0; h < 4; ++h) {
      qf[h] = ld2(qa + 2 * h * PS + 128);
      ka[h] = asb(ld2(qa + (9 + 2 * h) * PS));
      kb[h] = asb(ld2(k1 + (9 + 2 * h) * PS + 256));
      SB();
    }
#pragma unroll
    for (int h = 0; h < 4; ++h) {
      vt0[h] = cat8(trread((h < 2 ? ve0 : vo0) + (18 + 2 * h) * PS), trread((h < 2 ? ve1 : vo1) + (18 + 2 * h) * PS));
      vt1[h] = cat8(trread(vk0 + (20 + 2 * h) * PS + (h < 2 ? 0 : HS)), trread(vk1 + (20 + 2 * h) * PS + (h < 2 ? 0 : HS)));
    }
    SB();
    f4 s0[4], s1[4], s[4];
    float mx[4];
#pragma unroll
    for (int h = 0; h < 4; ++h) {
      bf8 qq = asb(qf[h] & qm[h]);
      s0[h] = mfma(ka[h], qq, b0);
      s1[h] = mfma(kb[h], qq, b1);
      SB();
    }
#pragma unroll
    for (int h = 0; h < 4; ++h) {
#pragma unroll
      for (int dd = 0; dd < 4; ++dd) s[h][dd] = fmax2(s0[h][dd], s1[h][dd]);
      mx[h] = fmax2(fmax2(fmax2(s[h][0], s[h][1]), s[h][2]), s[h][3]);
      SB();
    }
    {
      auto a = __builtin_amdgcn_permlane16_swap(asu(mx[0]), asu(mx[1]), false, false);
      auto b = __builtin_amdgcn_permlane16_swap(asu(mx[2]), asu(mx[3]), false, false);
      auto c = __builtin_amdgcn_permlane32_swap(asu(fmax2(asf(a[0]), asf(a[1]))), asu(fmax2(asf(b[0]), asf(b[1]))), false, false);
      unsigned m = asu(-cs * fmax2(asf(c[0]), asf(c[1])));
      auto e = __builtin_amdgcn_permlane32_swap(m, m, false, false);
      unsigned e0 = e[0], e1 = e[1];
      auto f0 = __builtin_amdgcn_permlane16_swap(e0, e0, false, false);
      auto f1 = __builtin_amdgcn_permlane16_swap(e1, e1, false, false);
      mx[0] = asf(f0[0]); mx[1] = asf(f0[1]); mx[2] = asf(f1[0]); mx[3] = asf(f1[1]);
    }
    SB();
    asm volatile("s_waitcnt vmcnt(0)" : "+v"(wr[0]), "+v"(wr[1]), "+v"(wr[2]), "+v"(wr[3]), "+v"(wr[4]), "+v"(wr[5]),
                 "+v"(wr[6]), "+v"(wr[7]), "+v"(wr[8]), "+v"(wr[9]), "+v"(wr[10]), "+v"(wr[11]), "+v"(wr[12]),
                 "+v"(wr[13]), "+v"(wr[14]), "+v"(wr[15]), "+v"(wr[16]), "+v"(wr[17]), "+v"(wr[18]), "+v"(wr[19]),
                 "+v"(wr[20]) :: "memory");
    if ((threadIdx.x & 255) == 192) post_flag(ws, (w < 4 ? F_A : F_L) + rt * 8 + hq, tag);
    f4 sum[4], o0[4], o1[4];
#pragma unroll
    for (int h = 0; h < 4; ++h) {
      float p[4];
#pragma unroll
      for (int dd = 0; dd < 4; ++dd) p[dd] = __builtin_amdgcn_exp2f(__builtin_fmaf(s[h][dd], cs, mx[h]));
      unsigned p01 = pk(p[0], p[1]), p23 = pk(p[2], p[3]);
      bf8 pf = asb(u4{p01 & ~m01, p23 & ~m23, p01 & m01, p23 & m23});
      sum[h] = mfma(asb(ones), pf, z);
      o0[h] = mfma(vt0[h], pf, z);
      o1[h] = mfma(vt1[h], pf, z);
      SB();
    }
    u2 gr[4];
    u4 left;
#pragma unroll
    for (int h = 0; h < 4; ++h) {
      const float inv = __builtin_amdgcn_rcpf(sum[h][0]);
      const unsigned a0 = pk(o0[h][0] * inv, o0[h][1] * inv), a1 = pk(o0[h][2] * inv, o0[h][3] * inv);
      const unsigned e0 = pk(o1[h][0] * inv, o1[h][1] * inv);
      if (h & 1) {
        const unsigned e1 = pk(o1[h][2] * inv, o1[h][3] * inv);
        left[h] = a1;
        gr[h] = u2{g0 ? e0 : a0, g0 ? e1 : a1};
        st16<SC1>(u4{gr[h - 1][0], gr[h - 1][1], gr[h][0], gr[h][1]}, ro, h == 1 ? os0 : os1);
        SB();
      } else {
        left[h] = e0;
        gr[h] = u2{a0, a1};
      }
    }
    st16<SC1>(left, ro, os2);
    const unsigned cs0 = chk(u4{gr[0][0], gr[0][1], gr[1][0], gr[1][1]}, chk(u4{gr[2][0], gr[2][1], gr[3][0], gr[3][1]}, 0));
    __builtin_amdgcn_raw_buffer_store_b32(chk(left, rowsum(cs0) + (unsigned)tag), ro, oh, 0, SC1);
  }
  STAMP(3);
  STAMPL(4);
  if (w >= 4) {
    __builtin_amdgcn_s_setprio(0);
    u64 fv = 0;
    GAS u64* fp = (GAS u64*)(ws + WS_FLAG + pc.fl * 128);
#pragma unroll
    for (int i = 0; i < 21; ++i) {
      if (i < 20 || w == 4) *(LAS u4*)(sm + L_WO + (4 * i + w - 4) * 1024 + lane * 16) = wr[i];
      if (i == 10 && w == 5) asm volatile("global_load_dwordx2 %0, %1, off sc1" : "+v"(fv) : "v"(fp) : "memory");
    }
    arrive(L_CNT + 20);
    if (w == 5) {
      asm volatile("s_waitcnt vmcnt(0)" : "+v"(fv) :: "memory");
      if (!__all(fv == tag)) wait_flags(ws, pc.fl, tag);
      arrive(L_CNT + 28);
    }
    STAMPL(30);
  }
}

DEV void role_b(lptr sm, char* ws, const u16* x, const u16* wqkv, const u16* wo, const u16* q, const u16* hk, const u16* dv, int bid, int w, int lane, unsigned fw, unsigned fx) {
  const int tid = threadIdx.x, xcd = bid & 7, jb = (bid >> 3) - 16, me = xcd * 16 + jb;
  const int G = lane >> 4, i16 = lane & 15, w4 = w & 3;
  rsrc_t ru = mkrs(ws + WS_U, NB_ACT + NB_ACT / 4);
  const int xc = (xcd + XSH) & 7, cm = xc * 16 + jb;
  const int c5 = 512 + xcd * 8 + (jb & 7);
  const unsigned tb = L_WO + w * 9216;
  const u4 ny = {NOTYET, NOTYET, NOTYET, NOTYET};
  rsrc_t ry = mkrs(ws + WS_Y, NB_ACT);
  u64 tag;
  if (w < 4 || (w == 4 && jb < 8)) {
    const int grp = w < 4 ? xcd : 8, un = w < 4 ? jb * 4 + w : xcd * 8 + jb;
    const int q4 = i16 >> 2, p4 = lane & 3;
    const int f0 = (q4 >> 1) | (p4 << 1), f1 = (q4 >> 1) | (((p4 + 1) & 3) << 1);
    const unsigned a0 = tb + (8 * p4 + q4) * 128 + (((G >> 1) ^ f0) << 4) + (G & 1) * 8;
    const unsigned a1 = tb + (8 * p4 + q4) * 128 + (((G >> 1) ^ f1) << 4) + (G & 1) * 8;
    lptr wp = sm + L_OT + w * 512 + (G * 4 + (i16 & 3)) * 8;
    unsigned off = ((grp * 16 + G) * 256 + 4 * un) * 64 + i16 * 16;
    u2 K[3], V[2], wk;
    auto rd = [&](int s) {
      lptr b0 = (lptr)(size_t)(a0 ^ (s << 5)), b1 = (lptr)(size_t)(a1 ^ (s << 5));
      wk = *(LAS u2*)(wp + s * 128);
      K[0] = __builtin_bit_cast(u2, trread(b0 + 512));
      K[1] = __builtin_bit_cast(u2, trread(b1 + 1024));
      K[2] = __builtin_bit_cast(u2, trread(b1 + 1536));
      V[0] = __builtin_bit_cast(u2, trread(b0 + 5120));
      V[1] = __builtin_bit_cast(u2, trread(b0 + 5120 + 512));
    };
    if (w < 4) h_dma(hk, w, cm * 4 + w, lane);
    tag = lds_tag(sm);
    const unsigned key = (unsigned)tag;
    if (w < 4) WAITV(4); else WAITV(0);
    if (w < 4) arrive(L_CNT + 44);
    STAMP(1);
    rd(0);
#pragma unroll 1
    for (int s = 0; s < 4; ++s) {
      const unsigned wA = alignb(wk[1], wk[1]), wB = alignb(wk[0], wk[0]);
      const unsigned kk[6] = {K[0][0], K[0][1], K[1][0], K[1][1], K[2][0], K[2][1]};
      const unsigned vv[4] = {V[0][0], V[0][1], V[1][0], V[1][1]};
      rd((s + 1) & 3);
      unsigned sh[5];
      float ev[4], od[4], ve[4], vo[4];
#pragma unroll
      for (int d = 0; d < 5; ++d) sh[d] = alignb(kk[d + 1], kk[d]);
      SB();
#pragma unroll
      for (int d = 0; d < 4; ++d) { ev[d] = dot2z(sh[d], wA); od[d] = dot2z(kk[d + 1], wA); }
      SB();
#pragma unroll
      for (int d = 0; d < 4; ++d) { ev[d] = dot2a(sh[d + 1], wB, ev[d]); od[d] = dot2a(kk[d + 2], wB, od[d]); }
      SB();
#pragma unroll
      for (int d = 0; d < 4; ++d) { ve[d] = lo16(vv[d]); vo[d] = hi16(vv[d]); pin(ve[d]); pin(vo[d]); }
      SB();
      u4 o;
#pragma unroll
      for (int d = 0; d < 4; ++d) o[3 - d] = pk(od[d] * vo[d], ev[d] * ve[d]);
      st16<SC1>(o, ru, off);
      __builtin_amdgcn_raw_buffer_store_b32(chk(o, key), ru, NB_ACT + (off >> 2), 0, SC1);
      off += 65536;
      SB();
    }
    STAMP(2);
    if (w < 4) arrive(L_CNT + 40);
  } else if (w == 7) {
    const u4 rx = mkdesc(x, NB_ACT), rw = mkdesc(wqkv, NB_W);
    __builtin_amdgcn_s_setprio(3);
    STAMPW(22, 7);
    if (PDELAY) __builtin_amdgcn_s_sleep(PDELAY);
#pragma unroll 1
    for (int st = PFS0; st < 9; ++st) {
      if (st == PF1) reach(L_CNT + 44, 4);
      if (st == PFA) reach(L_CNT + 40, 4);
      if (st == PFB) reach(L_CNT + 4, 1);
      touch(rw, fw, st * 64 * 3456);
      touch(rx, fx, st * 128);
    }
    STAMPW(23, 7);
    reach(L_CNT + 4, 1);
    const u4 rq = mkdesc(q, NB_ACT);
    touch(rq, (jb * 128 + lane) * 1152 + ((xcd + YSH) & 7) * 128, 0);
    touch(rq, (jb * 128 + lane) * 1152 + ((xcd + YSH) & 7) * 128, 64 * 1152);
    if (jb >= 8) {
      const int un = bid - 192;
      touch(rq, lane < 32 ? ((un >> 2) * 128 + 64 * (un & 1) + 8 * ((un >> 1) & 1) + (lane >> 3) * 16 + (lane & 7)) * 1152 + 1024 : OOB, 0);
    }
    const unsigned y4 = (xc * 32 + (lane >> 5)) * 8192 + jb * 512 + (lane & 31) * 16;
#pragma unroll 1
    for (int i = 0; i < 16; i += 2) { st16<SC1>(ny, ry, y4 + i * 16384); st16<SC1>(ny, ry, y4 + i * 16384 + 16384); }
    if (jb >= 8) {
#pragma unroll
      for (int i = 0; i < 4; ++i) st16<SC1>(ny, ry, (256 + i * 8 + (lane >> 3)) * 8192 + (c5 & 63) * 128 + (lane & 7) * 16);
    }
    tag = lds_tag(sm);
    WAITV(0);
    arrive(L_CNT + 16);
    if (lane == 0) post_flag(ws, F_Y + cm, tag);
    STAMPW(25, 7);
    __builtin_amdgcn_s_sleep(12);
    if (jb >= 8) __builtin_amdgcn_s_sleep(16);
    wait_flags(ws, F_Y + (jb < 8 ? xcd * 16 + i16 : (lane >> 3) * 16 + 8 + (lane & 7)), tag);
    arrive(L_CNT + 24);
  } else if (w == 6) {
    if (jb >= 8) {
      reach(L_CNT + 40, 4);
      h_dma(hk, 4, c5, lane);
      WAITV(0);
      h_shift(sm + 4 * CH_SZ, lane);
      arrive(L_CNT + 32);
    }
  } else {
    const int slot = w == 5 ? jb : 2 + jb;
    if (w == 5) {
      tag = get_tag(ws);
      if (lane == 0) *(LAS u64*)(sm + L_TAG) = tag;
      arrive(L_CNT);
      if (lane == 0) post_flag(ws, F_P + me, tag);
    } else {
      tag = lds_tag(sm);
    }
    if (w == 5 || jb >= 14) {
      const u4 rwo = mkdesc(wo, NB_WO);
      const int e4 = (lane * 57) >> 9;
      const unsigned vb = (e4 >> 1) * 1152 + xcd * 144 + (lane - e4 * 9) * 16, vt = (e4 & 1) * 1152;
      int ks = slot + ROTK * xcd;
      if (ks >= 18) ks -= 18;
#pragma unroll 1
      for (int i = 0; i < 8; ++i) {
        const int ci = 4 * ks + (i >> 1), h = (ci * 57) >> 9, c = ci - 9 * h;
        const int row = h * 72 + (c == 8 ? 16 : 36 * (c >> 2) + 4 * (c & 3)) + 2 * (i & 1);
        dma16m(rwo, tb + i * 576, vb + __umul24(vt, c == 8 ? 36 : (c & 3) ? 16 : 32), row * 1152, (1ull << 36) - 1);
      }
      WAITV(0);
      rsrc_t rpw = mkrs(ws + WS_WO, NB_WO);
      lptr p = (lptr)(size_t)tb + (G * 8 + ((i16 >> 2) << 1)) * 144 + (lane & 3) * 8;
      const unsigned po = (xcd * 18 + slot) * 4608;
      u4 f[5];
#pragma unroll
      for (int n = 0; n < 5; ++n) f[n] = __builtin_bit_cast(u4, cat8(trread(p + n * 32), trread(p + 144 + n * 32)));
#pragma unroll
      for (int n = 0; n < 4; ++n) st16<SC1>(f[n], rpw, po + n * 1024 + lane * 16);
      st16<SC1>(f[4], rpw, i16 < 8 ? po + 4096 + (G * 8 + i16) * 16 : OOB);
      WAITV(0);
      if (lane == 0) post_flag(ws, F_W + xcd * 32 + slot, tag);
      STAMPW(21, 5);
    }
  }
  const int r = lane & 15, g = lane >> 4;
  if (w >= 4) {
    if (w < 6) {
      const int ct = (bid >> 3) & 7;
      __builtin_amdgcn_s_setprio(2);
      if (w == 5) {
        reach(L_CNT + 4, 1);
        wait_flags(ws, F_W + ct * 32 + (lane < 18 ? lane : 0), tag);
      } else if (jb >= 8) {
        reach(L_CNT + 4, 1);
        u4 u5[4];
#pragma unroll 1
        for (int spin = 0; spin < SPIN_MAX; ++spin) {
          unsigned hx[4], bad = 0;
#pragma unroll
          for (int i = 0; i < 4; ++i) {
            u5[i] = ld16<SC1>(ru, lane * 64 + (c5 & 3) * 16, (c5 >> 2) * 16384 + i * 4096);
            hx[i] = __builtin_amdgcn_raw_buffer_load_b32(ru, lane * 16 + (c5 & 3) * 4, NB_ACT + (c5 >> 2) * 4096 + i * 1024, SC1);
          }
#pragma unroll
          for (int i = 0; i < 4; ++i) bad |= chk(u5[i], (unsigned)tag) ^ hx[i];
          if (__all(bad == 0)) break;
          asm volatile("s_sleep 4" ::: "memory");
        }
#pragma unroll
        for (int i = 0; i < 4; ++i) *(LAS u4*)(sm + U_OFF + 480 + 4 * CH_SZ + lane * 16 + i * 1024) = u5[i];
      }
      if (w == 4) arrive(L_CNT + 32);
      arrive(L_CNT + 44);
      reach(L_CNT + 44, 6);
      reach(L_CNT + 8, 4);
      STAMPL(16);
      const u4 rp = mkdesc(ws + WS_WO + ct * 82944, 82944);
#pragma unroll 1
      for (int i = (w & 1) * 1024; i < 81 * 1024; i += 2048) dma16c(rp, L_WO + i, lane * 16, i);
      WAITV(0);
      STAMPL(17);
    }
    arrive(L_CNT + 20);
    __builtin_amdgcn_s_setprio(0);
    return;
  }
  typedef const __attribute__((address_space(4))) unsigned* cptr;
  const unsigned dv0 = ((cptr)dv)[cm * 2 + (w >> 1)], dv1 = ((cptr)dv)[c5 >> 1];
  WAITV(0);
  __builtin_amdgcn_s_sleep(UDELAY);
  STAMP(3);
  {
    u4 uv[4];
#pragma unroll 1
    for (int spin = 0; spin < SPIN_MAX; ++spin) {
      unsigned hx[4], bad = 0;
#pragma unroll
      for (int i = 0; i < 4; ++i) {
        uv[i] = ld16<SC1>(ru, tid * 16, cm * 16384 + i * 4096);
        hx[i] = __builtin_amdgcn_raw_buffer_load_b32(ru, tid * 4, NB_ACT + cm * 4096 + i * 1024, SC1);
      }
      if (spin == 0) h_shift(sm + w * CH_SZ, lane);
#pragma unroll
      for (int i = 0; i < 4; ++i) bad |= chk(uv[i], (unsigned)tag) ^ hx[i];
      if (__all(bad == 0)) break;
      STAMP(20);
      asm volatile("s_sleep 1" ::: "memory");
    }
    STAMP(4);
    if (w == 3) arrive(L_CNT + 4);
#pragma unroll
    for (int i = 0; i < 4; ++i) *(LAS u4*)(sm + U_OFF + 480 + (tid & 3) * CH_SZ + (tid >> 2) * 16 + i * 1024) = uv[i];
  }
  arrive(L_CNT + 8);
  const int xd = 15 - r - 8 * g;
#pragma unroll 1
  for (int rnd = 0; rnd < (jb >= 8 ? 2 : 1); ++rnd) {
    const int dd0 = rnd ? 2 * w : 0, ch = rnd ? c5 : cm * 4 + w;
    const unsigned dval = dd0 == 0 ? ((rnd ? dv1 : dv0) >> ((ch & 1) * 16)) & 0xffffu : 0;
    lptr cb = sm + (rnd ? 4 : w) * CH_SZ;
    lptr aa[4];
#pragma unroll
    for (int i = 0; i < 4; ++i) { aa[i] = cb + (r & 1) * H_CP + 4 + 4 * (4 * g + (r >> 1)) + dd0 * 64 + i * 1024; pin(aa[i]); }
    lptr ba = cb + U_OFF + 16 * (31 + 2 * r - g) - 128 - dd0 * 64;
    pin(ba);
    u4 ad;
#pragma unroll
    for (int d = 0; d < 4; ++d) ad[d] = (xd >> 1) == d ? dval << ((xd & 1) * 16) : 0u;
    pin(ad);
    reach(rnd ? L_CNT + 32 : L_CNT + 8, rnd ? 2 : 4);
    STAMP(rnd ? 19 : 5);
    f4 acc[8];
    bf8 A[2][8], B[2][8];
    auto rd = [&](int dd, bf8 (&a)[8], bf8 (&b)[8]) {
#pragma unroll
      for (int i = 0; i < 8; ++i) {
        const LAS unsigned* p = (const LAS unsigned*)(aa[i >> 1] + dd * 64 + (i & 1) * 512);
        a[i] = asb(u4{p[0], p[1], p[2], p[3]});
        b[i] = *(const LAS bf8*)(ba + 128 - dd * 64 + i * 512);
      }
    };
    auto mm = [&](bf8 (&a)[8], bf8 (&b)[8]) {
#pragma unroll
      for (int i = 0; i < 8; ++i)
#pragma unroll
        for (int jj = 0; jj + i < 8; ++jj) acc[i + jj] = mfma(a[i], b[jj], acc[i + jj]);
#pragma unroll
      for (int i = 0; i < 24; ++i) { SGB(0x8, 1); SGB(0x100, 1); }
      SGB(0x8, 12);
      SB();
    };
    rd(0, A[0], B[0]);
#pragma unroll
    for (int n = 0; n < 8; ++n) acc[n] = mfma(asb(ad), B[0][n], f4{0.f, 0.f, 0.f, 0.f});
    SB();
#pragma unroll 1
    for (int it = rnd * 3; it < 4; ++it) {
      rd(1, A[1], B[1]);
      mm(A[0], B[0]);
      rd(2, A[0], B[0]);
      mm(A[1], B[1]);
#pragma unroll
      for (int i = 0; i < 4; ++i) aa[i] += 128;
      ba -= 128;
    }
    STAMP(6 + rnd);
    const unsigned yo = (((ch >> 6) * 16 + 2 * (g & 1) + (r >> 3)) * 2 + (g >> 1)) * 8192 + (ch & 63) * 128 + (r & 7) * 16;
    auto put = [&](f4 x, f4 y, int a) {
      auto s0 = __builtin_amdgcn_permlane16_swap(pk(x[0], x[1]), pk(y[0], y[1]), false, false);
      auto s1 = __builtin_amdgcn_permlane16_swap(pk(x[2], x[3]), pk(y[2], y[3]), false, false);
      st16<SC1>(u4{s0[0], s1[0], s0[1], s1[1]}, ry, yo + a * 65536);
    };
    if (rnd == 0) {
      reach(L_CNT + 16, 1);
#pragma unroll
      for (int a = 0; a < 4; ++a) put(acc[2 * a], acc[2 * a + 1], a);
    } else {
      LAS f4* mine = (LAS f4*)(sm + w * CH_SZ + lane * 16);
#pragma unroll
      for (int n = 0; n < 8; ++n) mine[n * 64] = acc[n];
      arrive(L_CNT + 16);
      reach(L_CNT + 16, 5);
      constexpr int CS = CH_SZ / 16;
      LAS f4* p = (LAS f4*)(sm + lane * 16) + w * 128;
      put((p[0] + p[CS]) + (p[2 * CS] + p[3 * CS]), (p[64] + p[CS + 64]) + (p[2 * CS + 64] + p[3 * CS + 64]), w);
    }
  }
  STAMP(8);
}

DEV void finish(lptr sm, lptr yt, char* ws, const u16* q, u16* out, int grp, int idx, int w4, int lane) {
  rsrc_t ry = mkrs(ws + WS_Y, NB_ACT), rq = mkrs(q, NB_ACT), rout = mkrs(out, 2 * NB_ACT);
  const int tile = idx + 8 * (w4 >> 1), orow = tile * 128 + 8 * (w4 & 1) + (lane >> 3);
  const unsigned qo = orow * 1152 + grp * 128 + (lane & 7) * 16, yo = ((grp * 16 + tile) * 2 + (w4 & 1)) * 8192 + lane * 16;
  u4 qv[8], yv[8];
#pragma unroll
  for (int i = 0; i < 8; ++i) qv[i] = ld16<0>(rq, qo, i * 16 * 1152);
  reach(L_CNT + 24, 1);
  f2 qf[8][4], yy[4][4];
#pragma unroll
  for (int i = 0; i < 16; ++i) { yy[i >> 2][i & 3] = f2{0.f, 0.f}; pin(yy[i >> 2][i & 3]); }
  asm volatile("s_waitcnt vmcnt(0)" : "+v"(qv[0]), "+v"(qv[1]), "+v"(qv[2]), "+v"(qv[3]), "+v"(qv[4]), "+v"(qv[5]),
               "+v"(qv[6]), "+v"(qv[7]) :: "memory");
  STAMP(26);
  __builtin_amdgcn_s_sleep(YDELAY);
#pragma unroll 1
  for (int spin = 0; spin < SPIN_MAX; ++spin) {
    unsigned mx = 0;
#pragma unroll
    for (int i = 0; i < 8; ++i) yv[i] = ld16<SC1>(ry, yo, i * 1024);
    SB();
#pragma unroll
    for (int i = 0; i < 32; ++i) {
      qf[i >> 2][i & 3] = f2{lo16(qv[i >> 2][i & 3]), hi16(qv[i >> 2][i & 3])};
      pin(qf[i >> 2][i & 3]);
    }
#pragma unroll
    for (int i = 0; i < 8; ++i) mx = max3u(max3u(mx, yv[i][0], yv[i][1]), yv[i][2], yv[i][3]);
    if (__all(mx != NOTYET)) break;
    STAMP(27 + (spin < 3 ? spin : 3));
    asm volatile("s_sleep 2" ::: "memory");
  }
  STAMP(12);
  unsigned oo = orow * 2304 + 1152 + grp * 128 + (lane & 7) * 16;
#pragma unroll
  for (int i = 0; i < 8; ++i) *(LAS u4*)(yt + lane * 16 + i * 1040) = yv[i];
  const unsigned p = (unsigned)(size_t)yt + (lane & 7) * 1040 + (lane >> 3) * 2;
#pragma unroll
  for (int t = 0; t < 2; ++t) {
#pragma unroll
    for (int i = 0; i < 16; ++i) {
      float a = yy[i >> 2][i & 3][0], b = yy[i >> 2][i & 3][1];
      asm volatile("ds_read_u16_d16_hi %0, %2 offset:%3\n\tds_read_u16_d16_hi %1, %2 offset:%4"
                   : "+v"(a), "+v"(b) : "v"(p), "i"(t * 64 + (i >> 2) * 16 + (i & 3) * 256), "i"(t * 64 + (i >> 2) * 16 + (i & 3) * 256 + 128)
                   : "memory");
      yy[i >> 2][i & 3] = f2{a, b};
    }
#pragma unroll
    for (int i = 0; i < 4; ++i) {
      if (i == 0) asm volatile("s_waitcnt lgkmcnt(14)" : "+v"(yy[0][0]), "+v"(yy[0][1]), "+v"(yy[0][2]), "+v"(yy[0][3]), "+v"(yy[1][0]), "+v"(yy[1][1]), "+v"(yy[1][2]), "+v"(yy[1][3]));
      if (i == 2) asm volatile("s_waitcnt lgkmcnt(0)" : "+v"(yy[2][0]), "+v"(yy[2][1]), "+v"(yy[2][2]), "+v"(yy[2][3]), "+v"(yy[3][0]), "+v"(yy[3][1]), "+v"(yy[3][2]), "+v"(yy[3][3]));
      u4 o;
#pragma unroll
      for (int d = 0; d < 4; ++d) { const f2 m = yy[i][d] * qf[t * 4 + i][d]; o[d] = pk(m[0], m[1]); }
      st16<OA1>(o, rout, oo + (t * 64 + i * 16) * 2304);
      SB();
    }
  }
}

DEV void finish8(lptr yt, char* ws, const u16* q, u16* out, int un, int w, int lane) {
  rsrc_t ry = mkrs(ws + WS_Y, NB_ACT), rq = mkrs(q, NB_ACT), rout = mkrs(out, 2 * NB_ACT);
  const int orow = (un >> 2) * 128 + 64 * (un & 1) + 8 * ((un >> 1) & 1) + 16 * w + (lane >> 3);
  u4 qv = ld16<0>(rq, orow * 1152 + 1024 + (lane & 7) * 16, 0), yv;
  reach(L_CNT + 24, 1);
  asm volatile("s_waitcnt vmcnt(0)" : "+v"(qv) :: "memory");
#pragma unroll 1
  for (int spin = 0; spin < SPIN_MAX; ++spin) {
    yv = ld16<SC1>(ry, lane * 128 + ((un & 1) * 4 + w) * 16, (256 + (un >> 1)) * 8192);
    if (__all(max4(yv, 0) != NOTYET)) break;
    asm volatile("s_sleep 1" ::: "memory");
  }
  STAMP(12);
  *(LAS u4*)(yt + (lane >> 3) * 144 + (lane & 7) * 16) = yv;
  WAITL0();
  const LAS u16* p = (const LAS u16*)(yt + (lane & 7) * 144 + (lane >> 3) * 2);
  u4 o;
#pragma unroll
  for (int d = 0; d < 4; ++d) o[d] = pk(lo16(p[d * 16]) * lo16(qv[d]), lo16(p[d * 16 + 8]) * hi16(qv[d]));
  st16<OA1>(o, rout, orow * 2304 + 1152 + 1024 + (lane & 7) * 16);
}

DEV void out_proj(lptr sm, char* ws, u16* out, int bid, int lw, int lane, const Proj& pc) {
  lptr wa[2] = {(lptr)(size_t)pc.wa, (lptr)(size_t)(pc.wa + 9 * 4608)};
  lptr wb[2] = {(lptr)(size_t)pc.wb, (lptr)(size_t)(pc.wb + 9 * 4608)};
  pin(wa[1]); pin(wb[1]);
  rsrc_t ro = mkrs(ws + WS_O, NB_ACT + 65536);
  const unsigned ob = pc.ob, oo[3] = {pc.oo0, pc.oo1, pc.oo2};
  lptr ot = (lptr)(size_t)pc.ot, ow = (lptr)(size_t)pc.ow;
  reach(L_CNT + 20, 4);
  f4 acc[5];
  const u64 tag = *(LAS u64*)(sm + L_TAG);
#pragma unroll 1
  for (int att = 0; att < 4096; ++att) {
    constexpr int PRE = 7, AHEAD = 9;
    bf8 wf[PRE][5];
    auto rdw = [&](int i) {
#pragma unroll
      for (int n = 0; n < 5; ++n)
        wf[i % PRE][n] = *(const LAS bf8*)((n < 4 ? wa[i / 9] + n * 1024 : wb[i / 9]) + (i % 9) * 4608);
    };
#pragma unroll
    for (int i = 0; i < PRE - 1; ++i) rdw(i);
    SB();
    if (lw == 0 && att == 0 && bid >= 128) {
      wait_flags(ws, pc.fl, tag);
      arrive(L_CNT + 28);
    }
    reach(L_CNT + 28, 1);
    STAMPL(att ? 31 : 9);
    u4 of[18];
    auto ldo = [&](int i) { of[i] = ld16<SC1>(ro, ob, i * 4096); };
#pragma unroll
    for (int i = 0; i < AHEAD; ++i) { ldo(i); SB(); }
#pragma unroll
    for (int n = 0; n < 5; ++n) acc[n] = f4{0.f, 0.f, 0.f, 0.f};
    unsigned sum = 2 * (unsigned)tag, h0, h1;
#pragma unroll
    for (int i = 0; i < 18; ++i) {
      if (i + PRE - 1 < 18) rdw(i + PRE - 1);
#pragma unroll
      for (int n = 0; n < 5; ++n) acc[n] = mfma(wf[i % PRE][n], asb(of[i]), acc[n]);
      sum = chk(of[i], sum);
      if (i + AHEAD < 18) ldo(i + AHEAD);
      if (i == 9) h0 = __builtin_amdgcn_raw_buffer_load_b32(ro, pc.oh, 0, SC1);
      if (i == 10) h1 = __builtin_amdgcn_raw_buffer_load_b32(ro, pc.oh + 256, 0, SC1);
      SGB(0x8, 1); SGB(0x20, 1); SGB(0x100, 1);
#pragma unroll
      for (int n = 1; n < 5; ++n) { SGB(0x8, 1); SGB(0x100, 1); SGB(0x2, 1); }
      SB();
    }
    if (__all(rowsum(sum - h0 - h1) == 0)) break;
    asm volatile("" ::: "memory");
  }
  STAMPL(10);
#pragma unroll
  for (int n = 0; n < 5; ++n)
    if (n < 4 || lane < 32) *(LAS u2*)(ow + n * 32) = u2{pk(acc[n][0], acc[n][1]), pk(acc[n][2], acc[n][3])};
  WAITL0();
  rsrc_t rout = mkrs(out, 2 * NB_ACT);
#pragma unroll
  for (int i = 0; i < 3; ++i) st16<OA2>(*(const LAS u4*)(ot + i * 1024), rout, oo[i]);
  STAMPL(15);
}

__global__ __launch_bounds__(512) __attribute__((amdgpu_waves_per_eu(2, 2))) void fused(
    const u16*, const u16*, const u16*, const u16*, const u16*, const u16*, const u16*, const u16*, const u16*, u16*,
    char*) {
  typedef u64 u64x8 __attribute__((ext_vector_type(8)));
  typedef u64 u64x2 __attribute__((ext_vector_type(2)));
  u64x8 ka;
  u64x2 kb;
  u64 kc;
  asm volatile("s_load_dwordx16 %0, %3, 0x0\n\ts_load_dwordx4 %1, %3, 0x40\n\ts_load_dwordx2 %2, %3, 0x50"
               : "=&s"(ka), "=&s"(kb), "=&s"(kc) : "s"(__builtin_amdgcn_kernarg_segment_ptr()));
  extern __shared__ __attribute__((aligned(16))) char smem[];
  lptr sm = (lptr)smem;
  const int tid = threadIdx.x, lane = tid & 63, w = __builtin_amdgcn_readfirstlane(tid >> 6);
  const int bid = blockIdx.x;
  if (bid < 128 && (bid >> 3) + 16 * tid < CODE_LINES) {
    u64 pc;
    asm volatile("s_getpc_b64 %0" : "=s"(pc));
    asm volatile("s_mov_b32 m0, %0\n\ts_nop 0\n\tglobal_load_lds_dword %1, %2"
                 :: "s"(L_PF), "v"(((bid >> 3) + 16 * tid) * 128), "s"(pc & ~127ull) : "memory");
  }
  const int pgrp = w < 4 ? bid & 7 : 8, pun = w < 4 ? ((bid >> 3) - 16) * 4 + w : bid - 128 - ((bid >> 3) - 16) * 7 + (bid & 7) * 7;
  unsigned o0 = (pun * 32 + (lane >> 3)) * 1152 + pgrp * 128 + (((lane & 7) ^ ((lane >> 4) & 1)) << 4);
  const int t27 = lane >= 27, cc = lane - 27 * t27, part = (cc >= 9) + (cc >= 18);
  unsigned vw = t27 * (4 * 3456) + part * (1152 - 144) + cc * 16 + ((bid & 1) * 4 + ((bid >> 3) & 3)) * 144;
  if (w == 7 && bid >= 128) {
    const int jb = (bid >> 3) - 16, kr = (lane * 4370) >> 16, c15 = lane - kr * 15, p5 = (c15 * 52) >> 8;
    vw = lane < 60 ? (jb * 4 + kr) * 3456 + p5 * (1152 - 640) + ((bid & 1) * 4 + c15) * 128 : OOB;
    o0 = lane < 33 ? (((bid & 7) >> 1) * 512 - 16 + jb * 33 + lane) * 1152 : OOB;
  }
  pin(o0); pin(vw);
  if (bid >= 128 && tid < 12) *(LAS unsigned*)(sm + L_CNT + tid * 4) = 0;
  asm volatile("s_waitcnt lgkmcnt(0)" : "+s"(ka), "+s"(kb), "+s"(kc) :: "memory");
  const u16 *x = (const u16*)ka[0], *wqkv = (const u16*)ka[1], *wo = (const u16*)ka[2], *q = (const u16*)ka[3];
  const u16 *hk = (const u16*)ka[7], *dv = (const u16*)kb[0];
  u16* out = (u16*)kb[1];
  char* ws = (char*)kc;
  if (__builtin_expect(bid >= 128, 1)) {
    if (__builtin_expect(w < 4 || (w == 4 && bid < 192), 1)) {
      const u4 rk = mkdesc((const void*)ka[4], NB_ACT), rv = mkdesc((const void*)ka[5], NB_ACT);
      const unsigned tb = L_WO + w * 9216;
      dma16(rk, tb, o0 - 8 * 1152, 0);
#pragma unroll
      for (int i = 0; i < 4; ++i) {
        dma16(rk, tb + 1024 + i * 1024, o0 ^ (((i + 1) & 3) << 5), i * 8 * 1152);
        dma16(rv, tb + 5120 + i * 1024, o0 ^ (i << 5), i * 8 * 1152);
        if (i == 0) dma16m(mkdesc((const void*)ka[6], 4608), L_OT + w * 512, lane * 16, pgrp * 512, 0xffffffffull);
      }
    }
    if (w == 7) {
#pragma unroll
      for (int st = PFE; st < PFS0; ++st) {
        touch(mkdesc(wqkv, NB_W), vw, st * 64 * 3456);
        touch(mkdesc(x, NB_ACT), o0, st * 128);
      }
    }
    __builtin_amdgcn_s_barrier();
    asm volatile("" ::: "memory");
  }
  Proj pc;
  if (__builtin_expect(bid < 128, 1)) role_a(sm, ws, x, wqkv, bid, w, lane, pc, vw);
  else {
    role_b(sm, ws, x, wqkv, wo, q, hk, dv, bid, w, lane, vw, o0);
    if (w >= 4) proj_consts(pc, bid, w & 3, lane);
    else asm volatile("" : "=v"(pc.wa), "=v"(pc.wb), "=v"(pc.ob), "=v"(pc.oh), "=v"(pc.oo0), "=v"(pc.oo1), "=v"(pc.oo2),
                      "=v"(pc.ot), "=v"(pc.ow), "=v"(pc.fl));
  }
  if (w >= 4) {
    out_proj(sm, ws, out, bid, w & 3, lane, pc);
  } else if (bid >= 128) {
    if (bid >= 192) finish8(sm + w * CH_SZ + U_OFF, ws, q, out, bid - 192, w, lane);
    else finish(sm, sm + w * CH_SZ, ws, q, out, bid & 7, (bid >> 3) - 16, w, lane);
    STAMP(13);
  } else {
    if (bid == 64 && w == 1) {
      const u64 tag = *(LAS u64*)(sm + L_TAG);
      GAS u64* p = (GAS u64*)(ws + WS_FLAG + lane * 128);
#pragma unroll 1
      for (int spin = 0; spin < SPIN_MAX; ++spin) {
        const u64 a = __hip_atomic_load(p, RLX_AGENT), b = __hip_atomic_load(p + 1024, RLX_AGENT);
        const u64 c = __hip_atomic_load(p + 2048, RLX_AGENT), d = __hip_atomic_load(p + 3072, RLX_AGENT);
        if (__all(a == tag && b == tag && c == tag && d == tag)) break;
        __builtin_amdgcn_s_sleep(PSLEEP);
      }
      if (lane == 0) __hip_atomic_store((GAS u64*)ws, tag, RLX_AGENT);
    }
  }
}

extern "C" void kernel_launch(void* const* d_in, const int* in_sizes, int n_in, void* d_out, int out_size, void* d_ws,
                              size_t ws_size, hipStream_t stream) {
  (void)hipFuncSetAttribute((const void*)fused, hipFuncAttributeMaxDynamicSharedMemorySize, LDS_BYTES);
  fused<<<256, 512, LDS_BYTES, stream>>>((const u16*)d_in[0], (const u16*)d_in[1], (const u16*)d_in[2], (const u16*)d_in[3],
                                         (const u16*)d_in[4], (const u16*)d_in[5], (const u16*)d_in[6], (const u16*)d_in[7],
                                         (const u16*)d_in[8], (u16*)d_out, (char*)d_ws);
}
```

```cpp
#define STAMPS
#include <hip/hip_runtime.h>

typedef unsigned short u16;
typedef unsigned long long u64;
typedef short s4 __attribute__((ext_vector_type(4)));
typedef __bf16 bf2 __attribute__((ext_vector_type(2)));
typedef __bf16 bf8 __attribute__((ext_vector_type(8)));
typedef float f2 __attribute__((ext_vector_type(2)));
typedef float f4 __attribute__((ext_vector_type(4)));
typedef unsigned u4 __attribute__((ext_vector_type(4)));
typedef unsigned u2 __attribute__((ext_vector_type(2)));
typedef __amdgpu_buffer_rsrc_t rsrc_t;
#define LAS __attribute__((address_space(3)))
#define GAS __attribute__((address_space(1)))
typedef LAS char* lptr;
#define RLX_AGENT __ATOMIC_RELAXED, __HIP_MEMORY_SCOPE_AGENT
#define DEV __device__ __forceinline__

constexpr int NB_ACT = 2048 * 576 * 2;
constexpr int NB_W = 576 * 1728 * 2;
constexpr int NB_WO = 576 * 576 * 2;
constexpr unsigned OOB = 0x80000000u;
constexpr unsigned NOTYET = 0xffffffffu;
constexpr int SC1 = 16;
constexpr int CODE_LINES = 248;
#define ROTK 0
#define PFS 12
#define TDELAY 0
#define YSH 0
#define PF0 2
#define HDELAY 6
#define PFA 6
#define PFB 9
#define PDELAY 0
#define EARLYHINT 1
#define HSLOT 0
#define PSLOT 0
#define PF1 4
#define LPRIO 0
#define HSWAP 1
#define UDELAY 1
#define YDELAY 0
#define OA1 2
#define OA2 0
#define PFS0 2
#define PFE 0
#define XSH 1
#define OSH 2
#define PSLEEP 2
constexpr int SPIN_MAX = 1 << 15;

constexpr int WS_FLAG = 4096;
constexpr int F_A = 0, F_P = 128, F_Y = 256, F_Q = 512, F_W = 640, F_G = 896, F_L = 960;
constexpr int WS_U = 1 << 20;
constexpr int WS_Y = WS_U + (3 << 20);
constexpr int WS_O = WS_Y + (3 << 20);
constexpr int WS_WO = WS_O + (3 << 20);
constexpr int WS_DBG = 16 << 20;

constexpr int LDS_BYTES = 163840;
constexpr int XB = 144 * 128;
constexpr int SLOT = XB + 64 * 432;
constexpr int PS = 1160, HS = 32640;
constexpr int L_WO = 65152;
constexpr int L_OT = L_WO + 83200;
constexpr int H_CP = 4160, U_OFF = 8448, CH_SZ = 13024;
constexpr int L_TAG = L_OT + 10240;
constexpr int L_CNT = L_TAG + 16;
constexpr int L_YA = L_TAG + 128;
constexpr int L_PF = LDS_BYTES - 256;

#define STAMPW(i, wv) do { if (threadIdx.x == (wv) * 64) *(LAS u64*)(size_t)(L_PF - 256 + (i) * 8) = __builtin_amdgcn_s_memtime(); } while (0)
#define STAMP(i) STAMPW(i, 0)
#define STAMPL(i) STAMPW(i, 4)

DEV rsrc_t mkrs(const void* p, int bytes) { return __builtin_amdgcn_make_buffer_rsrc((void*)p, 0, bytes, 0x00020000); }
DEV u4 mkdesc(const void* p, int bytes) {
  u64 a = (u64)p;
  return u4{(unsigned)a, (unsigned)(a >> 32) & 0xffffu, (unsigned)bytes, 0x00020000u};
}
DEV void dma16(u4 r, unsigned lds, unsigned voff, unsigned soff) {
  asm volatile("s_mov_b32 m0, %0\n\ts_nop 0\n\tbuffer_load_dwordx4 %1, %2, %3 offen lds"
               :: "s"(lds), "v"(voff), "s"(r), "s"(soff) : "memory");
}
DEV void dma16c(u4 r, unsigned lds, unsigned voff, unsigned soff) {
  asm volatile("s_mov_b32 m0, %0\n\ts_nop 0\n\tbuffer_load_dwordx4 %1, %2, %3 offen sc1 lds"
               :: "s"(lds), "v"(voff), "s"(r), "s"(soff) : "memory");
}
DEV void dma16m(u4 r, unsigned lds, unsigned voff, unsigned soff, u64 mask) {
  asm volatile("s_mov_b32 m0, %0\n\ts_mov_b64 exec, %4\n\tbuffer_load_dwordx4 %1, %2, %3 offen lds\n\ts_mov_b64 exec, -1"
               :: "s"(lds), "v"(voff), "s"(r), "s"(soff), "s"(mask) : "memory");
}
template <int AUX> DEV void st16(u4 v, rsrc_t r, unsigned off) { __builtin_amdgcn_raw_buffer_store_b128(v, r, off, 0, AUX); }
template <int AUX> DEV u4 ld16(rsrc_t r, unsigned off, unsigned soff) { return __builtin_amdgcn_raw_buffer_load_b128(r, off, soff, AUX); }
DEV u4 ld16c(u4 r, unsigned voff, unsigned soff) {
  u4 v;
  asm volatile("buffer_load_dwordx4 %0, %1, %2, %3 offen sc1" : "=v"(v) : "v"(voff), "s"(r), "s"(soff) : "memory");
  return v;
}
DEV void touch(u4 r, unsigned voff, unsigned soff) {
  asm volatile("s_mov_b32 m0, %0\n\ts_nop 0\n\tbuffer_load_dword %1, %2, %3 offen lds"
               :: "s"(L_PF), "v"(voff), "s"(r), "s"(soff) : "memory");
}
DEV s4 trread(lptr p) { return __builtin_amdgcn_ds_read_tr16_b64_v4i16((LAS s4*)p); }
DEV bf8 cat8(s4 a, s4 b) { return __builtin_bit_cast(bf8, __builtin_shufflevector(a, b, 0, 1, 2, 3, 4, 5, 6, 7)); }
DEV bf8 asb(u4 v) { return __builtin_bit_cast(bf8, v); }
DEV f4 mfma(bf8 a, bf8 b, f4 c) { return __builtin_amdgcn_mfma_f32_16x16x32_bf16(a, b, c, 0, 0, 0); }
DEV unsigned pk(float lo, float hi) { f2 v = {lo, hi}; return __builtin_bit_cast(unsigned, __builtin_convertvector(v, bf2)); }
DEV float asf(unsigned u) { return __builtin_bit_cast(float, u); }
DEV unsigned asu(float f) { return __builtin_bit_cast(unsigned, f); }
DEV float lo16(unsigned x) { return asf(x << 16); }
DEV float hi16(unsigned x) { return asf(x & 0xffff0000u); }
DEV float dot2(unsigned a, unsigned b, float c) {
  return __builtin_amdgcn_fdot2_f32_bf16(__builtin_bit_cast(bf2, a), __builtin_bit_cast(bf2, b), c, false);
}
DEV float dot2z(unsigned a, unsigned b) {
  float d;
  asm("v_dot2_f32_bf16 %0, %1, %2, 0" : "=v"(d) : "v"(a), "v"(b));
  return d;
}
DEV float dot2a(unsigned a, unsigned b, float c) {
  float d;
  asm("v_dot2_f32_bf16 %0, %1, %2, %3" : "=v"(d) : "v"(a), "v"(b), "v"(c));
  return d;
}
DEV unsigned alignb(unsigned hi, unsigned lo) { return __builtin_amdgcn_alignbit(hi, lo, 16); }
DEV unsigned umax(unsigned a, unsigned b) { return a > b ? a : b; }
DEV unsigned max4(u4 v, unsigned m) { return umax(umax(umax(v[0], v[1]), m), umax(v[2], v[3])); }
DEV unsigned max3u(unsigned a, unsigned b, unsigned c) { return umax(umax(a, b), c); }
DEV unsigned max12(u4 a, u4 b, u4 c) {
  return umax(max3u(max3u(max3u(a[0], a[1], a[2]), a[3], b[0]), b[1], b[2]), max3u(max3u(b[3], c[0], c[1]), c[2], c[3]));
}
DEV unsigned chk(u4 v, unsigned key) { return (v[0] + v[1] + v[2]) + (v[3] + key); }
DEV unsigned rowsum(unsigned x) {
  auto a = __builtin_amdgcn_permlane16_swap(x, x, false, false);
  const unsigned y = a[0] + a[1];
  auto b = __builtin_amdgcn_permlane32_swap(y, y, false, false);
  return b[0] + b[1];
}
DEV float fmax2(float a, float b) { return __builtin_elementwise_maximum(a, b); }
#define WAITV(n) asm volatile("s_waitcnt vmcnt(" #n ")" ::: "memory")
#define WAITL0() asm volatile("s_waitcnt lgkmcnt(0)" ::: "memory")
#define SB() __builtin_amdgcn_sched_barrier(0)
#define SGB(m, n) __builtin_amdgcn_sched_group_barrier(m, n, 0)
DEV void bar() { WAITL0(); __builtin_amdgcn_s_barrier(); asm volatile("" ::: "memory"); }
template <class T> DEV void pin(T& x) { asm volatile("" : "+v"(x)); }

DEV u64 get_tag(char* ws) {
  u64 e;
  asm volatile("s_load_dwordx2 %0, %1, 0x0\n\ts_waitcnt lgkmcnt(0)" : "=s"(e) : "s"(ws) : "memory");
  return e * 6364136223846793005ull + 1442695040888963407ull;
}
DEV void post_flag(char* ws, int idx, u64 tag) { __hip_atomic_store((GAS u64*)(ws + WS_FLAG + idx * 128), tag, RLX_AGENT); }
DEV void wait_flags(char* ws, int idx, u64 tag) {
  GAS u64* p = (GAS u64*)(ws + WS_FLAG + idx * 128);
#pragma unroll 1
  for (int spin = 0; spin < SPIN_MAX; ++spin) {
    if (__all(__hip_atomic_load(p, RLX_AGENT) == tag)) break;
    __builtin_amdgcn_s_sleep(PSLEEP);
  }
  __builtin_amdgcn_fence(__ATOMIC_ACQUIRE, "wavefront");
}

DEV void arrive(unsigned word) {
  unsigned one = 1;
  asm volatile("s_waitcnt lgkmcnt(0)\n\ts_mov_b64 exec, 1\n\tds_add_u32 %0, %1\n\ts_mov_b64 exec, -1"
               :: "v"(word), "v"(one) : "memory");
}
DEV void reach(unsigned word, unsigned n) {
#pragma unroll 1
  for (int spin = 0; spin < (1 << 22); ++spin) {
    unsigned v;
    asm volatile("ds_read_b32 %0, %1\n\ts_waitcnt lgkmcnt(0)" : "=v"(v) : "v"(word) : "memory");
    if ((unsigned)__builtin_amdgcn_readfirstlane(v) >= n) break;
    __builtin_amdgcn_s_sleep(1);
  }
}

DEV u4 ld2(lptr p) {
  const u2 a = *(const LAS u2*)p, b = *(const LAS u2*)(p + HS);
  return u4{a[0], a[1], b[0], b[1]};
}
DEV u64 lds_tag(lptr sm) {
  reach(L_CNT, 1);
  return *(LAS u64*)(sm + L_TAG);
}
DEV void h_dma(const u16* hk, int c, int ch, int lane) {
  const u4 rh = mkdesc(hk, NB_ACT);
#pragma unroll
  for (int i = 0; i < 4; ++i) dma16(rh, c * CH_SZ + H_CP + 32 + i * 1024, lane * 16, ch * 4096 + i * 1024);
}
DEV void h_shift(lptr cb, int lane) {
  if (lane < 34) *(LAS u4*)(cb + (lane < 30 ? U_OFF + lane * 16 : ((lane >> 1) & 1) * H_CP + (lane & 1) * 16)) = u4{0, 0, 0, 0};
  lptr hb = cb + 32 + lane * 16;
  u4 ha[4];
  unsigned hp[4];
#pragma unroll
  for (int i = 0; i < 4; ++i) { ha[i] = *(LAS u4*)(hb + H_CP + i * 1024); hp[i] = *(LAS unsigned*)(hb + H_CP - 4 + i * 1024); }
#pragma unroll
  for (int i = 0; i < 4; ++i)
    *(LAS u4*)(hb + i * 1024) =
        u4{alignb(ha[i][0], hp[i]), alignb(ha[i][1], ha[i][0]), alignb(ha[i][2], ha[i][1]), alignb(ha[i][3], ha[i][2])};
}
DEV void att_consts(lptr sm, int lane, u4 (&tc)[9], int first) {
  const LAS u4* tb = (const LAS u4*)(sm + L_OT) + lane;
#pragma unroll
  for (int i = 0; i < 9; ++i) tc[i] = tb[(i < 4 ? i : i == 4 ? 4 + first : i + 1) * 64];
}

struct Proj { unsigned wa, wb, ob, oh, oo0, oo1, oo2, ot, ow, fl; };
DEV void proj_consts(Proj& p, int bid, int lw, int lane) {
  const int rt = ((bid + OSH) & 7) * 4 + ((bid >> 6) & 1) * 2 + (bid >> 7), ct = (bid >> 3) & 7;
  const int r = lane & 15, g = lane >> 4;
  p.wa = L_WO + lane * 16;
  p.wb = L_WO + 4096 + (g * 8 + (r & 7)) * 16;
  p.ob = rt * 72 * 1024 + g * 1024 + lw * 256 + r * 16;
  p.oh = NB_ACT + (rt * 4 + lw) * 512 + lane * 4;
  pin(p.oh);
  unsigned oo[3];
#pragma unroll
  for (int i = 0; i < 3; ++i) {
    const int it = lane + 64 * i, row = (it * 57) >> 9, c = it - row * 9;
    oo[i] = (i == 2 && lane >= 16) ? OOB : (rt * 64 + 32 * (lw >> 1) + (lw & 1)) * 2304 + __umul24(row, 4608) + ct * 144 + c * 16;
  }
  p.oo0 = oo[0]; p.oo1 = oo[1]; p.oo2 = oo[2];
  p.ot = L_OT + lw * 2304 + lane * 16;
  p.ow = L_OT + lw * 2304 + r * 144 + g * 8;
  p.fl = ((rt & 1) ? F_A : F_L) + (rt >> 1) * 8 + (lane & 7);
  pin(p.wa); pin(p.wb); pin(p.ob); pin(p.oo0); pin(p.oo1); pin(p.oo2); pin(p.ot); pin(p.ow); pin(p.fl);
}

template <int HI>
DEV void g1_compute(lptr sm, int n0, int lane, u4 (&tc)[9], int first) {
  const int r = lane & 15, g = lane >> 4;
  lptr xa0 = sm + r * 128 + ((g ^ ((r >> 1) & 7)) << 4);
  lptr xa1 = (lptr)((size_t)xa0 ^ 64);
  lptr wa = sm + XB + (((g >> 1) << 4) + ((g & 1) << 3) + ((r >> 2) << 1)) * 432 + (lane & 3) * 8 + n0 * 32;
#define USED(m, n) (HI ? ((n) > 0 || (m) >= 5) : ((n) < 3 || (m) < 5))
  f4 acc[9][4];
#pragma unroll
  for (int m = 0; m < 9; ++m)
#pragma unroll
    for (int n = 0; n < 4; ++n) {
      acc[m][n] = f4{0.f, 0.f, 0.f, 0.f};
      if (USED(m, n)) pin(acc[m][n]);
    }
  bf8 xf[9], wf[2][4];
  auto mm = [&](bf8 (&wv)[4], int m) {
#pragma unroll
    for (int n = 0; n < 4; ++n)
      if (USED(m, n)) acc[m][n] = mfma(wv[n], xf[m], acc[m][n]);
  };
  auto rdx = [&](lptr xp, int m) { xf[m] = *(const LAS bf8*)(xp + m * 2048); };
  auto rdw = [&](lptr wp, bf8 (&wv)[4], int n) { wv[n] = cat8(trread(wp + n * 32), trread(wp + 432 + n * 32)); };
  auto mix = [&]() {
#pragma unroll
    for (int i = 0; i < 4; ++i) { SGB(0x8, 1); SGB(0x100, 1); }
    SB();
  };
  auto step = [&](bf8 (&wc)[4], bf8 (&wn)[4], lptr xp, lptr wp, int m0) {
#pragma unroll
    for (int m = m0; m < 9; ++m) {
      mm(wc, m);
      if (m - m0 < 4) rdw(wp, wn, m - m0);
      if (m0 == 0) { if (m > 0) rdx(xp, m - 1); }
      else if (m - m0 < 4) rdx(xp, m - m0);
      else { rdx(xp, 2 * (m - m0) - 4); if (2 * (m - m0) - 3 < m) rdx(xp, 2 * (m - m0) - 3); }
      mix();
    }
#pragma unroll
    for (int m = (m0 == 0 ? 8 : 2 * (9 - m0) - 4); m < 9; ++m) rdx(xp, m);
    SB();
  };
  constexpr int MS = 3;
  bar();
#pragma unroll
  for (int n = 0; n < 4; ++n) rdw(wa, wf[0], n);
#pragma unroll
  for (int m = 0; m < 9; ++m) rdx(xa0, m);
  SB();
  int so = 0;
#pragma unroll 1
  for (int k = 0; k < 8; ++k) {
    step(wf[0], wf[1], xa1 + so, wa + so + 32 * 432, 0);
    so = so == 2 * SLOT ? 0 : so + SLOT;
#pragma unroll
    for (int m = 0; m < MS; ++m) mm(wf[1], m);
    SB();
    bar();
    step(wf[1], wf[0], xa0 + so, wa + so, MS);
  }
  xa1 += 2 * SLOT; wa += 2 * SLOT + 32 * 432;
  pin(xa1); pin(wa);
  STAMP(1);
  lptr d = sm + (2 * n0 + (g >> 1)) * PS + r * 8 + (g & 1) * HS;
  bf8 xg[9];
#pragma unroll
  for (int m = 0; m < 11; ++m) {
    if (m < 9) mm(wf[0], m);
    if (m < 2) { rdw(wa, wf[1], 2 * m); rdw(wa, wf[1], 2 * m + 1); }
    if (m < 9) xg[m] = *(const LAS bf8*)(xa1 + m * 2048);
    if (m >= 2) {
#pragma unroll
      for (int n = 0; n < 4; ++n)
        if (USED(m - 2, n)) acc[m - 2][n] = mfma(wf[1][n], xg[m - 2], acc[m - 2][n]);
    }
    if (m == 7) att_consts(sm, lane, tc, first);
    if (m == 8) arrive(L_CNT + 8);
#pragma unroll
    for (int o = m - 3; o < (m == 10 ? 9 : m - 2); ++o) {
      if (o >= 0) {
#pragma unroll
        for (int n = 0; n < 4; ++n)
          if (USED(o, n))
            *(LAS u2*)(d + n * 2 * PS + o * 128) = u2{pk(acc[o][n][0], acc[o][n][1]), pk(acc[o][n][2], acc[o][n][3])};
      }
    }
    if (m < 3) {
#pragma unroll
      for (int i = 0; i < 4; ++i) { SGB(0x8, 1); SGB(0x100, 2); }
    } else {
      SGB(0x8, 1); SGB(0x100, 1);
#pragma unroll
      for (int i = 0; i < 4; ++i) { SGB(0x8, 1); SGB(0x2, 2); SGB(0x8, 1); SGB(0x200, 1); }
    }
    SB();
  }
}

DEV void role_a(lptr sm, char* ws, const u16* x, const u16* wqkv, int bid, int w, int lane, Proj& pc, unsigned vw) {
  const int xcd = bid & 7, j = bid >> 3;
  const int rt = (xcd >> 1) * 4 + (j >> 2), hq = (xcd & 1) * 4 + (j & 3);
  const u4 rp = mkdesc(ws + WS_WO + (j & 7) * 82944, 82944);
  u64 tag;
  u4 tc[9], wr[21];
  const int aw = w ^ 4;
  const int first = rt + (aw >> 1) == 0;
  if (__builtin_expect(w >= 4, 1)) {
    const int lw = w - 4;
    const u4 rx = mkdesc(x, NB_ACT), rw = mkdesc(wqkv, NB_W);
    auto wpiece = [&](unsigned d, int kt, int i) {
      dma16m(rw, d + XB + (lw * 8 + i) * 864, vw, (kt * 64 + lw * 16 + 8 * (i >> 2) + (i & 3)) * 3456, (1ull << 54) - 1);
    };
    auto tailp = [&](unsigned d, int kt) {
#pragma unroll
      for (int i = 2; i < 8; ++i) wpiece(d, kt, i);
    };
    STAMPL(16);
    tailp(0, 0);
    SB();
    unsigned vx[6];
#pragma unroll
    for (int i = 0; i < 6; ++i) {
      const int xr = lw * 36 + i * 6 + (lane >> 3);
      vx[i] = __umul24(rt * 128 + xr, 1152) + (((lane & 7) ^ ((xr >> 1) & 7)) << 4) - 16 * 1152;
    }
    auto headp = [&](unsigned d, int kt) {
#pragma unroll
      for (int i = 0; i < 6; ++i) dma16m(rx, d + (lw * 6 + i) * 768, vx[i], kt * 128, (1ull << 48) - 1);
#pragma unroll
      for (int i = 0; i < 2; ++i) wpiece(d, kt, i);
    };
    headp(0, 0);
    unsigned d = SLOT;
    const int fi = F_W + (j & 7) * 32 + (lane < 18 ? lane : 0);
    GAS u64* fp = (GAS u64*)(ws + WS_FLAG + fi * 128);
    u64 fw = 0;
#pragma unroll 1
    for (int k = 1; k < 9; ++k) {
      headp(d, k);
      WAITV(8);
      bar();
      STAMPL(16 + k);
      if (k == 8 && lw == 3) asm volatile("global_load_dwordx2 %0, %1, off sc1" : "+v"(fw) : "v"(fp) : "memory");
      tailp(d, k);
      d = d == 2 * SLOT ? 0 : d + SLOT;
    }
    STAMPL(26);
    tag = *(LAS u64*)(sm + L_TAG);
    att_consts(sm, lane, tc, first);
    proj_consts(pc, bid, lw, lane);
    asm volatile("s_waitcnt vmcnt(0)" : "+v"(fw) :: "memory");
    if (lw == 3 && !__all(fw == tag)) wait_flags(ws, fi, tag);
    bar();
    STAMPL(27);
#pragma unroll
    for (int i = 0; i < 21; ++i) wr[i] = ld16c(rp, lane * 16, (4 * i + lw) * 1024);
    __builtin_amdgcn_s_setprio(LPRIO);
    STAMPL(28);
  } else {
    tag = get_tag(ws);
    if (threadIdx.x == 0) { *(LAS u64*)(sm + L_TAG) = tag; *(LAS u4*)(sm + L_CNT) = u4{0, 0, 0, 0}; *(LAS u4*)(sm + L_CNT + 16) = u4{0, 0, 0, 0}; *(LAS u4*)(sm + L_CNT + 32) = u4{0, 0, 0, 0}; }
    {
      const int r = lane & 15, g = lane >> 4, q4 = r >> 2, p4 = lane & 3;
      LAS u4* tb = (LAS u4*)(sm + L_OT) + lane;
      u4 t0, t1;
      if (w < 2) {
#pragma unroll
        for (int dd = 0; dd < 4; ++dd) {
          t0[dd] = __builtin_amdgcn_sbfe((int)(0x1ffu << (2 * w)), 4 * g + dd, 1);
          t1[dd] = __builtin_amdgcn_sbfe((int)(0x3feu << (2 * w)), 4 * g + dd, 1);
        }
      } else if (w == 2) {
#pragma unroll
        for (int dd = 0; dd < 4; ++dd) {
          const int kk = 4 * g + dd - r;
          t0[dd] = (kk >= 1 && kk <= 8) ? 0u : 0xff800000u;
          t1[dd] = (kk >= 1 && kk <= 8 && g >= 2) ? 0u : 0xff800000u;
        }
      } else {
#pragma unroll
        for (int dd = 0; dd < 4; ++dd) t0[dd] = 4 * g + dd - r <= -8 ? 0u : 0xff800000u;
        t1[0] = (t0[0] ? 0u : 0xffffu) | (t0[1] ? 0u : 0xffff0000u);
        t1[1] = (t0[2] ? 0u : 0xffffu) | (t0[3] ? 0u : 0xffff0000u);
        t1[2] = g * PS + r * 16;
        t1[3] = g * PS + (r & 7) * 16;
        const int kr0 = (4 * g + q4) * 16, kr1 = kr0 + (g >= 2 ? 128 : 256);
        const int ve = (p4 >> 1) * PS + (p4 & 1) * HS, vo = ((p4 + 1) >> 1) * PS + ((p4 + 1) & 1) * HS;
        tb[8 * 64] = u4{(unsigned)(kr0 + ve), (unsigned)(kr1 + ve), (unsigned)(kr0 + vo), (unsigned)(kr1 + vo)};
        const unsigned os = (rt * 144 + hq * 9 + g) * 1024 + r * 16;
        tb[9 * 64] = u4{(unsigned)kr0, (unsigned)kr1, os, g ? OOB : os + 8192};
      }
      tb[2 * w * 64] = t0;
      tb[(2 * w + 1) * 64] = t1;
    }
    if (w & 1) g1_compute<1>(sm, 3 + (w >> 1) * 7, lane, tc, first);
    else g1_compute<0>(sm, (w >> 1) * 7, lane, tc, first);
#pragma unroll
    for (int i = 0; i < 21; ++i) asm volatile("" : "=v"(wr[i]));
    asm volatile("" : "=v"(pc.wa), "=v"(pc.wb), "=v"(pc.ob), "=v"(pc.oh), "=v"(pc.oo0), "=v"(pc.oo1), "=v"(pc.oo2),
                 "=v"(pc.ot), "=v"(pc.ow), "=v"(pc.fl));
  }
  {
    const unsigned rb = (32 * (aw >> 1) + (aw & 1)) * 8;
    const u4 qm[4] = {tc[0], tc[1], tc[2], tc[3]}, u0 = tc[4], u1 = tc[5], c0 = tc[6], c1 = tc[7], c2 = tc[8];
    const f4 b0 = __builtin_bit_cast(f4, u0), b1 = __builtin_bit_cast(f4, u1);
    const unsigned m01 = c0[0], m23 = c0[1];
    lptr qa = sm + rb + c0[2], k1 = sm + rb + c0[3];
    lptr ve0 = sm + rb + c1[0], ve1 = sm + rb + c1[1], vo0 = sm + rb + c1[2], vo1 = sm + rb + c1[3];
    lptr vk0 = sm + rb + c2[0], vk1 = sm + rb + c2[1];
    const unsigned ow = (aw >> 2) * 73728 + (aw & 3) * 256;
    unsigned os0 = c2[2] + ow, os1 = os0 + 4096, os2 = c2[3] + ow;
    const bool g0 = lane < 16;
    rsrc_t ro = mkrs(ws + WS_O, NB_ACT + 65536);
    unsigned oh = g0 ? NB_ACT + ((rt * 8 + aw) * 8 + hq) * 64 + lane * 4 : OOB;
    pin(oh);
    u4 ones = {0x3f803f80u, 0x3f803f80u, 0x3f803f80u, 0x3f803f80u};
    pin(qa); pin(k1); pin(ve0); pin(ve1); pin(vo0); pin(vo1); pin(vk0); pin(vk1); pin(os0); pin(os1); pin(os2); pin(ones);
    STAMP(14);
    if (w < 4) arrive(L_CNT);
    reach(w < 4 ? L_CNT : L_CNT + 8, 4);
    STAMP(2);
    STAMPL(29);
    const float cs = 0.23570226039551584f * 1.4426950408889634f;
    const f4 z = {0.f, 0.f, 0.f, 0.f};
    u4 qf[4];
    bf8 ka[4], kb[4], vt0[4], vt1[4];
#pragma unroll
    for (int h = 0; h < 4; ++h) {
      qf[h] = ld2(qa + 2 * h * PS + 128);
      ka[h] = asb(ld2(qa + (9 + 2 * h) * PS));
      kb[h] = asb(ld2(k1 + (9 + 2 * h) * PS + 256));
      SB();
    }
#pragma unroll
    for (int h = 0; h < 4; ++h) {
      vt0[h] = cat8(trread((h < 2 ? ve0 : vo0) + (18 + 2 * h) * PS), trread((h < 2 ? ve1 : vo1) + (18 + 2 * h) * PS));
      vt1[h] = cat8(trread(vk0 + (20 + 2 * h) * PS + (h < 2 ? 0 : HS)), trread(vk1 + (20 + 2 * h) * PS + (h < 2 ? 0 : HS)));
    }
    SB();
    f4 s0[4], s1[4], s[4];
    float mx[4];
#pragma unroll
    for (int h = 0; h < 4; ++h) {
      bf8 qq = asb(qf[h] & qm[h]);
      s0[h] = mfma(ka[h], qq, b0);
      s1[h] = mfma(kb[h], qq, b1);
      SB();
    }
#pragma unroll
    for (int h = 0; h < 4; ++h) {
#pragma unroll
      for (int dd = 0; dd < 4; ++dd) s[h][dd] = fmax2(s0[h][dd], s1[h][dd]);
      mx[h] = fmax2(fmax2(fmax2(s[h][0], s[h][1]), s[h][2]), s[h][3]);
      SB();
    }
    {
      auto a = __builtin_amdgcn_permlane16_swap(asu(mx[0]), asu(mx[1]), false, false);
      auto b = __builtin_amdgcn_permlane16_swap(asu(mx[2]), asu(mx[3]), false, false);
      auto c = __builtin_amdgcn_permlane32_swap(asu(fmax2(asf(a[0]), asf(a[1]))), asu(fmax2(asf(b[0]), asf(b[1]))), false, false);
      unsigned m = asu(-cs * fmax2(asf(c[0]), asf(c[1])));
      auto e = __builtin_amdgcn_permlane32_swap(m, m, false, false);
      unsigned e0 = e[0], e1 = e[1];
      auto f0 = __builtin_amdgcn_permlane16_swap(e0, e0, false, false);
      auto f1 = __builtin_amdgcn_permlane16_swap(e1, e1, false, false);
      mx[0] = asf(f0[0]); mx[1] = asf(f0[1]); mx[2] = asf(f1[0]); mx[3] = asf(f1[1]);
    }
    SB();
    asm volatile("s_waitcnt vmcnt(0)" : "+v"(wr[0]), "+v"(wr[1]), "+v"(wr[2]), "+v"(wr[3]), "+v"(wr[4]), "+v"(wr[5]),
                 "+v"(wr[6]), "+v"(wr[7]), "+v"(wr[8]), "+v"(wr[9]), "+v"(wr[10]), "+v"(wr[11]), "+v"(wr[12]),
                 "+v"(wr[13]), "+v"(wr[14]), "+v"(wr[15]), "+v"(wr[16]), "+v"(wr[17]), "+v"(wr[18]), "+v"(wr[19]),
                 "+v"(wr[20]) :: "memory");
    if ((threadIdx.x & 255) == 192) post_flag(ws, (w < 4 ? F_A : F_L) + rt * 8 + hq, tag);
    f4 sum[4], o0[4], o1[4];
#pragma unroll
    for (int h = 0; h < 4; ++h) {
      float p[4];
#pragma unroll
      for (int dd = 0; dd < 4; ++dd) p[dd] = __builtin_amdgcn_exp2f(__builtin_fmaf(s[h][dd], cs, mx[h]));
      unsigned p01 = pk(p[0], p[1]), p23 = pk(p[2], p[3]);
      bf8 pf = asb(u4{p01 & ~m01, p23 & ~m23, p01 & m01, p23 & m23});
      sum[h] = mfma(asb(ones), pf, z);
      o0[h] = mfma(vt0[h], pf, z);
      o1[h] = mfma(vt1[h], pf, z);
      SB();
    }
    u2 gr[4];
    u4 left;
#pragma unroll
    for (int h = 0; h < 4; ++h) {
      const float inv = __builtin_amdgcn_rcpf(sum[h][0]);
      const unsigned a0 = pk(o0[h][0] * inv, o0[h][1] * inv), a1 = pk(o0[h][2] * inv, o0[h][3] * inv);
      const unsigned e0 = pk(o1[h][0] * inv, o1[h][1] * inv);
      if (h & 1) {
        const unsigned e1 = pk(o1[h][2] * inv, o1[h][3] * inv);
        left[h] = a1;
        gr[h] = u2{g0 ? e0 : a0, g0 ? e1 : a1};
        st16<SC1>(u4{gr[h - 1][0], gr[h - 1][1], gr[h][0], gr[h][1]}, ro, h == 1 ? os0 : os1);
        SB();
      } else {
        left[h] = e0;
        gr[h] = u2{a0, a1};
      }
    }
    st16<SC1>(left, ro, os2);
    const unsigned cs0 = chk(u4{gr[0][0], gr[0][1], gr[1][0], gr[1][1]}, chk(u4{gr[2][0], gr[2][1], gr[3][0], gr[3][1]}, 0));
    __builtin_amdgcn_raw_buffer_store_b32(chk(left, rowsum(cs0) + (unsigned)tag), ro, oh, 0, SC1);
  }
  STAMP(3);
  STAMPL(4);
  if (w >= 4) {
    __builtin_amdgcn_s_setprio(0);
    u64 fv = 0;
    GAS u64* fp = (GAS u64*)(ws + WS_FLAG + pc.fl * 128);
#pragma unroll
    for (int i = 0; i < 21; ++i) {
      if (i < 20 || w == 4) *(LAS u4*)(sm + L_WO + (4 * i + w - 4) * 1024 + lane * 16) = wr[i];
      if (i == 10 && w == 5) asm volatile("global_load_dwordx2 %0, %1, off sc1" : "+v"(fv) : "v"(fp) : "memory");
    }
    arrive(L_CNT + 20);
    if (w == 5) {
      asm volatile("s_waitcnt vmcnt(0)" : "+v"(fv) :: "memory");
      if (!__all(fv == tag)) wait_flags(ws, pc.fl, tag);
      arrive(L_CNT + 28);
    }
    STAMPL(30);
  }
}

DEV void role_b(lptr sm, char* ws, const u16* x, const u16* wqkv, const u16* wo, const u16* q, const u16* hk, const u16* dv, int bid, int w, int lane, unsigned fw, unsigned fx) {
  const int tid = threadIdx.x, xcd = bid & 7, jb = (bid >> 3) - 16, me = xcd * 16 + jb;
  const int G = lane >> 4, i16 = lane & 15, w4 = w & 3;
  rsrc_t ru = mkrs(ws + WS_U, NB_ACT + NB_ACT / 4);
  const int xc = (xcd + XSH) & 7, cm = xc * 16 + jb;
  const int c5 = 512 + xcd * 8 + (jb & 7);
  const unsigned tb = L_WO + w * 9216;
  const u4 ny = {NOTYET, NOTYET, NOTYET, NOTYET};
  rsrc_t ry = mkrs(ws + WS_Y, NB_ACT);
  u64 tag;
  if (w < 4 || (w == 4 && jb < 8)) {
    const int grp = w < 4 ? xcd : 8, un = w < 4 ? jb * 4 + w : xcd * 8 + jb;
    const int q4 = i16 >> 2, p4 = lane & 3;
    const int f0 = (q4 >> 1) | (p4 << 1), f1 = (q4 >> 1) | (((p4 + 1) & 3) << 1);
    const unsigned a0 = tb + (8 * p4 + q4) * 128 + (((G >> 1) ^ f0) << 4) + (G & 1) * 8;
    const unsigned a1 = tb + (8 * p4 + q4) * 128 + (((G >> 1) ^ f1) << 4) + (G & 1) * 8;
    lptr wp = sm + L_OT + w * 512 + (G * 4 + (i16 & 3)) * 8;
    unsigned off = ((grp * 16 + G) * 256 + 4 * un) * 64 + i16 * 16;
    u2 K[3], V[2], wk;
    auto rd = [&](int s) {
      lptr b0 = (lptr)(size_t)(a0 ^ (s << 5)), b1 = (lptr)(size_t)(a1 ^ (s << 5));
      wk = *(LAS u2*)(wp + s * 128);
      K[0] = __builtin_bit_cast(u2, trread(b0 + 512));
      K[1] = __builtin_bit_cast(u2, trread(b1 + 1024));
      K[2] = __builtin_bit_cast(u2, trread(b1 + 1536));
      V[0] = __builtin_bit_cast(u2, trread(b0 + 5120));
      V[1] = __builtin_bit_cast(u2, trread(b0 + 5120 + 512));
    };
    if (w < 4) h_dma(hk, w, cm * 4 + w, lane);
    tag = lds_tag(sm);
    const unsigned key = (unsigned)tag;
    if (w < 4) WAITV(4); else WAITV(0);
    if (w < 4) arrive(L_CNT + 44);
    STAMP(1);
    rd(0);
#pragma unroll 1
    for (int s = 0; s < 4; ++s) {
      const unsigned wA = alignb(wk[1], wk[1]), wB = alignb(wk[0], wk[0]);
      const unsigned kk[6] = {K[0][0], K[0][1], K[1][0], K[1][1], K[2][0], K[2][1]};
      const unsigned vv[4] = {V[0][0], V[0][1], V[1][0], V[1][1]};
      rd((s + 1) & 3);
      unsigned sh[5];
      float ev[4], od[4], ve[4], vo[4];
#pragma unroll
      for (int d = 0; d < 5; ++d) sh[d] = alignb(kk[d + 1], kk[d]);
      SB();
#pragma unroll
      for (int d = 0; d < 4; ++d) { ev[d] = dot2z(sh[d], wA); od[d] = dot2z(kk[d + 1], wA); }
      SB();
#pragma unroll
      for (int d = 0; d < 4; ++d) { ev[d] = dot2a(sh[d + 1], wB, ev[d]); od[d] = dot2a(kk[d + 2], wB, od[d]); }
      SB();
#pragma unroll
      for (int d = 0; d < 4; ++d) { ve[d] = lo16(vv[d]); vo[d] = hi16(vv[d]); pin(ve[d]); pin(vo[d]); }
      SB();
      u4 o;
#pragma unroll
      for (int d = 0; d < 4; ++d) o[3 - d] = pk(od[d] * vo[d], ev[d] * ve[d]);
      st16<SC1>(o, ru, off);
      __builtin_amdgcn_raw_buffer_store_b32(chk(o, key), ru, NB_ACT + (off >> 2), 0, SC1);
      off += 65536;
      SB();
    }
    STAMP(2);
    if (w < 4) arrive(L_CNT + 40);
  } else if (w == 7) {
    const u4 rx = mkdesc(x, NB_ACT), rw = mkdesc(wqkv, NB_W);
    __builtin_amdgcn_s_setprio(3);
    STAMPW(22, 7);
    if (PDELAY) __builtin_amdgcn_s_sleep(PDELAY);
#pragma unroll 1
    for (int st = PFS0; st < 9; ++st) {
      if (st == PF1) reach(L_CNT + 44, 4);
      if (st == PFA) reach(L_CNT + 40, 4);
      if (st == PFB) reach(L_CNT + 4, 1);
      touch(rw, fw, st * 64 * 3456);
      touch(rx, fx, st * 128);
    }
    STAMPW(23, 7);
    reach(L_CNT + 4, 1);
    const u4 rq = mkdesc(q, NB_ACT);
    touch(rq, (jb * 128 + lane) * 1152 + ((xcd + YSH) & 7) * 128, 0);
    touch(rq, (jb * 128 + lane) * 1152 + ((xcd + YSH) & 7) * 128, 64 * 1152);
    if (jb >= 8) {
      const int un = bid - 192;
      touch(rq, lane < 32 ? ((un >> 2) * 128 + 64 * (un & 1) + 8 * ((un >> 1) & 1) + (lane >> 3) * 16 + (lane & 7)) * 1152 + 1024 : OOB, 0);
    }
    const unsigned y4 = (xc * 32 + (lane >> 5)) * 8192 + jb * 512 + (lane & 31) * 16;
#pragma unroll 1
    for (int i = 0; i < 16; i += 2) { st16<SC1>(ny, ry, y4 + i * 16384); st16<SC1>(ny, ry, y4 + i * 16384 + 16384); }
    if (jb >= 8) {
#pragma unroll
      for (int i = 0; i < 4; ++i) st16<SC1>(ny, ry, (256 + i * 8 + (lane >> 3)) * 8192 + (c5 & 63) * 128 + (lane & 7) * 16);
    }
    tag = lds_tag(sm);
    WAITV(0);
    arrive(L_CNT + 16);
    if (lane == 0) post_flag(ws, F_Y + cm, tag);
    STAMPW(25, 7);
    __builtin_amdgcn_s_sleep(12);
    if (jb >= 8) __builtin_amdgcn_s_sleep(16);
    wait_flags(ws, F_Y + (jb < 8 ? xcd * 16 + i16 : (lane >> 3) * 16 + 8 + (lane & 7)), tag);
    arrive(L_CNT + 24);
  } else if (w == 6) {
    if (jb >= 8) {
      reach(L_CNT + 40, 4);
      h_dma(hk, 4, c5, lane);
      WAITV(0);
      h_shift(sm + 4 * CH_SZ, lane);
      arrive(L_CNT + 32);
    }
  } else {
    const int slot = w == 5 ? jb : 2 + jb;
    if (w == 5) {
      tag = get_tag(ws);
      if (lane == 0) *(LAS u64*)(sm + L_TAG) = tag;
      arrive(L_CNT);
      if (lane == 0) post_flag(ws, F_P + me, tag);
    } else {
      tag = lds_tag(sm);
    }
    if (w == 5 || jb >= 14) {
      const u4 rwo = mkdesc(wo, NB_WO);
      const int e4 = (lane * 57) >> 9;
      const unsigned vb = (e4 >> 1) * 1152 + xcd * 144 + (lane - e4 * 9) * 16, vt = (e4 & 1) * 1152;
      int ks = slot + ROTK * xcd;
      if (ks >= 18) ks -= 18;
#pragma unroll 1
      for (int i = 0; i < 8; ++i) {
        const int ci = 4 * ks + (i >> 1), h = (ci * 57) >> 9, c = ci - 9 * h;
        const int row = h * 72 + (c == 8 ? 16 : 36 * (c >> 2) + 4 * (c & 3)) + 2 * (i & 1);
        dma16m(rwo, tb + i * 576, vb + __umul24(vt, c == 8 ? 36 : (c & 3) ? 16 : 32), row * 1152, (1ull << 36) - 1);
      }
      WAITV(0);
      rsrc_t rpw = mkrs(ws + WS_WO, NB_WO);
      lptr p = (lptr)(size_t)tb + (G * 8 + ((i16 >> 2) << 1)) * 144 + (lane & 3) * 8;
      const unsigned po = (xcd * 18 + slot) * 4608;
      u4 f[5];
#pragma unroll
      for (int n = 0; n < 5; ++n) f[n] = __builtin_bit_cast(u4, cat8(trread(p + n * 32), trread(p + 144 + n * 32)));
#pragma unroll
      for (int n = 0; n < 4; ++n) st16<SC1>(f[n], rpw, po + n * 1024 + lane * 16);
      st16<SC1>(f[4], rpw, i16 < 8 ? po + 4096 + (G * 8 + i16) * 16 : OOB);
      WAITV(0);
      if (lane == 0) post_flag(ws, F_W + xcd * 32 + slot, tag);
      STAMPW(21, 5);
    }
  }
  const int r = lane & 15, g = lane >> 4;
  if (w >= 4) {
    if (w < 6) {
      const int ct = (bid >> 3) & 7;
      __builtin_amdgcn_s_setprio(2);
      if (w == 5) {
        reach(L_CNT + 4, 1);
        wait_flags(ws, F_W + ct * 32 + (lane < 18 ? lane : 0), tag);
      } else if (jb >= 8) {
        reach(L_CNT + 4, 1);
        u4 u5[4];
#pragma unroll 1
        for (int spin = 0; spin < SPIN_MAX; ++spin) {
          unsigned hx[4], bad = 0;
#pragma unroll
          for (int i = 0; i < 4; ++i) {
            u5[i] = ld16<SC1>(ru, lane * 64 + (c5 & 3) * 16, (c5 >> 2) * 16384 + i * 4096);
            hx[i] = __builtin_amdgcn_raw_buffer_load_b32(ru, lane * 16 + (c5 & 3) * 4, NB_ACT + (c5 >> 2) * 4096 + i * 1024, SC1);
          }
#pragma unroll
          for (int i = 0; i < 4; ++i) bad |= chk(u5[i], (unsigned)tag) ^ hx[i];
          if (__all(bad == 0)) break;
          asm volatile("s_sleep 4" ::: "memory");
        }
#pragma unroll
        for (int i = 0; i < 4; ++i) *(LAS u4*)(sm + U_OFF + 480 + 4 * CH_SZ + lane * 16 + i * 1024) = u5[i];
      }
      if (w == 4) arrive(L_CNT + 32);
      arrive(L_CNT + 44);
      reach(L_CNT + 44, 6);
      reach(L_CNT + 8, 4);
      STAMPL(16);
      const u4 rp = mkdesc(ws + WS_WO + ct * 82944, 82944);
#pragma unroll 1
      for (int i = (w & 1) * 1024; i < 81 * 1024; i += 2048) dma16c(rp, L_WO + i, lane * 16, i);
      WAITV(0);
      STAMPL(17);
    }
    arrive(L_CNT + 20);
    __builtin_amdgcn_s_setprio(0);
    return;
  }
  typedef const __attribute__((address_space(4))) unsigned* cptr;
  const unsigned dv0 = ((cptr)dv)[cm * 2 + (w >> 1)], dv1 = ((cptr)dv)[c5 >> 1];
  WAITV(0);
  __builtin_amdgcn_s_sleep(UDELAY);
  STAMP(3);
  {
    u4 uv[4];
#pragma unroll 1
    for (int spin = 0; spin < SPIN_MAX; ++spin) {
      unsigned hx[4], bad = 0;
#pragma unroll
      for (int i = 0; i < 4; ++i) {
        uv[i] = ld16<SC1>(ru, tid * 16, cm * 16384 + i * 4096);
        hx[i] = __builtin_amdgcn_raw_buffer_load_b32(ru, tid * 4, NB_ACT + cm * 4096 + i * 1024, SC1);
      }
      if (spin == 0) h_shift(sm + w * CH_SZ, lane);
#pragma unroll
      for (int i = 0; i < 4; ++i) bad |= chk(uv[i], (unsigned)tag) ^ hx[i];
      if (__all(bad == 0)) break;
      STAMP(20);
      asm volatile("s_sleep 1" ::: "memory");
    }
    STAMP(4);
    if (w == 3) arrive(L_CNT + 4);
#pragma unroll
    for (int i = 0; i < 4; ++i) *(LAS u4*)(sm + U_OFF + 480 + (tid & 3) * CH_SZ + (tid >> 2) * 16 + i * 1024) = uv[i];
  }
  arrive(L_CNT + 8);
  const int xd = 15 - r - 8 * g;
#pragma unroll 1
  for (int rnd = 0; rnd < (jb >= 8 ? 2 : 1); ++rnd) {
    const int dd0 = rnd ? 2 * w : 0, ch = rnd ? c5 : cm * 4 + w;
    const unsigned dval = dd0 == 0 ? ((rnd ? dv1 : dv0) >> ((ch & 1) * 16)) & 0xffffu : 0;
    lptr cb = sm + (rnd ? 4 : w) * CH_SZ;
    lptr aa[4];
#pragma unroll
    for (int i = 0; i < 4; ++i) { aa[i] = cb + (r & 1) * H_CP + 4 + 4 * (4 * g + (r >> 1)) + dd0 * 64 + i * 1024; pin(aa[i]); }
    lptr ba = cb + U_OFF + 16 * (31 + 2 * r - g) - 128 - dd0 * 64;
    pin(ba);
    u4 ad;
#pragma unroll
    for (int d = 0; d < 4; ++d) ad[d] = (xd >> 1) == d ? dval << ((xd & 1) * 16) : 0u;
    pin(ad);
    reach(rnd ? L_CNT + 32 : L_CNT + 8, rnd ? 2 : 4);
    STAMP(rnd ? 19 : 5);
    f4 acc[8];
    bf8 A[2][8], B[2][8];
    auto rd = [&](int dd, bf8 (&a)[8], bf8 (&b)[8]) {
#pragma unroll
      for (int i = 0; i < 8; ++i) {
        const LAS unsigned* p = (const LAS unsigned*)(aa[i >> 1] + dd * 64 + (i & 1) * 512);
        a[i] = asb(u4{p[0], p[1], p[2], p[3]});
        b[i] = *(const LAS bf8*)(ba + 128 - dd * 64 + i * 512);
      }
    };
    auto mm = [&](bf8 (&a)[8], bf8 (&b)[8]) {
#pragma unroll
      for (int i = 0; i < 8; ++i)
#pragma unroll
        for (int jj = 0; jj + i < 8; ++jj) acc[i + jj] = mfma(a[i], b[jj], acc[i + jj]);
#pragma unroll
      for (int i = 0; i < 24; ++i) { SGB(0x8, 1); SGB(0x100, 1); }
      SGB(0x8, 12);
      SB();
    };
    rd(0, A[0], B[0]);
#pragma unroll
    for (int n = 0; n < 8; ++n) acc[n] = mfma(asb(ad), B[0][n], f4{0.f, 0.f, 0.f, 0.f});
    SB();
#pragma unroll 1
    for (int it = rnd * 3; it < 4; ++it) {
      rd(1, A[1], B[1]);
      mm(A[0], B[0]);
      rd(2, A[0], B[0]);
      mm(A[1], B[1]);
#pragma unroll
      for (int i = 0; i < 4; ++i) aa[i] += 128;
      ba -= 128;
    }
    STAMP(6 + rnd);
    const unsigned yo = (((ch >> 6) * 16 + 2 * (g & 1) + (r >> 3)) * 2 + (g >> 1)) * 8192 + (ch & 63) * 128 + (r & 7) * 16;
    auto put = [&](f4 x, f4 y, int a) {
      auto s0 = __builtin_amdgcn_permlane16_swap(pk(x[0], x[1]), pk(y[0], y[1]), false, false);
      auto s1 = __builtin_amdgcn_permlane16_swap(pk(x[2], x[3]), pk(y[2], y[3]), false, false);
      st16<SC1>(u4{s0[0], s1[0], s0[1], s1[1]}, ry, yo + a * 65536);
    };
    if (rnd == 0) {
      reach(L_CNT + 16, 1);
#pragma unroll
      for (int a = 0; a < 4; ++a) put(acc[2 * a], acc[2 * a + 1], a);
    } else {
      LAS f4* mine = (LAS f4*)(sm + w * CH_SZ + lane * 16);
#pragma unroll
      for (int n = 0; n < 8; ++n) mine[n * 64] = acc[n];
      arrive(L_CNT + 16);
      reach(L_CNT + 16, 5);
      constexpr int CS = CH_SZ / 16;
      LAS f4* p = (LAS f4*)(sm + lane * 16) + w * 128;
      put((p[0] + p[CS]) + (p[2 * CS] + p[3 * CS]), (p[64] + p[CS + 64]) + (p[2 * CS + 64] + p[3 * CS + 64]), w);
    }
  }
  STAMP(8);
}

DEV void finish(lptr sm, lptr yt, char* ws, const u16* q, u16* out, int grp, int idx, int w4, int lane) {
  rsrc_t ry = mkrs(ws + WS_Y, NB_ACT), rq = mkrs(q, NB_ACT), rout = mkrs(out, 2 * NB_ACT);
  const int tile = idx + 8 * (w4 >> 1), orow = tile * 128 + 8 * (w4 & 1) + (lane >> 3);
  const unsigned qo = orow * 1152 + grp * 128 + (lane & 7) * 16, yo = ((grp * 16 + tile) * 2 + (w4 & 1)) * 8192 + lane * 16;
  u4 qv[8], yv[8];
#pragma unroll
  for (int i = 0; i < 8; ++i) qv[i] = ld16<0>(rq, qo, i * 16 * 1152);
  reach(L_CNT + 24, 1);
  f2 qf[8][4], yy[4][4];
#pragma unroll
  for (int i = 0; i < 16; ++i) { yy[i >> 2][i & 3] = f2{0.f, 0.f}; pin(yy[i >> 2][i & 3]); }
  asm volatile("s_waitcnt vmcnt(0)" : "+v"(qv[0]), "+v"(qv[1]), "+v"(qv[2]), "+v"(qv[3]), "+v"(qv[4]), "+v"(qv[5]),
               "+v"(qv[6]), "+v"(qv[7]) :: "memory");
  STAMP(26);
  __builtin_amdgcn_s_sleep(YDELAY);
#pragma unroll 1
  for (int spin = 0; spin < SPIN_MAX; ++spin) {
    unsigned mx = 0;
#pragma unroll
    for (int i = 0; i < 8; ++i) yv[i] = ld16<SC1>(ry, yo, i * 1024);
    SB();
#pragma unroll
    for (int i = 0; i < 32; ++i) {
      qf[i >> 2][i & 3] = f2{lo16(qv[i >> 2][i & 3]), hi16(qv[i >> 2][i & 3])};
      pin(qf[i >> 2][i & 3]);
    }
#pragma unroll
    for (int i = 0; i < 8; ++i) mx = max3u(max3u(mx, yv[i][0], yv[i][1]), yv[i][2], yv[i][3]);
    if (__all(mx != NOTYET)) break;
    STAMP(27 + (spin < 3 ? spin : 3));
    asm volatile("s_sleep 2" ::: "memory");
  }
  STAMP(12);
  unsigned oo = orow * 2304 + 1152 + grp * 128 + (lane & 7) * 16;
#pragma unroll
  for (int i = 0; i < 8; ++i) *(LAS u4*)(yt + lane * 16 + i * 1040) = yv[i];
  const unsigned p = (unsigned)(size_t)yt + (lane & 7) * 1040 + (lane >> 3) * 2;
#pragma unroll
  for (int t = 0; t < 2; ++t) {
#pragma unroll
    for (int i = 0; i < 16; ++i) {
      float a = yy[i >> 2][i & 3][0], b = yy[i >> 2][i & 3][1];
      asm volatile("ds_read_u16_d16_hi %0, %2 offset:%3\n\tds_read_u16_d16_hi %1, %2 offset:%4"
                   : "+v"(a), "+v"(b) : "v"(p), "i"(t * 64 + (i >> 2) * 16 + (i & 3) * 256), "i"(t * 64 + (i >> 2) * 16 + (i & 3) * 256 + 128)
                   : "memory");
      yy[i >> 2][i & 3] = f2{a, b};
    }
#pragma unroll
    for (int i = 0; i < 4; ++i) {
      if (i == 0) asm volatile("s_waitcnt lgkmcnt(14)" : "+v"(yy[0][0]), "+v"(yy[0][1]), "+v"(yy[0][2]), "+v"(yy[0][3]), "+v"(yy[1][0]), "+v"(yy[1][1]), "+v"(yy[1][2]), "+v"(yy[1][3]));
      if (i == 2) asm volatile("s_waitcnt lgkmcnt(0)" : "+v"(yy[2][0]), "+v"(yy[2][1]), "+v"(yy[2][2]), "+v"(yy[2][3]), "+v"(yy[3][0]), "+v"(yy[3][1]), "+v"(yy[3][2]), "+v"(yy[3][3]));
      u4 o;
#pragma unroll
      for (int d = 0; d < 4; ++d) { const f2 m = yy[i][d] * qf[t * 4 + i][d]; o[d] = pk(m[0], m[1]); }
      st16<OA1>(o, rout, oo + (t * 64 + i * 16) * 2304);
      SB();
    }
  }
}

DEV void finish8(lptr yt, char* ws, const u16* q, u16* out, int un, int w, int lane) {
  rsrc_t ry = mkrs(ws + WS_Y, NB_ACT), rq = mkrs(q, NB_ACT), rout = mkrs(out, 2 * NB_ACT);
  const int orow = (un >> 2) * 128 + 64 * (un & 1) + 8 * ((un >> 1) & 1) + 16 * w + (lane >> 3);
  u4 qv = ld16<0>(rq, orow * 1152 + 1024 + (lane & 7) * 16, 0), yv;
  reach(L_CNT + 24, 1);
  asm volatile("s_waitcnt vmcnt(0)" : "+v"(qv) :: "memory");
#pragma unroll 1
  for (int spin = 0; spin < SPIN_MAX; ++spin) {
    yv = ld16<SC1>(ry, lane * 128 + ((un & 1) * 4 + w) * 16, (256 + (un >> 1)) * 8192);
    if (__all(max4(yv, 0) != NOTYET)) break;
    asm volatile("s_sleep 1" ::: "memory");
  }
  STAMP(12);
  *(LAS u4*)(yt + (lane >> 3) * 144 + (lane & 7) * 16) = yv;
  WAITL0();
  const LAS u16* p = (const LAS u16*)(yt + (lane & 7) * 144 + (lane >> 3) * 2);
  u4 o;
#pragma unroll
  for (int d = 0; d < 4; ++d) o[d] = pk(lo16(p[d * 16]) * lo16(qv[d]), lo16(p[d * 16 + 8]) * hi16(qv[d]));
  st16<OA1>(o, rout, orow * 2304 + 1152 + 1024 + (lane & 7) * 16);
}

DEV void out_proj(lptr sm, char* ws, u16* out, int bid, int lw, int lane, const Proj& pc) {
  lptr wa[2] = {(lptr)(size_t)pc.wa, (lptr)(size_t)(pc.wa + 9 * 4608)};
  lptr wb[2] = {(lptr)(size_t)pc.wb, (lptr)(size_t)(pc.wb + 9 * 4608)};
  pin(wa[1]); pin(wb[1]);
  rsrc_t ro = mkrs(ws + WS_O, NB_ACT + 65536);
  const unsigned ob = pc.ob, oo[3] = {pc.oo0, pc.oo1, pc.oo2};
  lptr ot = (lptr)(size_t)pc.ot, ow = (lptr)(size_t)pc.ow;
  reach(L_CNT + 20, 4);
  f4 acc[5];
  const u64 tag = *(LAS u64*)(sm + L_TAG);
#pragma unroll 1
  for (int att = 0; att < 4096; ++att) {
    constexpr int PRE = 7, AHEAD = 9;
    bf8 wf[PRE][5];
    auto rdw = [&](int i) {
#pragma unroll
      for (int n = 0; n < 5; ++n)
        wf[i % PRE][n] = *(const LAS bf8*)((n < 4 ? wa[i / 9] + n * 1024 : wb[i / 9]) + (i % 9) * 4608);
    };
#pragma unroll
    for (int i = 0; i < PRE - 1; ++i) rdw(i);
    SB();
    if (lw == 0 && att == 0 && bid >= 128) {
      wait_flags(ws, pc.fl, tag);
      arrive(L_CNT + 28);
    }
    reach(L_CNT + 28, 1);
    STAMPL(att ? 31 : 9);
    u4 of[18];
    auto ldo = [&](int i) { of[i] = ld16<SC1>(ro, ob, i * 4096); };
#pragma unroll
    for (int i = 0; i < AHEAD; ++i) { ldo(i); SB(); }
#pragma unroll
    for (int n = 0; n < 5; ++n) acc[n] = f4{0.f, 0.f, 0.f, 0.f};
    unsigned sum = 2 * (unsigned)tag, h0, h1;
#pragma unroll
    for (int i = 0; i < 18; ++i) {
      if (i + PRE - 1 < 18) rdw(i + PRE - 1);
#pragma unroll
      for (int n = 0; n < 5; ++n) acc[n] = mfma(wf[i % PRE][n], asb(of[i]), acc[n]);
      sum = chk(of[i], sum);
      if (i + AHEAD < 18) ldo(i + AHEAD);
      if (i == 9) h0 = __builtin_amdgcn_raw_buffer_load_b32(ro, pc.oh, 0, SC1);
      if (i == 10) h1 = __builtin_amdgcn_raw_buffer_load_b32(ro, pc.oh + 256, 0, SC1);
      SGB(0x8, 1); SGB(0x20, 1); SGB(0x100, 1);
#pragma unroll
      for (int n = 1; n < 5; ++n) { SGB(0x8, 1); SGB(0x100, 1); SGB(0x2, 1); }
      SB();
    }
    if (__all(rowsum(sum - h0 - h1) == 0)) break;
    asm volatile("" ::: "memory");
  }
  STAMPL(10);
#pragma unroll
  for (int n = 0; n < 5; ++n)
    if (n < 4 || lane < 32) *(LAS u2*)(ow + n * 32) = u2{pk(acc[n][0], acc[n][1]), pk(acc[n][2], acc[n][3])};
  WAITL0();
  rsrc_t rout = mkrs(out, 2 * NB_ACT);
#pragma unroll
  for (int i = 0; i < 3; ++i) st16<OA2>(*(const LAS u4*)(ot + i * 1024), rout, oo[i]);
  STAMPL(15);
}

__global__ __launch_bounds__(512) __attribute__((amdgpu_waves_per_eu(2, 2))) void fused(
    const u16*, const u16*, const u16*, const u16*, const u16*, const u16*, const u16*, const u16*, const u16*, u16*,
    char*) {
  typedef u64 u64x8 __attribute__((ext_vector_type(8)));
  typedef u64 u64x2 __attribute__((ext_vector_type(2)));
  u64x8 ka;
  u64x2 kb;
  u64 kc;
  asm volatile("s_load_dwordx16 %0, %3, 0x0\n\ts_load_dwordx4 %1, %3, 0x40\n\ts_load_dwordx2 %2, %3, 0x50"
               : "=&s"(ka), "=&s"(kb), "=&s"(kc) : "s"(__builtin_amdgcn_kernarg_segment_ptr()));
  extern __shared__ __attribute__((aligned(16))) char smem[];
  lptr sm = (lptr)smem;
  const int tid = threadIdx.x, lane = tid & 63, w = __builtin_amdgcn_readfirstlane(tid >> 6);
  const int bid = blockIdx.x;
  u64 rt0, ct0;
  asm volatile("s_memrealtime %0\n\ts_memtime %1" : "=s"(rt0), "=s"(ct0));
  if (tid >= 256 && tid < 288) *(LAS u64*)(size_t)(L_PF - 256 + (tid - 256) * 8) = 0;
  if (bid < 128 && (bid >> 3) + 16 * tid < CODE_LINES) {
    u64 pc;
    asm volatile("s_getpc_b64 %0" : "=s"(pc));
    asm volatile("s_mov_b32 m0, %0\n\ts_nop 0\n\tglobal_load_lds_dword %1, %2"
                 :: "s"(L_PF), "v"(((bid >> 3) + 16 * tid) * 128), "s"(pc & ~127ull) : "memory");
  }
  const int pgrp = w < 4 ? bid & 7 : 8, pun = w < 4 ? ((bid >> 3) - 16) * 4 + w : bid - 128 - ((bid >> 3) - 16) * 7 + (bid & 7) * 7;
  unsigned o0 = (pun * 32 + (lane >> 3)) * 1152 + pgrp * 128 + (((lane & 7) ^ ((lane >> 4) & 1)) << 4);
  const int t27 = lane >= 27, cc = lane - 27 * t27, part = (cc >= 9) + (cc >= 18);
  unsigned vw = t27 * (4 * 3456) + part * (1152 - 144) + cc * 16 + ((bid & 1) * 4 + ((bid >> 3) & 3)) * 144;
  if (w == 7 && bid >= 128) {
    const int jb = (bid >> 3) - 16, kr = (lane * 4370) >> 16, c15 = lane - kr * 15, p5 = (c15 * 52) >> 8;
    vw = lane < 60 ? (jb * 4 + kr) * 3456 + p5 * (1152 - 640) + ((bid & 1) * 4 + c15) * 128 : OOB;
    o0 = lane < 33 ? (((bid & 7) >> 1) * 512 - 16 + jb * 33 + lane) * 1152 : OOB;
  }
  pin(o0); pin(vw);
  if (bid >= 128 && tid < 12) *(LAS unsigned*)(sm + L_CNT + tid * 4) = 0;
  asm volatile("s_waitcnt lgkmcnt(0)" : "+s"(ka), "+s"(kb), "+s"(kc) :: "memory");
  const u16 *x = (const u16*)ka[0], *wqkv = (const u16*)ka[1], *wo = (const u16*)ka[2], *q = (const u16*)ka[3];
  const u16 *hk = (const u16*)ka[7], *dv = (const u16*)kb[0];
  u16* out = (u16*)kb[1];
  char* ws = (char*)kc;
  if (__builtin_expect(bid >= 128, 1)) {
    if (__builtin_expect(w < 4 || (w == 4 && bid < 192), 1)) {
      const u4 rk = mkdesc((const void*)ka[4], NB_ACT), rv = mkdesc((const void*)ka[5], NB_ACT);
      const unsigned tb = L_WO + w * 9216;
      dma16(rk, tb, o0 - 8 * 1152, 0);
#pragma unroll
      for (int i = 0; i < 4; ++i) {
        dma16(rk, tb + 1024 + i * 1024, o0 ^ (((i + 1) & 3) << 5), i * 8 * 1152);
        dma16(rv, tb + 5120 + i * 1024, o0 ^ (i << 5), i * 8 * 1152);
        if (i == 0) dma16m(mkdesc((const void*)ka[6], 4608), L_OT + w * 512, lane * 16, pgrp * 512, 0xffffffffull);
      }
    }
    if (w == 7) {
#pragma unroll
      for (int st = PFE; st < PFS0; ++st) {
        touch(mkdesc(wqkv, NB_W), vw, st * 64 * 3456);
        touch(mkdesc(x, NB_ACT), o0, st * 128);
      }
    }
    __builtin_amdgcn_s_barrier();
    asm volatile("" ::: "memory");
  }
  Proj pc;
  if (__builtin_expect(bid < 128, 1)) role_a(sm, ws, x, wqkv, bid, w, lane, pc, vw);
  else {
    role_b(sm, ws, x, wqkv, wo, q, hk, dv, bid, w, lane, vw, o0);
    if (w >= 4) proj_consts(pc, bid, w & 3, lane);
    else asm volatile("" : "=v"(pc.wa), "=v"(pc.wb), "=v"(pc.ob), "=v"(pc.oh), "=v"(pc.oo0), "=v"(pc.oo1), "=v"(pc.oo2),
                      "=v"(pc.ot), "=v"(pc.ow), "=v"(pc.fl));
  }
  if (w >= 4) {
    out_proj(sm, ws, out, bid, w & 3, lane, pc);
  } else if (bid >= 128) {
    if (bid >= 192) finish8(sm + w * CH_SZ + U_OFF, ws, q, out, bid - 192, w, lane);
    else finish(sm, sm + w * CH_SZ, ws, q, out, bid & 7, (bid >> 3) - 16, w, lane);
    STAMP(13);
  } else {
    if (bid == 64 && w == 1) {
      const u64 tag = *(LAS u64*)(sm + L_TAG);
      GAS u64* p = (GAS u64*)(ws + WS_FLAG + lane * 128);
#pragma unroll 1
      for (int spin = 0; spin < SPIN_MAX; ++spin) {
        const u64 a = __hip_atomic_load(p, RLX_AGENT), b = __hip_atomic_load(p + 1024, RLX_AGENT);
        const u64 c = __hip_atomic_load(p + 2048, RLX_AGENT), d = __hip_atomic_load(p + 3072, RLX_AGENT);
        if (__all(a == tag && b == tag && c == tag && d == tag)) break;
        __builtin_amdgcn_s_sleep(PSLEEP);
      }
      if (lane == 0) __hip_atomic_store((GAS u64*)ws, tag, RLX_AGENT);
    }
  }
  bar();
  STAMP(11);
  WAITL0();
  if (tid > 0 && tid < 32) ((GAS u64*)(ws + WS_DBG))[blockIdx.x * 32 + tid] = *(LAS u64*)(size_t)(L_PF - 256 + tid * 8);
  if (threadIdx.x == 0) {
    ((GAS u64*)(ws + WS_DBG + 65536))[blockIdx.x * 2] = rt0; ((GAS u64*)(ws + WS_DBG))[blockIdx.x * 32] = ct0;
    ((GAS u64*)(ws + WS_DBG + 65536))[blockIdx.x * 2 + 1] = __builtin_amdgcn_s_memrealtime();
  }
}

__global__ void k_spin(const char* ws){
  const u64* st=(const u64*)(ws+WS_DBG); const u64* rt=(const u64*)(ws+WS_DBG+65536);
  u64 sum=0; for(int b=0;b<128;b++) sum+=st[b*32+16]-st[b*32+0]; u64 d=sum*10/(128-0);
  if(d>1000000) d=1000000;
  u64 s=__builtin_amdgcn_s_memrealtime();
  while(__builtin_amdgcn_s_memrealtime()-s<d) __builtin_amdgcn_s_sleep(8);
}
extern "C" void kernel_launch(void* const* d_in, const int* in_sizes, int n_in, void* d_out, int out_size, void* d_ws,
                              size_t ws_size, hipStream_t stream) {
  (void)hipFuncSetAttribute((const void*)fused, hipFuncAttributeMaxDynamicSharedMemorySize, LDS_BYTES);
  fused<<<256, 512, LDS_BYTES, stream>>>((const u16*)d_in[0], (const u16*)d_in[1], (const u16*)d_in[2], (const u16*)d_in[3],
                                         (const u16*)d_in[4], (const u16*)d_in[5], (const u16*)d_in[6], (const u16*)d_in[7],
                                         (const u16*)d_in[8], (u16*)d_out, (char*)d_ws);
  k_spin<<<1,1,0,stream>>>((const char*)d_ws);
}
```

```cpp
#include <hip/hip_runtime.h>

typedef unsigned short u16;
typedef unsigned long long u64;
typedef short s4 __attribute__((ext_vector_type(4)));
typedef __bf16 bf2 __attribute__((ext_vector_type(2)));
typedef __bf16 bf8 __attribute__((ext_vector_type(8)));
typedef float f2 __attribute__((ext_vector_type(2)));
typedef float f4 __attribute__((ext_vector_type(4)));
typedef unsigned u4 __attribute__((ext_vector_type(4)));
typedef unsigned u2 __attribute__((ext_vector_type(2)));
typedef __amdgpu_buffer_rsrc_t rsrc_t;
#define LAS __attribute__((address_space(3)))
#define GAS __attribute__((address_space(1)))
typedef LAS char* lptr;
#define RLX_AGENT __ATOMIC_RELAXED, __HIP_MEMORY_SCOPE_AGENT
#define DEV __device__ __forceinline__

constexpr int NB_ACT = 2048 * 576 * 2;
constexpr int NB_W = 576 * 1728 * 2;
constexpr int NB_WO = 576 * 576 * 2;
constexpr unsigned OOB = 0x80000000u;
constexpr unsigned NOTYET = 0xffffffffu;
constexpr int SC1 = 16;
constexpr int CODE_LINES = 266;
#define ROTK 0
#define PFS 12
#define TDELAY 0
#define YSH 0
#define PF0 2
#define HDELAY 6
#define PFA 6
#define PFB 8
#define PDELAY 0
#define EARLYHINT 0
#define HSLOT 2
#define PSLOT 0
#define PF1 4
#define LPRIO 0
#define HSWAP 1
#define UDELAY 1
#define YDELAY 0
#define OA1 2
#define OA2 0
#define PFS0 2
#define PFE 0
#define GPRE 7
#define GAH 9
#define PFBN 4
#define GMS 3
#define XSH 1
#define OSH 2
#define PSLEEP 2
constexpr int SPIN_MAX = 1 << 19;

constexpr int WS_FLAG = 4096;
constexpr int F_A = 0, F_P = 128, F_Y = 256, F_Q = 512, F_W = 640, F_G = 896, F_L = 960;
constexpr int WS_U = 1 << 20;
constexpr int WS_Y = WS_U + (3 << 20);
constexpr int WS_O = WS_Y + (3 << 20);
constexpr int WS_WO = WS_O + (3 << 20);
constexpr int WS_DBG = 16 << 20;

constexpr int LDS_BYTES = 163840;
constexpr int XB = 144 * 128;
constexpr int SLOT = XB + 64 * 432;
constexpr int PS = 1160, HS = 32640;
constexpr int L_WO = 65152;
constexpr int L_OT = L_WO + 83200;
constexpr int H_CP = 4160, U_OFF = 8448, CH_SZ = 13024;
constexpr int L_TAG = L_OT + 10240;
constexpr int L_CNT = L_TAG + 16;
constexpr int L_YA = L_TAG + 128;
constexpr int L_PF = LDS_BYTES - 256;

#define STAMPW(i, wv) do {} while (0)
#define STAMP(i) STAMPW(i, 0)
#define STAMPL(i) STAMPW(i, 4)

DEV rsrc_t mkrs(const void* p, int bytes) { return __builtin_amdgcn_make_buffer_rsrc((void*)p, 0, bytes, 0x00020000); }
DEV u4 mkdesc(const void* p, int bytes) {
  u64 a = (u64)p;
  return u4{(unsigned)a, (unsigned)(a >> 32) & 0xffffu, (unsigned)bytes, 0x00020000u};
}
DEV void dma16(u4 r, unsigned lds, unsigned voff, unsigned soff) {
  asm volatile("s_mov_b32 m0, %0\n\ts_nop 0\n\tbuffer_load_dwordx4 %1, %2, %3 offen lds"
               :: "s"(lds), "v"(voff), "s"(r), "s"(soff) : "memory");
}
DEV void dma16c(u4 r, unsigned lds, unsigned voff, unsigned soff) {
  asm volatile("s_mov_b32 m0, %0\n\ts_nop 0\n\tbuffer_load_dwordx4 %1, %2, %3 offen sc1 lds"
               :: "s"(lds), "v"(voff), "s"(r), "s"(soff) : "memory");
}
DEV void dma16m(u4 r, unsigned lds, unsigned voff, unsigned soff, u64 mask) {
  asm volatile("s_mov_b32 m0, %0\n\ts_mov_b64 exec, %4\n\tbuffer_load_dwordx4 %1, %2, %3 offen lds\n\ts_mov_b64 exec, -1"
               :: "s"(lds), "v"(voff), "s"(r), "s"(soff), "s"(mask) : "memory");
}
template <int AUX> DEV void st16(u4 v, rsrc_t r, unsigned off) { __builtin_amdgcn_raw_buffer_store_b128(v, r, off, 0, AUX); }
template <int AUX> DEV u4 ld16(rsrc_t r, unsigned off, unsigned soff) { return __builtin_amdgcn_raw_buffer_load_b128(r, off, soff, AUX); }
DEV u4 ld16c(u4 r, unsigned voff, unsigned soff) {
  u4 v;
  asm volatile("buffer_load_dwordx4 %0, %1, %2, %3 offen sc1" : "=v"(v) : "v"(voff), "s"(r), "s"(soff) : "memory");
  return v;
}
DEV void touch(u4 r, unsigned voff, unsigned soff) {
  asm volatile("s_mov_b32 m0, %0\n\ts_nop 0\n\tbuffer_load_dword %1, %2, %3 offen lds"
               :: "s"(L_PF), "v"(voff), "s"(r), "s"(soff) : "memory");
}
DEV s4 trread(lptr p) { return __builtin_amdgcn_ds_read_tr16_b64_v4i16((LAS s4*)p); }
DEV bf8 cat8(s4 a, s4 b) { return __builtin_bit_cast(bf8, __builtin_shufflevector(a, b, 0, 1, 2, 3, 4, 5, 6, 7)); }
DEV bf8 asb(u4 v) { return __builtin_bit_cast(bf8, v); }
DEV f4 mfma(bf8 a, bf8 b, f4 c) { return __builtin_amdgcn_mfma_f32_16x16x32_bf16(a, b, c, 0, 0, 0); }
DEV unsigned pk(float lo, float hi) { f2 v = {lo, hi}; return __builtin_bit_cast(unsigned, __builtin_convertvector(v, bf2)); }
DEV float asf(unsigned u) { return __builtin_bit_cast(float, u); }
DEV unsigned asu(float f) { return __builtin_bit_cast(unsigned, f); }
DEV float lo16(unsigned x) { return asf(x << 16); }
DEV float hi16(unsigned x) { return asf(x & 0xffff0000u); }
DEV float dot2(unsigned a, unsigned b, float c) {
  return __builtin_amdgcn_fdot2_f32_bf16(__builtin_bit_cast(bf2, a), __builtin_bit_cast(bf2, b), c, false);
}
DEV float dot2z(unsigned a, unsigned b) {
  float d;
  asm("v_dot2_f32_bf16 %0, %1, %2, 0" : "=v"(d) : "v"(a), "v"(b));
  return d;
}
DEV float dot2a(unsigned a, unsigned b, float c) {
  float d;
  asm("v_dot2_f32_bf16 %0, %1, %2, %3" : "=v"(d) : "v"(a), "v"(b), "v"(c));
  return d;
}
DEV unsigned alignb(unsigned hi, unsigned lo) { return __builtin_amdgcn_alignbit(hi, lo, 16); }
DEV unsigned umax(unsigned a, unsigned b) { return a > b ? a : b; }
DEV unsigned max4(u4 v, unsigned m) { return umax(umax(umax(v[0], v[1]), m), umax(v[2], v[3])); }
DEV unsigned max3u(unsigned a, unsigned b, unsigned c) { return umax(umax(a, b), c); }
DEV unsigned max12(u4 a, u4 b, u4 c) {
  return umax(max3u(max3u(max3u(a[0], a[1], a[2]), a[3], b[0]), b[1], b[2]), max3u(max3u(b[3], c[0], c[1]), c[2], c[3]));
}
DEV unsigned chk(u4 v, unsigned key) { return (v[0] + v[1] + v[2]) + (v[3] + key); }
DEV unsigned rowsum(unsigned x) {
  auto a = __builtin_amdgcn_permlane16_swap(x, x, false, false);
  const unsigned y = a[0] + a[1];
  auto b = __builtin_amdgcn_permlane32_swap(y, y, false, false);
  return b[0] + b[1];
}
DEV float fmax2(float a, float b) { return __builtin_elementwise_maximum(a, b); }
#define WAITV(n) asm volatile("s_waitcnt vmcnt(" #n ")" ::: "memory")
#define WAITL0() asm volatile("s_waitcnt lgkmcnt(0)" ::: "memory")
#define SB() __builtin_amdgcn_sched_barrier(0)
#define SGB(m, n) __builtin_amdgcn_sched_group_barrier(m, n, 0)
DEV void bar() { WAITL0(); __builtin_amdgcn_s_barrier(); asm volatile("" ::: "memory"); }
template <class T> DEV void pin(T& x) { asm volatile("" : "+v"(x)); }

DEV u64 get_tag(char* ws) {
  u64 e;
  asm volatile("s_load_dwordx2 %0, %1, 0x0\n\ts_waitcnt lgkmcnt(0)" : "=s"(e) : "s"(ws) : "memory");
  return e * 6364136223846793005ull + 1442695040888963407ull;
}
DEV void post_flag(char* ws, int idx, u64 tag) { __hip_atomic_store((GAS u64*)(ws + WS_FLAG + idx * 128), tag, RLX_AGENT); }
DEV void wait_flags(char* ws, int idx, u64 tag) {
  GAS u64* p = (GAS u64*)(ws + WS_FLAG + idx * 128);
#pragma unroll 1
  for (int spin = 0; spin < SPIN_MAX; ++spin) {
    if (__all(__hip_atomic_load(p, RLX_AGENT) == tag)) break;
    __builtin_amdgcn_s_sleep(PSLEEP);
  }
  __builtin_amdgcn_fence(__ATOMIC_ACQUIRE, "wavefront");
}

DEV void arrive(unsigned word) {
  unsigned one = 1;
  asm volatile("s_waitcnt lgkmcnt(0)\n\ts_mov_b64 exec, 1\n\tds_add_u32 %0, %1\n\ts_mov_b64 exec, -1"
               :: "v"(word), "v"(one) : "memory");
}
DEV void reach(unsigned word, unsigned n) {
#pragma unroll 1
  for (int spin = 0; spin < (1 << 24); ++spin) {
    unsigned v;
    asm volatile("ds_read_b32 %0, %1\n\ts_waitcnt lgkmcnt(0)" : "=v"(v) : "v"(word) : "memory");
    if ((unsigned)__builtin_amdgcn_readfirstlane(v) >= n) break;
    __builtin_amdgcn_s_sleep(1);
  }
}

DEV u4 ld2(lptr p) {
  const u2 a = *(const LAS u2*)p, b = *(const LAS u2*)(p + HS);
  return u4{a[0], a[1], b[0], b[1]};
}
DEV u64 lds_tag(lptr sm) {
  reach(L_CNT, 1);
  return *(LAS u64*)(sm + L_TAG);
}
DEV void h_dma(const u16* hk, int c, int ch, int lane) {
  const u4 rh = mkdesc(hk, NB_ACT);
#pragma unroll
  for (int i = 0; i < 4; ++i) dma16(rh, c * CH_SZ + H_CP + 32 + i * 1024, lane * 16, ch * 4096 + i * 1024);
}
DEV void h_shift(lptr cb, int lane) {
  if (lane < 34) *(LAS u4*)(cb + (lane < 30 ? U_OFF + lane * 16 : ((lane >> 1) & 1) * H_CP + (lane & 1) * 16)) = u4{0, 0, 0, 0};
  asm volatile("" ::: "memory");
  lptr hb = cb + 32 + lane * 16;
  u4 ha[4];
  unsigned hp[4];
#pragma unroll
  for (int i = 0; i < 4; ++i) { ha[i] = *(LAS u4*)(hb + H_CP + i * 1024); hp[i] = *(LAS unsigned*)(hb + H_CP - 4 + i * 1024); }
#pragma unroll
  for (int i = 0; i < 4; ++i)
    *(LAS u4*)(hb + i * 1024) =
        u4{alignb(ha[i][0], hp[i]), alignb(ha[i][1], ha[i][0]), alignb(ha[i][2], ha[i][1]), alignb(ha[i][3], ha[i][2])};
}
DEV void att_consts(lptr sm, int lane, u4 (&tc)[9], int first) {
  const LAS u4* tb = (const LAS u4*)(sm + L_OT) + lane;
#pragma unroll
  for (int i = 0; i < 9; ++i) tc[i] = tb[(i < 4 ? i : i == 4 ? 4 + first : i + 1) * 64];
}

struct Proj { unsigned wa, wb, ob, oh, oo0, oo1, oo2, ot, ow, fl; };
DEV void proj_consts(Proj& p, int bid, int lw, int lane) {
  const int rt = ((bid + OSH) & 7) * 4 + ((bid >> 6) & 1) * 2 + (bid >> 7), ct = (bid >> 3) & 7;
  const int r = lane & 15, g = lane >> 4;
  p.wa = L_WO + lane * 16;
  p.wb = L_WO + 4096 + (g * 8 + (r & 7)) * 16;
  p.ob = rt * 72 * 1024 + g * 1024 + lw * 256 + r * 16;
  p.oh = NB_ACT + (rt * 4 + lw) * 512 + lane * 4;
  pin(p.oh);
  unsigned oo[3];
#pragma unroll
  for (int i = 0; i < 3; ++i) {
    const int it = lane + 64 * i, row = (it * 57) >> 9, c = it - row * 9;
    oo[i] = (i == 2 && lane >= 16) ? OOB : (rt * 64 + 32 * (lw >> 1) + (lw & 1)) * 2304 + __umul24(row, 4608) + ct * 144 + c * 16;
  }
  p.oo0 = oo[0]; p.oo1 = oo[1]; p.oo2 = oo[2];
  p.ot = L_OT + lw * 2304 + lane * 16;
  p.ow = L_OT + lw * 2304 + r * 144 + g * 8;
  p.fl = ((rt & 1) ? F_A : F_L) + (rt >> 1) * 8 + (lane & 7);
  pin(p.wa); pin(p.wb); pin(p.ob); pin(p.oo0); pin(p.oo1); pin(p.oo2); pin(p.ot); pin(p.ow); pin(p.fl);
}

template <int HI>
DEV void g1_compute(lptr sm, int n0, int lane, u4 (&tc)[9], int first) {
  const int r = lane & 15, g = lane >> 4;
  lptr xa0 = sm + r * 128 + ((g ^ ((r >> 1) & 7)) << 4);
  lptr xa1 = (lptr)((size_t)xa0 ^ 64);
  lptr wa = sm + XB + (((g >> 1) << 4) + ((g & 1) << 3) + ((r >> 2) << 1)) * 432 + (lane & 3) * 8 + n0 * 32;
#define USED(m, n) (HI ? ((n) > 0 || (m) >= 5) : ((n) < 3 || (m) < 5))
  f4 acc[9][4];
#pragma unroll
  for (int m = 0; m < 9; ++m)
#pragma unroll
    for (int n = 0; n < 4; ++n) {
      acc[m][n] = f4{0.f, 0.f, 0.f, 0.f};
      if (USED(m, n)) pin(acc[m][n]);
    }
  bf8 xf[9], wf[2][4];
  auto mm = [&](bf8 (&wv)[4], int m) {
#pragma unroll
    for (int n = 0; n < 4; ++n)
      if (USED(m, n)) acc[m][n] = mfma(wv[n], xf[m], acc[m][n]);
  };
  auto rdx = [&](lptr xp, int m) { xf[m] = *(const LAS bf8*)(xp + m * 2048); };
  auto rdw = [&](lptr wp, bf8 (&wv)[4], int n) { wv[n] = cat8(trread(wp + n * 32), trread(wp + 432 + n * 32)); };
  auto mix = [&]() {
#pragma unroll
    for (int i = 0; i < 4; ++i) { SGB(0x8, 1); SGB(0x100, 1); }
    SB();
  };
  auto step = [&](bf8 (&wc)[4], bf8 (&wn)[4], lptr xp, lptr wp, int m0) {
#pragma unroll
    for (int m = m0; m < 9; ++m) {
      mm(wc, m);
      if (m - m0 < 4) rdw(wp, wn, m - m0);
      if (m0 == 0) { if (m > 0) rdx(xp, m - 1); }
      else if (m - m0 < 4) rdx(xp, m - m0);
      else { rdx(xp, 2 * (m - m0) - 4); if (2 * (m - m0) - 3 < m) rdx(xp, 2 * (m - m0) - 3); }
      mix();
    }
#pragma unroll
    for (int m = (m0 == 0 ? 8 : 2 * (9 - m0) - 4); m < 9; ++m) rdx(xp, m);
    SB();
  };
  constexpr int MS = GMS;
  bar();
#pragma unroll
  for (int n = 0; n < 4; ++n) rdw(wa, wf[0], n);
#pragma unroll
  for (int m = 0; m < 9; ++m) rdx(xa0, m);
  SB();
  int so = 0;
#pragma unroll 1
  for (int k = 0; k < 8; ++k) {
    step(wf[0], wf[1], xa1 + so, wa + so + 32 * 432, 0);
    so = so == 2 * SLOT ? 0 : so + SLOT;
#pragma unroll
    for (int m = 0; m < MS; ++m) mm(wf[1], m);
    SB();
    bar();
    step(wf[1], wf[0], xa0 + so, wa + so, MS);
  }
  xa1 += 2 * SLOT; wa += 2 * SLOT + 32 * 432;
  pin(xa1); pin(wa);
  STAMP(1);
  lptr d = sm + (2 * n0 + (g >> 1)) * PS + r * 8 + (g & 1) * HS;
  bf8 xg[9];
#pragma unroll
  for (int m = 0; m < 11; ++m) {
    if (m < 9) mm(wf[0], m);
    if (m < 2) { rdw(wa, wf[1], 2 * m); rdw(wa, wf[1], 2 * m + 1); }
    if (m < 9) xg[m] = *(const LAS bf8*)(xa1 + m * 2048);
    if (m >= 2) {
#pragma unroll
      for (int n = 0; n < 4; ++n)
        if (USED(m - 2, n)) acc[m - 2][n] = mfma(wf[1][n], xg[m - 2], acc[m - 2][n]);
    }
    if (m == 7) att_consts(sm, lane, tc, first);
    if (m == 8) arrive(L_CNT + 8);
#pragma unroll
    for (int o = m - 3; o < (m == 10 ? 9 : m - 2); ++o) {
      if (o >= 0) {
#pragma unroll
        for (int n = 0; n < 4; ++n)
          if (USED(o, n))
            *(LAS u2*)(d + n * 2 * PS + o * 128) = u2{pk(acc[o][n][0], acc[o][n][1]), pk(acc[o][n][2], acc[o][n][3])};
      }
    }
    if (m < 3) {
#pragma unroll
      for (int i = 0; i < 4; ++i) { SGB(0x8, 1); SGB(0x100, 2); }
    } else {
      SGB(0x8, 1); SGB(0x100, 1);
#pragma unroll
      for (int i = 0; i < 4; ++i) { SGB(0x8, 1); SGB(0x2, 2); SGB(0x8, 1); SGB(0x200, 1); }
    }
    SB();
  }
}

DEV void role_a(lptr sm, char* ws, const u16* x, const u16* wqkv, int bid, int w, int lane, Proj& pc, unsigned vw) {
  const int xcd = bid & 7, j = bid >> 3;
  const int rt = (xcd >> 1) * 4 + (j >> 2), hq = (xcd & 1) * 4 + (j & 3);
  const u4 rp = mkdesc(ws + WS_WO + (j & 7) * 82944, 82944);
  u64 tag;
  u4 tc[9], wr[21];
  const int aw = w ^ 4;
  const int first = rt + (aw >> 1) == 0;
  if (__builtin_expect(w >= 4, 1)) {
    const int lw = w - 4;
    const u4 rx = mkdesc(x, NB_ACT), rw = mkdesc(wqkv, NB_W);
    auto wpiece = [&](unsigned d, int kt, int i) {
      dma16m(rw, d + XB + (lw * 8 + i) * 864, vw, (kt * 64 + lw * 16 + 8 * (i >> 2) + (i & 3)) * 3456, (1ull << 54) - 1);
    };
    auto tailp = [&](unsigned d, int kt) {
#pragma unroll
      for (int i = 2; i < 8; ++i) wpiece(d, kt, i);
    };
    STAMPL(16);
    tailp(0, 0);
    SB();
    unsigned vx[6];
#pragma unroll
    for (int i = 0; i < 6; ++i) {
      const int xr = lw * 36 + i * 6 + (lane >> 3);
      vx[i] = __umul24(rt * 128 + xr, 1152) + (((lane & 7) ^ ((xr >> 1) & 7)) << 4) - 16 * 1152;
    }
    auto headp = [&](unsigned d, int kt) {
#pragma unroll
      for (int i = 0; i < 6; ++i) dma16m(rx, d + (lw * 6 + i) * 768, vx[i], kt * 128, (1ull << 48) - 1);
#pragma unroll
      for (int i = 0; i < 2; ++i) wpiece(d, kt, i);
    };
    proj_consts(pc, bid, lw, lane);
    headp(0, 0);
    unsigned d = SLOT;
    const int fi = F_W + (j & 7) * 32 + (lane < 18 ? lane : 0);
    GAS u64* fp = (GAS u64*)(ws + WS_FLAG + fi * 128);
    u64 fw = 0;
#pragma unroll 1
    for (int k = 1; k < 9; ++k) {
      headp(d, k);
      WAITV(8);
      bar();
      STAMPL(16 + k);
      if (k == 8 && lw == 3) asm volatile("global_load_dwordx2 %0, %1, off sc1" : "+v"(fw) : "v"(fp) : "memory");
      tailp(d, k);
      d = d == 2 * SLOT ? 0 : d + SLOT;
    }
    STAMPL(26);
    tag = *(LAS u64*)(sm + L_TAG);
    asm volatile("s_waitcnt vmcnt(0)" : "+v"(fw) :: "memory");
    if (lw == 3 && !__all(fw == tag)) wait_flags(ws, fi, tag);
    bar();
    STAMPL(27);
    att_consts(sm, lane, tc, first);
#pragma unroll
    for (int i = 0; i < 21; ++i) wr[i] = ld16c(rp, lane * 16, (4 * i + lw) * 1024);
    __builtin_amdgcn_s_setprio(LPRIO);
    STAMPL(28);
  } else {
    tag = get_tag(ws);
    if (threadIdx.x == 0) { *(LAS u64*)(sm + L_TAG) = tag; *(LAS u4*)(sm + L_CNT) = u4{0, 0, 0, 0}; *(LAS u4*)(sm + L_CNT + 16) = u4{0, 0, 0, 0}; *(LAS u4*)(sm + L_CNT + 32) = u4{0, 0, 0, 0}; }
    {
      const int r = lane & 15, g = lane >> 4, q4 = r >> 2, p4 = lane & 3;
      LAS u4* tb = (LAS u4*)(sm + L_OT) + lane;
      u4 t0, t1;
      if (w < 2) {
#pragma unroll
        for (int dd = 0; dd < 4; ++dd) {
          t0[dd] = __builtin_amdgcn_sbfe((int)(0x1ffu << (2 * w)), 4 * g + dd, 1);
          t1[dd] = __builtin_amdgcn_sbfe((int)(0x3feu << (2 * w)), 4 * g + dd, 1);
        }
      } else if (w == 2) {
#pragma unroll
        for (int dd = 0; dd < 4; ++dd) {
          const int kk = 4 * g + dd - r;
          t0[dd] = (kk >= 1 && kk <= 8) ? 0u : 0xff800000u;
          t1[dd] = (kk >= 1 && kk <= 8 && g >= 2) ? 0u : 0xff800000u;
        }
      } else {
#pragma unroll
        for (int dd = 0; dd < 4; ++dd) t0[dd] = 4 * g + dd - r <= -8 ? 0u : 0xff800000u;
        t1[0] = (t0[0] ? 0u : 0xffffu) | (t0[1] ? 0u : 0xffff0000u);
        t1[1] = (t0[2] ? 0u : 0xffffu) | (t0[3] ? 0u : 0xffff0000u);
        t1[2] = g * PS + r * 16;
        t1[3] = g * PS + (r & 7) * 16;
        const int kr0 = (4 * g + q4) * 16, kr1 = kr0 + (g >= 2 ? 128 : 256);
        const int ve = (p4 >> 1) * PS + (p4 & 1) * HS, vo = ((p4 + 1) >> 1) * PS + ((p4 + 1) & 1) * HS;
        tb[8 * 64] = u4{(unsigned)(kr0 + ve), (unsigned)(kr1 + ve), (unsigned)(kr0 + vo), (unsigned)(kr1 + vo)};
        const unsigned os = (rt * 144 + hq * 9 + g) * 1024 + r * 16;
        tb[9 * 64] = u4{(unsigned)kr0, (unsigned)kr1, os, g ? OOB : os + 8192};
      }
      tb[2 * w * 64] = t0;
      tb[(2 * w + 1) * 64] = t1;
    }
    if (w & 1) g1_compute<1>(sm, 3 + (w >> 1) * 7, lane, tc, first);
    else g1_compute<0>(sm, (w >> 1) * 7, lane, tc, first);
    arrive(L_CNT);
#pragma unroll
    for (int i = 0; i < 21; ++i) asm volatile("" : "=v"(wr[i]));
    asm volatile("" : "=v"(pc.wa), "=v"(pc.wb), "=v"(pc.ob), "=v"(pc.oh), "=v"(pc.oo0), "=v"(pc.oo1), "=v"(pc.oo2),
                 "=v"(pc.ot), "=v"(pc.ow), "=v"(pc.fl));
  }
  {
    const unsigned rb = (32 * (aw >> 1) + (aw & 1)) * 8;
    const u4 qm[4] = {tc[0], tc[1], tc[2], tc[3]}, u0 = tc[4], u1 = tc[5], c0 = tc[6], c1 = tc[7], c2 = tc[8];
    const f4 b0 = __builtin_bit_cast(f4, u0), b1 = __builtin_bit_cast(f4, u1);
    const unsigned m01 = c0[0], m23 = c0[1];
    lptr qa = sm + rb + c0[2], k1 = sm + rb + c0[3];
    lptr ve0 = sm + rb + c1[0], ve1 = sm + rb + c1[1], vo0 = sm + rb + c1[2], vo1 = sm + rb + c1[3];
    lptr vk0 = sm + rb + c2[0], vk1 = sm + rb + c2[1];
    const unsigned ow = (aw >> 2) * 73728 + (aw & 3) * 256;
    unsigned os0 = c2[2] + ow, os1 = os0 + 4096, os2 = c2[3] + ow;
    const bool g0 = lane < 16;
    rsrc_t ro = mkrs(ws + WS_O, NB_ACT + 65536);
    unsigned oh = g0 ? NB_ACT + ((rt * 8 + aw) * 8 + hq) * 64 + lane * 4 : OOB;
    pin(oh);
    u4 ones = {0x3f803f80u, 0x3f803f80u, 0x3f803f80u, 0x3f803f80u};
    pin(qa); pin(k1); pin(ve0); pin(ve1); pin(vo0); pin(vo1); pin(vk0); pin(vk1); pin(os0); pin(os1); pin(os2); pin(ones);
    STAMP(14);
    reach(w < 4 ? L_CNT : L_CNT + 8, 4);
    STAMP(2);
    STAMPL(29);
    const float cs = 0.23570226039551584f * 1.4426950408889634f;
    const f4 z = {0.f, 0.f, 0.f, 0.f};
    u4 qf[4];
    bf8 ka[4], kb[4], vt0[4], vt1[4];
    f4 s0[4], s1[4], s[4];
    float mx[4];
    auto rdqk = [&](int h) {
      qf[h] = ld2(qa + 2 * h * PS + 128);
      ka[h] = asb(ld2(qa + (9 + 2 * h) * PS));
      kb[h] = asb(ld2(k1 + (9 + 2 * h) * PS + 256));
      SB();
    };
    auto rdv = [&](int h) {
      vt0[h] = cat8(trread((h < 2 ? ve0 : vo0) + (18 + 2 * h) * PS), trread((h < 2 ? ve1 : vo1) + (18 + 2 * h) * PS));
      vt1[h] = cat8(trread(vk0 + (20 + 2 * h) * PS + (h < 2 ? 0 : HS)), trread(vk1 + (20 + 2 * h) * PS + (h < 2 ? 0 : HS)));
    };
    rdqk(0); rdqk(1); rdqk(2);
#pragma unroll
    for (int h = 0; h < 4; ++h) {
      u4 qq = qf[h] & qm[h];
      pin(qq);
      if (h == 0) rdqk(3); else rdv(h - 1);
      SB();
      s0[h] = mfma(ka[h], asb(qq), b0);
      s1[h] = mfma(kb[h], asb(qq), b1);
      SB();
    }
    rdv(3);
    SB();
#pragma unroll
    for (int h = 0; h < 4; ++h) {
#pragma unroll
      for (int dd = 0; dd < 4; ++dd) s[h][dd] = fmax2(s0[h][dd], s1[h][dd]);
      mx[h] = fmax2(fmax2(fmax2(s[h][0], s[h][1]), s[h][2]), s[h][3]);
      SB();
    }
    {
      auto a = __builtin_amdgcn_permlane16_swap(asu(mx[0]), asu(mx[1]), false, false);
      auto b = __builtin_amdgcn_permlane16_swap(asu(mx[2]), asu(mx[3]), false, false);
      auto c = __builtin_amdgcn_permlane32_swap(asu(fmax2(asf(a[0]), asf(a[1]))), asu(fmax2(asf(b[0]), asf(b[1]))), false, false);
      unsigned m = asu(-cs * fmax2(asf(c[0]), asf(c[1])));
      auto e = __builtin_amdgcn_permlane32_swap(m, m, false, false);
      unsigned e0 = e[0], e1 = e[1];
      auto f0 = __builtin_amdgcn_permlane16_swap(e0, e0, false, false);
      auto f1 = __builtin_amdgcn_permlane16_swap(e1, e1, false, false);
      mx[0] = asf(f0[0]); mx[1] = asf(f0[1]); mx[2] = asf(f1[0]); mx[3] = asf(f1[1]);
    }
    SB();
    asm volatile("s_waitcnt vmcnt(0)" : "+v"(wr[0]), "+v"(wr[1]), "+v"(wr[2]), "+v"(wr[3]), "+v"(wr[4]), "+v"(wr[5]),
                 "+v"(wr[6]), "+v"(wr[7]), "+v"(wr[8]), "+v"(wr[9]), "+v"(wr[10]), "+v"(wr[11]), "+v"(wr[12]),
                 "+v"(wr[13]), "+v"(wr[14]), "+v"(wr[15]), "+v"(wr[16]), "+v"(wr[17]), "+v"(wr[18]), "+v"(wr[19]),
                 "+v"(wr[20]) :: "memory");
    f4 sum[4], o0[4], o1[4];
    bf8 pf[4];
#pragma unroll
    for (int h = 0; h < 5; ++h) {
      if (h < 4) {
#pragma unroll
        for (int dd = 0; dd < 4; ++dd) s[h][dd] = __builtin_fmaf(s[h][dd], cs, mx[h]);
        pin(s[h]);
      }
      if (h > 0) {
        sum[h - 1] = mfma(asb(ones), pf[h - 1], z);
        o0[h - 1] = mfma(vt0[h - 1], pf[h - 1], z);
        o1[h - 1] = mfma(vt1[h - 1], pf[h - 1], z);
      }
      if (h < 4) {
        const unsigned p01 = pk(__builtin_amdgcn_exp2f(s[h][0]), __builtin_amdgcn_exp2f(s[h][1]));
        const unsigned p23 = pk(__builtin_amdgcn_exp2f(s[h][2]), __builtin_amdgcn_exp2f(s[h][3]));
        pf[h] = asb(u4{p01 & ~m01, p23 & ~m23, p01 & m01, p23 & m23});
      }
      if (h > 0 && h < 4) { SGB(0x8, 1); SGB(0x400, 1); SGB(0x8, 1); SGB(0x400, 1); SGB(0x8, 1); }
      SB();
    }
    u2 gr[4];
    u4 left;
#pragma unroll
    for (int h = 0; h < 4; ++h) {
      const float inv = __builtin_amdgcn_rcpf(sum[h][0]);
      const unsigned a0 = pk(o0[h][0] * inv, o0[h][1] * inv), a1 = pk(o0[h][2] * inv, o0[h][3] * inv);
      const unsigned e0 = pk(o1[h][0] * inv, o1[h][1] * inv);
      if (h & 1) {
        const unsigned e1 = pk(o1[h][2] * inv, o1[h][3] * inv);
        left[h] = a1;
        gr[h] = u2{g0 ? e0 : a0, g0 ? e1 : a1};
        st16<SC1>(u4{gr[h - 1][0], gr[h - 1][1], gr[h][0], gr[h][1]}, ro, h == 1 ? os0 : os1);
        SB();
      } else {
        left[h] = e0;
        gr[h] = u2{a0, a1};
      }
    }
    st16<SC1>(left, ro, os2);
    const unsigned cs0 = chk(u4{gr[0][0], gr[0][1], gr[1][0], gr[1][1]}, chk(u4{gr[2][0], gr[2][1], gr[3][0], gr[3][1]}, 0));
    __builtin_amdgcn_raw_buffer_store_b32(chk(left, rowsum(cs0) + (unsigned)tag), ro, oh, 0, SC1);
  }
  if ((threadIdx.x & 255) == 192) post_flag(ws, (w < 4 ? F_A : F_L) + rt * 8 + hq, tag);
  STAMP(3);
  STAMPL(4);
  if (w >= 4) {
    __builtin_amdgcn_s_setprio(0);
    u64 fv = 0;
    GAS u64* fp = (GAS u64*)(ws + WS_FLAG + pc.fl * 128);
#pragma unroll
    for (int i = 0; i < 21; ++i) {
      if (i < 20 || w == 4) *(LAS u4*)(sm + L_WO + (4 * i + w - 4) * 1024 + lane * 16) = wr[i];
      if (i == 10 && w == 5) asm volatile("global_load_dwordx2 %0, %1, off sc1" : "+v"(fv) : "v"(fp) : "memory");
    }
    arrive(L_CNT + 20);
    if (w == 5) {
      asm volatile("s_waitcnt vmcnt(0)" : "+v"(fv) :: "memory");
      if (!__all(fv == tag)) wait_flags(ws, pc.fl, tag);
      arrive(L_CNT + 28);
    }
    STAMPL(30);
  }
}

DEV void role_b(lptr sm, char* ws, const u16* x, const u16* wqkv, const u16* wo, const u16* q, const u16* hk, const u16* dv, int bid, int w, int lane, unsigned fw, unsigned fx) {
  const int tid = threadIdx.x, xcd = bid & 7, jb = (bid >> 3) - 16, me = xcd * 16 + jb;
  const int G = lane >> 4, i16 = lane & 15, w4 = w & 3;
  rsrc_t ru = mkrs(ws + WS_U, NB_ACT + NB_ACT / 4);
  const int xc = (xcd + XSH) & 7, cm = xc * 16 + jb;
  const int c5 = 512 + xcd * 8 + (jb & 7);
  const unsigned tb = L_WO + w * 9216;
  const u4 ny = {NOTYET, NOTYET, NOTYET, NOTYET};
  rsrc_t ry = mkrs(ws + WS_Y, NB_ACT);
  u64 tag;
  if (w < 4 || (w == 4 && jb < 8)) {
    const int grp = w < 4 ? xcd : 8, un = w < 4 ? jb * 4 + w : xcd * 8 + jb;
    const int q4 = i16 >> 2, p4 = lane & 3;
    const int f0 = (q4 >> 1) | (p4 << 1), f1 = (q4 >> 1) | (((p4 + 1) & 3) << 1);
    const unsigned a0 = tb + (8 * p4 + q4) * 128 + (((G >> 1) ^ f0) << 4) + (G & 1) * 8;
    const unsigned a1 = tb + (8 * p4 + q4) * 128 + (((G >> 1) ^ f1) << 4) + (G & 1) * 8;
    lptr wp = sm + L_OT + w * 512 + (G * 4 + (i16 & 3)) * 8;
    unsigned off = ((grp * 16 + G) * 256 + 4 * un) * 64 + i16 * 16;
    u2 K[3], V[2], wk;
    auto rd = [&](int s) {
      lptr b0 = (lptr)(size_t)(a0 ^ (s << 5)), b1 = (lptr)(size_t)(a1 ^ (s << 5));
      wk = *(LAS u2*)(wp + s * 128);
      K[0] = __builtin_bit_cast(u2, trread(b0 + 512));
      K[1] = __builtin_bit_cast(u2, trread(b1 + 1024));
      K[2] = __builtin_bit_cast(u2, trread(b1 + 1536));
      V[0] = __builtin_bit_cast(u2, trread(b0 + 5120));
      V[1] = __builtin_bit_cast(u2, trread(b0 + 5120 + 512));
    };
    tag = lds_tag(sm);
    const unsigned key = (unsigned)tag;
    WAITV(0);
    if (w < 4) arrive(L_CNT + 44);
    STAMP(1);
    rd(0);
#pragma unroll 1
    for (int s = 0; s < 4; ++s) {
      const unsigned wA = alignb(wk[1], wk[1]), wB = alignb(wk[0], wk[0]);
      const unsigned kk[6] = {K[0][0], K[0][1], K[1][0], K[1][1], K[2][0], K[2][1]};
      const unsigned vv[4] = {V[0][0], V[0][1], V[1][0], V[1][1]};
      rd((s + 1) & 3);
      unsigned sh[5];
      float ev[4], od[4], ve[4], vo[4];
#pragma unroll
      for (int d = 0; d < 5; ++d) sh[d] = alignb(kk[d + 1], kk[d]);
      SB();
#pragma unroll
      for (int d = 0; d < 4; ++d) { ev[d] = dot2z(sh[d], wA); od[d] = dot2z(kk[d + 1], wA); }
      SB();
#pragma unroll
      for (int d = 0; d < 4; ++d) { ev[d] = dot2a(sh[d + 1], wB, ev[d]); od[d] = dot2a(kk[d + 2], wB, od[d]); }
      SB();
#pragma unroll
      for (int d = 0; d < 4; ++d) { ve[d] = lo16(vv[d]); vo[d] = hi16(vv[d]); pin(ve[d]); pin(vo[d]); }
      SB();
      u4 o;
#pragma unroll
      for (int d = 0; d < 4; ++d) o[3 - d] = pk(od[d] * vo[d], ev[d] * ve[d]);
      st16<SC1>(o, ru, off);
      __builtin_amdgcn_raw_buffer_store_b32(chk(o, key), ru, NB_ACT + (off >> 2), 0, SC1);
      off += 65536;
      SB();
    }
    STAMP(2);
    if (w < 4) h_dma(hk, w, cm * 4 + w, lane);
    if (w < 4) arrive(L_CNT + 40);
  } else if (w == 7) {
    const u4 rx = mkdesc(x, NB_ACT), rw = mkdesc(wqkv, NB_W);
    __builtin_amdgcn_s_setprio(3);
    STAMPW(22, 7);
    if (PDELAY) __builtin_amdgcn_s_sleep(PDELAY);
#pragma unroll 1
    for (int st = PFS0; st < 9; ++st) {
      if (st == PF1) reach(L_CNT + 44, 4);
      if (st == PFA) reach(L_CNT + 40, 4);
      if (st == PFB) reach(L_CNT + 12, PFBN);
      touch(rw, fw, st * 64 * 3456);
      touch(rx, fx, st * 128);
    }
    STAMPW(23, 7);
    reach(L_CNT + 4, 1);
    const u4 rq = mkdesc(q, NB_ACT);
    touch(rq, (jb * 128 + lane) * 1152 + ((xcd + YSH) & 7) * 128, 0);
    touch(rq, (jb * 128 + lane) * 1152 + ((xcd + YSH) & 7) * 128, 64 * 1152);
    if (jb >= 8) {
      const int un = bid - 192;
      touch(rq, lane < 32 ? ((un >> 2) * 128 + 64 * (un & 1) + 8 * ((un >> 1) & 1) + (lane >> 3) * 16 + (lane & 7)) * 1152 + 1024 : OOB, 0);
    }
    const unsigned y4 = (xc * 32 + (lane >> 5)) * 8192 + jb * 512 + (lane & 31) * 16;
#pragma unroll 1
    for (int i = 0; i < 16; i += 2) { st16<SC1>(ny, ry, y4 + i * 16384); st16<SC1>(ny, ry, y4 + i * 16384 + 16384); }
    if (jb >= 8) {
#pragma unroll
      for (int i = 0; i < 4; ++i) st16<SC1>(ny, ry, (256 + i * 8 + (lane >> 3)) * 8192 + (c5 & 63) * 128 + (lane & 7) * 16);
    }
    tag = lds_tag(sm);
    WAITV(0);
    arrive(L_CNT + 16);
    if (lane == 0) post_flag(ws, F_Y + cm, tag);
    STAMPW(25, 7);
    __builtin_amdgcn_s_sleep(12);
    if (jb >= 8) __builtin_amdgcn_s_sleep(16);
    wait_flags(ws, F_Y + (jb < 8 ? xcd * 16 + i16 : (lane >> 3) * 16 + 8 + (lane & 7)), tag);
    arrive(L_CNT + 24);
  } else if (w == 6) {
    if (jb >= 8) {
      reach(L_CNT + 40, 4);
      h_dma(hk, 4, c5, lane);
      WAITV(0);
      h_shift(sm + 4 * CH_SZ, lane);
      arrive(L_CNT + 32);
    }
  } else {
    const int slot = w == 5 ? jb : 2 + jb;
    if (w == 5) {
      tag = get_tag(ws);
      if (lane == 0) *(LAS u64*)(sm + L_TAG) = tag;
      arrive(L_CNT);
      if (lane == 0) post_flag(ws, F_P + me, tag);
    } else {
      tag = lds_tag(sm);
    }
    if (w == 5 || jb >= 14) {
      const u4 rwo = mkdesc(wo, NB_WO);
      const int e4 = (lane * 57) >> 9;
      const unsigned vb = (e4 >> 1) * 1152 + xcd * 144 + (lane - e4 * 9) * 16, vt = (e4 & 1) * 1152;
      int ks = slot + ROTK * xcd;
      if (ks >= 18) ks -= 18;
#pragma unroll 1
      for (int i = 0; i < 8; ++i) {
        const int ci = 4 * ks + (i >> 1), h = (ci * 57) >> 9, c = ci - 9 * h;
        const int row = h * 72 + (c == 8 ? 16 : 36 * (c >> 2) + 4 * (c & 3)) + 2 * (i & 1);
        dma16m(rwo, tb + i * 576, vb + __umul24(vt, c == 8 ? 36 : (c & 3) ? 16 : 32), row * 1152, (1ull << 36) - 1);
      }
      WAITV(0);
      rsrc_t rpw = mkrs(ws + WS_WO, NB_WO);
      lptr p = (lptr)(size_t)tb + (G * 8 + ((i16 >> 2) << 1)) * 144 + (lane & 3) * 8;
      const unsigned po = (xcd * 18 + slot) * 4608;
      u4 f[5];
#pragma unroll
      for (int n = 0; n < 5; ++n) f[n] = __builtin_bit_cast(u4, cat8(trread(p + n * 32), trread(p + 144 + n * 32)));
#pragma unroll
      for (int n = 0; n < 4; ++n) st16<SC1>(f[n], rpw, po + n * 1024 + lane * 16);
      st16<SC1>(f[4], rpw, i16 < 8 ? po + 4096 + (G * 8 + i16) * 16 : OOB);
      WAITV(0);
      if (lane == 0) post_flag(ws, F_W + xcd * 32 + slot, tag);
      STAMPW(21, 5);
    }
  }
  const int r = lane & 15, g = lane >> 4;
  if (w >= 4) {
    if (w < 6) {
      const int ct = (bid >> 3) & 7;
      __builtin_amdgcn_s_setprio(2);
      if (w == 5) {
        reach(L_CNT + 4, 1);
        wait_flags(ws, F_W + ct * 32 + (lane < 18 ? lane : 0), tag);
      } else if (jb >= 8) {
        reach(L_CNT + 4, 1);
        u4 u5[4];
#pragma unroll 1
        for (int spin = 0; spin < SPIN_MAX; ++spin) {
          unsigned hx[4], bad = 0;
#pragma unroll
          for (int i = 0; i < 4; ++i) {
            u5[i] = ld16<SC1>(ru, lane * 64 + (c5 & 3) * 16, (c5 >> 2) * 16384 + i * 4096);
            hx[i] = __builtin_amdgcn_raw_buffer_load_b32(ru, lane * 16 + (c5 & 3) * 4, NB_ACT + (c5 >> 2) * 4096 + i * 1024, SC1);
          }
#pragma unroll
          for (int i = 0; i < 4; ++i) bad |= chk(u5[i], (unsigned)tag) ^ hx[i];
          if (__all(bad == 0)) break;
          asm volatile("s_sleep 4" ::: "memory");
        }
#pragma unroll
        for (int i = 0; i < 4; ++i) *(LAS u4*)(sm + U_OFF + 480 + 4 * CH_SZ + lane * 16 + i * 1024) = u5[i];
      }
      if (w == 4) arrive(L_CNT + 32);
      arrive(L_CNT + 44);
      reach(L_CNT + 44, 6);
      reach(L_CNT + 8, 4);
      STAMPL(16);
      const u4 rp = mkdesc(ws + WS_WO + ct * 82944, 82944);
#pragma unroll 1
      for (int i = (w & 1) * 1024; i < 81 * 1024; i += 2048) dma16c(rp, L_WO + i, lane * 16, i);
      WAITV(0);
      STAMPL(17);
    }
    arrive(L_CNT + 20);
    __builtin_amdgcn_s_setprio(0);
    return;
  }
  typedef const __attribute__((address_space(4))) unsigned* cptr;
  const unsigned dv0 = ((cptr)dv)[cm * 2 + (w >> 1)], dv1 = ((cptr)dv)[c5 >> 1];
  WAITV(0);
  __builtin_amdgcn_s_sleep(UDELAY);
  STAMP(3);
  {
    u4 uv[4];
#pragma unroll 1
    for (int spin = 0; spin < SPIN_MAX; ++spin) {
      unsigned hx[4], bad = 0;
#pragma unroll
      for (int i = 0; i < 4; ++i) {
        uv[i] = ld16<SC1>(ru, tid * 16, cm * 16384 + i * 4096);
        hx[i] = __builtin_amdgcn_raw_buffer_load_b32(ru, tid * 4, NB_ACT + cm * 4096 + i * 1024, SC1);
      }
      if (spin == 0) { arrive(L_CNT + 12); h_shift(sm + w * CH_SZ, lane); }
#pragma unroll
      for (int i = 0; i < 4; ++i) bad |= chk(uv[i], (unsigned)tag) ^ hx[i];
      if (__all(bad == 0)) break;
      STAMP(20);
      asm volatile("s_sleep 1" ::: "memory");
    }
    STAMP(4);
    if (w == 3) arrive(L_CNT + 4);
#pragma unroll
    for (int i = 0; i < 4; ++i) *(LAS u4*)(sm + U_OFF + 480 + (tid & 3) * CH_SZ + (tid >> 2) * 16 + i * 1024) = uv[i];
  }
  arrive(L_CNT + 8);
  const int xd = 15 - r - 8 * g;
#pragma unroll 1
  for (int rnd = 0; rnd < (jb >= 8 ? 2 : 1); ++rnd) {
    const int dd0 = rnd ? 2 * w : 0, ch = rnd ? c5 : cm * 4 + w;
    const unsigned dval = dd0 == 0 ? ((rnd ? dv1 : dv0) >> ((ch & 1) * 16)) & 0xffffu : 0;
    lptr cb = sm + (rnd ? 4 : w) * CH_SZ;
    lptr aa[4];
#pragma unroll
    for (int i = 0; i < 4; ++i) { aa[i] = cb + (r & 1) * H_CP + 4 + 4 * (4 * g + (r >> 1)) + dd0 * 64 + i * 1024; pin(aa[i]); }
    lptr ba = cb + U_OFF + 16 * (31 + 2 * r - g) - 128 - dd0 * 64;
    pin(ba);
    u4 ad;
#pragma unroll
    for (int d = 0; d < 4; ++d) ad[d] = (xd >> 1) == d ? dval << ((xd & 1) * 16) : 0u;
    pin(ad);
    reach(rnd ? L_CNT + 32 : L_CNT + 8, rnd ? 2 : 4);
    STAMP(rnd ? 19 : 5);
    f4 acc[8];
    bf8 A[2][8], B[2][8];
    auto rd = [&](int dd, bf8 (&a)[8], bf8 (&b)[8]) {
#pragma unroll
      for (int i = 0; i < 8; ++i) {
        const LAS unsigned* p = (const LAS unsigned*)(aa[i >> 1] + dd * 64 + (i & 1) * 512);
        a[i] = asb(u4{p[0], p[1], p[2], p[3]});
        b[i] = *(const LAS bf8*)(ba + 128 - dd * 64 + i * 512);
      }
    };
    auto mm = [&](bf8 (&a)[8], bf8 (&b)[8]) {
#pragma unroll
      for (int i = 0; i < 8; ++i)
#pragma unroll
        for (int jj = 0; jj + i < 8; ++jj) acc[i + jj] = mfma(a[i], b[jj], acc[i + jj]);
#pragma unroll
      for (int i = 0; i < 24; ++i) { SGB(0x8, 1); SGB(0x100, 1); }
      SGB(0x8, 12);
      SB();
    };
    rd(0, A[0], B[0]);
#pragma unroll
    for (int n = 0; n < 8; ++n) acc[n] = mfma(asb(ad), B[0][n], f4{0.f, 0.f, 0.f, 0.f});
    SB();
#pragma unroll 1
    for (int it = rnd * 3; it < 4; ++it) {
      rd(1, A[1], B[1]);
      mm(A[0], B[0]);
      rd(2, A[0], B[0]);
      mm(A[1], B[1]);
#pragma unroll
      for (int i = 0; i < 4; ++i) aa[i] += 128;
      ba -= 128;
    }
    STAMP(6 + rnd);
    const unsigned yo = (((ch >> 6) * 16 + 2 * (g & 1) + (r >> 3)) * 2 + (g >> 1)) * 8192 + (ch & 63) * 128 + (r & 7) * 16;
    auto put = [&](f4 x, f4 y, int a) {
      auto s0 = __builtin_amdgcn_permlane16_swap(pk(x[0], x[1]), pk(y[0], y[1]), false, false);
      auto s1 = __builtin_amdgcn_permlane16_swap(pk(x[2], x[3]), pk(y[2], y[3]), false, false);
      const u4 lim = {NOTYET - 1, NOTYET - 1, NOTYET - 1, NOTYET - 1};
      st16<SC1>(__builtin_elementwise_min(u4{s0[0], s1[0], s0[1], s1[1]}, lim), ry, yo + a * 65536);
    };
    if (rnd == 0) {
      reach(L_CNT + 16, 1);
#pragma unroll
      for (int a = 0; a < 4; ++a) put(acc[2 * a], acc[2 * a + 1], a);
    } else {
      LAS f4* mine = (LAS f4*)(sm + w * CH_SZ + lane * 16);
#pragma unroll
      for (int n = 0; n < 8; ++n) mine[n * 64] = acc[n];
      arrive(L_CNT + 16);
      reach(L_CNT + 16, 5);
      constexpr int CS = CH_SZ / 16;
      LAS f4* p = (LAS f4*)(sm + lane * 16) + w * 128;
      put((p[0] + p[CS]) + (p[2 * CS] + p[3 * CS]), (p[64] + p[CS + 64]) + (p[2 * CS + 64] + p[3 * CS + 64]), w);
    }
  }
  STAMP(8);
}

DEV void finish(lptr sm, lptr yt, char* ws, const u16* q, u16* out, int grp, int idx, int w4, int lane) {
  rsrc_t ry = mkrs(ws + WS_Y, NB_ACT), rq = mkrs(q, NB_ACT), rout = mkrs(out, 2 * NB_ACT);
  const int tile = idx + 8 * (w4 >> 1), orow = tile * 128 + 8 * (w4 & 1) + (lane >> 3);
  const unsigned qo = orow * 1152 + grp * 128 + (lane & 7) * 16, yo = ((grp * 16 + tile) * 2 + (w4 & 1)) * 8192 + lane * 16;
  u4 qv[8], yv[8];
#pragma unroll
  for (int i = 0; i < 8; ++i) qv[i] = ld16<0>(rq, qo, i * 16 * 1152);
  reach(L_CNT + 24, 1);
  f2 qf[8][4], yy[4][4];
#pragma unroll
  for (int i = 0; i < 16; ++i) { yy[i >> 2][i & 3] = f2{0.f, 0.f}; pin(yy[i >> 2][i & 3]); }
  asm volatile("s_waitcnt vmcnt(0)" : "+v"(qv[0]), "+v"(qv[1]), "+v"(qv[2]), "+v"(qv[3]), "+v"(qv[4]), "+v"(qv[5]),
               "+v"(qv[6]), "+v"(qv[7]) :: "memory");
  STAMP(26);
#pragma unroll
  for (int i = 0; i < 32; ++i) {
    qf[i >> 2][i & 3] = f2{lo16(qv[i >> 2][i & 3]), hi16(qv[i >> 2][i & 3])};
    pin(qf[i >> 2][i & 3]);
  }
#pragma unroll 1
  for (int spin = 0; spin < SPIN_MAX; ++spin) {
    unsigned mx = 0;
#pragma unroll
    for (int i = 0; i < 8; ++i) yv[i] = ld16<SC1>(ry, yo, i * 1024);
#pragma unroll
    for (int i = 0; i < 8; ++i) mx = max3u(max3u(mx, yv[i][0], yv[i][1]), yv[i][2], yv[i][3]);
    if (__all(mx != NOTYET)) break;
    STAMP(27 + (spin < 3 ? spin : 3));
    asm volatile("s_sleep 2" ::: "memory");
  }
  STAMP(12);
  unsigned oo = orow * 2304 + 1152 + grp * 128 + (lane & 7) * 16;
#pragma unroll
  for (int i = 0; i < 8; ++i) *(LAS u4*)(yt + lane * 16 + i * 1040) = yv[i];
  const unsigned p = (unsigned)(size_t)yt + (lane & 7) * 1040 + (lane >> 3) * 2;
#pragma unroll
  for (int t = 0; t < 2; ++t) {
#pragma unroll
    for (int i = 0; i < 16; ++i) {
      float a = yy[i >> 2][i & 3][0], b = yy[i >> 2][i & 3][1];
      asm volatile("ds_read_u16_d16_hi %0, %2 offset:%3\n\tds_read_u16_d16_hi %1, %2 offset:%4"
                   : "+v"(a), "+v"(b) : "v"(p), "i"(t * 64 + (i >> 2) * 16 + (i & 3) * 256), "i"(t * 64 + (i >> 2) * 16 + (i & 3) * 256 + 128)
                   : "memory");
      yy[i >> 2][i & 3] = f2{a, b};
    }
#pragma unroll
    for (int i = 0; i < 4; ++i) {
      if (i == 0) asm volatile("s_waitcnt lgkmcnt(14)" : "+v"(yy[0][0]), "+v"(yy[0][1]), "+v"(yy[0][2]), "+v"(yy[0][3]), "+v"(yy[1][0]), "+v"(yy[1][1]), "+v"(yy[1][2]), "+v"(yy[1][3]));
      if (i == 2) asm volatile("s_waitcnt lgkmcnt(0)" : "+v"(yy[2][0]), "+v"(yy[2][1]), "+v"(yy[2][2]), "+v"(yy[2][3]), "+v"(yy[3][0]), "+v"(yy[3][1]), "+v"(yy[3][2]), "+v"(yy[3][3]));
      u4 o;
#pragma unroll
      for (int d = 0; d < 4; ++d) { const f2 m = yy[i][d] * qf[t * 4 + i][d]; o[d] = pk(m[0], m[1]); }
      st16<OA1>(o, rout, oo + (t * 64 + i * 16) * 2304);
      SB();
    }
  }
}

DEV void finish8(lptr yt, char* ws, const u16* q, u16* out, int un, int w, int lane) {
  rsrc_t ry = mkrs(ws + WS_Y, NB_ACT), rq = mkrs(q, NB_ACT), rout = mkrs(out, 2 * NB_ACT);
  const int orow = (un >> 2) * 128 + 64 * (un & 1) + 8 * ((un >> 1) & 1) + 16 * w + (lane >> 3);
  u4 qv = ld16<0>(rq, orow * 1152 + 1024 + (lane & 7) * 16, 0), yv;
  reach(L_CNT + 24, 1);
  asm volatile("s_waitcnt vmcnt(0)" : "+v"(qv) :: "memory");
#pragma unroll 1
  for (int spin = 0; spin < SPIN_MAX; ++spin) {
    yv = ld16<SC1>(ry, (w * 16 + (lane >> 2)) * 128 + (un & 1) * 64 + (lane & 3) * 16, (256 + (un >> 1)) * 8192);
    if (__all(max4(yv, 0) != NOTYET)) break;
    asm volatile("s_sleep 1" ::: "memory");
  }
  STAMP(12);
  *(LAS u4*)(yt + (w * 2 + (lane >> 5)) * 528 + (lane & 31) * 16) = yv;
  arrive(L_CNT + 36);
  reach(L_CNT + 36, 4);
  const LAS u16* p = (const LAS u16*)(yt + (lane & 7) * 528 + w * 16 + (lane >> 3) * 2);
  u4 o;
#pragma unroll
  for (int d = 0; d < 4; ++d) o[d] = pk(lo16(p[d * 64]) * lo16(qv[d]), lo16(p[d * 64 + 32]) * hi16(qv[d]));
  st16<OA1>(o, rout, orow * 2304 + 1152 + 1024 + (lane & 7) * 16);
}

DEV void out_proj(lptr sm, char* ws, u16* out, int bid, int lw, int lane, const Proj& pc) {
  lptr wa[2] = {(lptr)(size_t)pc.wa, (lptr)(size_t)(pc.wa + 9 * 4608)};
  lptr wb[2] = {(lptr)(size_t)pc.wb, (lptr)(size_t)(pc.wb + 9 * 4608)};
  pin(wa[1]); pin(wb[1]);
  rsrc_t ro = mkrs(ws + WS_O, NB_ACT + 65536);
  const unsigned ob = pc.ob, oo[3] = {pc.oo0, pc.oo1, pc.oo2};
  lptr ot = (lptr)(size_t)pc.ot, ow = (lptr)(size_t)pc.ow;
  reach(L_CNT + 20, 4);
  f4 acc[5];
  const u64 tag = *(LAS u64*)(sm + L_TAG);
#pragma unroll 1
  for (int att = 0; att < (1 << 17); ++att) {
    constexpr int PRE = GPRE, AHEAD = GAH;
    bf8 wf[PRE][5];
    auto rdw = [&](int i) {
#pragma unroll
      for (int n = 0; n < 5; ++n)
        wf[i % PRE][n] = *(const LAS bf8*)((n < 4 ? wa[i / 9] + n * 1024 : wb[i / 9]) + (i % 9) * 4608);
    };
#pragma unroll
    for (int i = 0; i < PRE - 1; ++i) rdw(i);
    SB();
    if (lw == 0 && att == 0 && bid >= 128) {
      wait_flags(ws, pc.fl, tag);
      arrive(L_CNT + 28);
    }
    reach(L_CNT + 28, 1);
    STAMPL(att ? 31 : 9);
    u4 of[18];
    const int ct = (bid >> 3) & 7;
    auto ldo = [&](int i) { of[i] = ld16<SC1>(ro, ob, (i + ROTK * ct < 18 ? i + ROTK * ct : i + ROTK * ct - 18) * 4096); };
#pragma unroll
    for (int i = 0; i < AHEAD; ++i) { ldo(i); SB(); }
#pragma unroll
    for (int n = 0; n < 5; ++n) acc[n] = f4{0.f, 0.f, 0.f, 0.f};
    unsigned sum = 2 * (unsigned)tag, h0, h1;
#pragma unroll
    for (int i = 0; i < 18; ++i) {
      if (i + PRE - 1 < 18) rdw(i + PRE - 1);
#pragma unroll
      for (int n = 0; n < 5; ++n) acc[n] = mfma(wf[i % PRE][n], asb(of[i]), acc[n]);
      sum = chk(of[i], sum);
      if (i + AHEAD < 18) ldo(i + AHEAD);
      if (i == 12) h0 = __builtin_amdgcn_raw_buffer_load_b32(ro, pc.oh, 0, SC1);
      if (i == 13) h1 = __builtin_amdgcn_raw_buffer_load_b32(ro, pc.oh + 256, 0, SC1);
      SGB(0x8, 1); SGB(0x20, 1); SGB(0x100, 1);
#pragma unroll
      for (int n = 1; n < 5; ++n) { SGB(0x8, 1); SGB(0x100, 1); SGB(0x2, 1); }
      SB();
    }
    if (__all(rowsum(sum - h0 - h1) == 0)) break;
    asm volatile("" ::: "memory");
  }
  STAMPL(10);
#pragma unroll
  for (int n = 0; n < 5; ++n)
    if (n < 4 || lane < 32) *(LAS u2*)(ow + n * 32) = u2{pk(acc[n][0], acc[n][1]), pk(acc[n][2], acc[n][3])};
  WAITL0();
  rsrc_t rout = mkrs(out, 2 * NB_ACT);
#pragma unroll
  for (int i = 0; i < 3; ++i) st16<OA2>(*(const LAS u4*)(ot + i * 1024), rout, oo[i]);
  STAMPL(15);
}

__global__ __launch_bounds__(512) __attribute__((amdgpu_waves_per_eu(2, 2))) void fused(
    const u16*, const u16*, const u16*, const u16*, const u16*, const u16*, const u16*, const u16*, const u16*, u16*,
    char*) {
  typedef u64 u64x8 __attribute__((ext_vector_type(8)));
  typedef u64 u64x2 __attribute__((ext_vector_type(2)));
  u64x8 ka;
  u64x2 kb;
  u64 kc;
  asm volatile("s_load_dwordx16 %0, %3, 0x0\n\ts_load_dwordx4 %1, %3, 0x40\n\ts_load_dwordx2 %2, %3, 0x50"
               : "=&s"(ka), "=&s"(kb), "=&s"(kc) : "s"(__builtin_amdgcn_kernarg_segment_ptr()));
  extern __shared__ __attribute__((aligned(16))) char smem[];
  lptr sm = (lptr)smem;
  const int tid = threadIdx.x, lane = tid & 63, w = __builtin_amdgcn_readfirstlane(tid >> 6);
  const int bid = blockIdx.x;
  if (bid < 128 && (bid >> 3) + 16 * tid < CODE_LINES) {
    u64 pc;
    asm volatile("s_getpc_b64 %0" : "=s"(pc));
    asm volatile("s_mov_b32 m0, %0\n\ts_nop 0\n\tglobal_load_lds_dword %1, %2"
                 :: "s"(L_PF), "v"(((bid >> 3) + 16 * tid) * 128), "s"(pc & ~127ull) : "memory");
  }
  const int pgrp = w < 4 ? bid & 7 : 8, pun = w < 4 ? ((bid >> 3) - 16) * 4 + w : bid - 128 - ((bid >> 3) - 16) * 7 + (bid & 7) * 7;
  unsigned o0 = (pun * 32 + (lane >> 3)) * 1152 + pgrp * 128 + (((lane & 7) ^ ((lane >> 4) & 1)) << 4);
  const int t27 = lane >= 27, cc = lane - 27 * t27, part = (cc >= 9) + (cc >= 18);
  unsigned vw = t27 * (4 * 3456) + part * (1152 - 144) + cc * 16 + ((bid & 1) * 4 + ((bid >> 3) & 3)) * 144;
  if (w == 7 && bid >= 128) {
    const int jb = (bid >> 3) - 16, kr = (lane * 4370) >> 16, c15 = lane - kr * 15, p5 = (c15 * 52) >> 8;
    vw = lane < 60 ? (jb * 4 + kr) * 3456 + p5 * (1152 - 640) + ((bid & 1) * 4 + c15) * 128 : OOB;
    o0 = lane < 33 ? (((bid & 7) >> 1) * 512 - 16 + jb * 33 + lane) * 1152 : OOB;
  }
  pin(o0); pin(vw);
  if (bid >= 128 && tid < 12) *(LAS unsigned*)(sm + L_CNT + tid * 4) = 0;
  asm volatile("s_waitcnt lgkmcnt(0)" : "+s"(ka), "+s"(kb), "+s"(kc) :: "memory");
  const u16 *x = (const u16*)ka[0], *wqkv = (const u16*)ka[1], *wo = (const u16*)ka[2], *q = (const u16*)ka[3];
  const u16 *hk = (const u16*)ka[7], *dv = (const u16*)kb[0];
  u16* out = (u16*)kb[1];
  char* ws = (char*)kc;
  if (__builtin_expect(bid >= 128, 1)) {
    if (__builtin_expect(w < 4 || (w == 4 && bid < 192), 1)) {
      const u4 rk = mkdesc((const void*)ka[4], NB_ACT), rv = mkdesc((const void*)ka[5], NB_ACT);
      const unsigned tb = L_WO + w * 9216;
      dma16(rk, tb, o0 - 8 * 1152, 0);
#pragma unroll
      for (int i = 0; i < 4; ++i) {
        dma16(rk, tb + 1024 + i * 1024, o0 ^ (((i + 1) & 3) << 5), i * 8 * 1152);
        dma16(rv, tb + 5120 + i * 1024, o0 ^ (i << 5), i * 8 * 1152);
        if (i == 0) dma16m(mkdesc((const void*)ka[6], 4608), L_OT + w * 512, lane * 16, pgrp * 512, 0xffffffffull);
      }
    }
    if (w == 7) {
#pragma unroll
      for (int st = PFE; st < PFS0; ++st) {
        touch(mkdesc(wqkv, NB_W), vw, st * 64 * 3456);
        touch(mkdesc(x, NB_ACT), o0, st * 128);
      }
    }
    __builtin_amdgcn_s_barrier();
    asm volatile("" ::: "memory");
  }
  Proj pc;
  if (__builtin_expect(bid < 128, 1)) role_a(sm, ws, x, wqkv, bid, w, lane, pc, vw);
  else {
    role_b(sm, ws, x, wqkv, wo, q, hk, dv, bid, w, lane, vw, o0);
    if (w >= 4) proj_consts(pc, bid, w & 3, lane);
    else asm volatile("" : "=v"(pc.wa), "=v"(pc.wb), "=v"(pc.ob), "=v"(pc.oh), "=v"(pc.oo0), "=v"(pc.oo1), "=v"(pc.oo2),
                      "=v"(pc.ot), "=v"(pc.ow), "=v"(pc.fl));
  }
  if (w >= 4) {
    out_proj(sm, ws, out, bid, w & 3, lane, pc);
  } else if (bid >= 128) {
    if (bid >= 192) finish8(sm + 4 * CH_SZ, ws, q, out, bid - 192, w, lane);
    else finish(sm, sm + w * CH_SZ, ws, q, out, bid & 7, (bid >> 3) - 16, w, lane);
    STAMP(13);
  } else {
    if (bid == 64 && w == 1) {
      const u64 tag = *(LAS u64*)(sm + L_TAG);
      GAS u64* p = (GAS u64*)(ws + WS_FLAG + lane * 128);
#pragma unroll 1
      for (int spin = 0; spin < SPIN_MAX; ++spin) {
        const u64 a = __hip_atomic_load(p, RLX_AGENT), b = __hip_atomic_load(p + 1024, RLX_AGENT);
        const u64 c = __hip_atomic_load(p + 2048, RLX_AGENT), d = __hip_atomic_load(p + 3072, RLX_AGENT);
        if (__all(a == tag && b == tag && c == tag && d == tag)) break;
        __builtin_amdgcn_s_sleep(PSLEEP);
      }
      if (lane == 0) __hip_atomic_store((GAS u64*)ws, tag, RLX_AGENT);
    }
  }
}

extern "C" void kernel_launch(void* const* d_in, const int* in_sizes, int n_in, void* d_out, int out_size, void* d_ws,
                              size_t ws_size, hipStream_t stream) {
  (void)hipFuncSetAttribute((const void*)fused, hipFuncAttributeMaxDynamicSharedMemorySize, LDS_BYTES);
  fused<<<256, 512, LDS_BYTES, stream>>>((const u16*)d_in[0], (const u16*)d_in[1], (const u16*)d_in[2], (const u16*)d_in[3],
                                         (const u16*)d_in[4], (const u16*)d_in[5], (const u16*)d_in[6], (const u16*)d_in[7],
                                         (const u16*)d_in[8], (u16*)d_out, (char*)d_ws);
}
```
